```python
import torch

def _softmax(x, axis=-1):
    return torch.softmax(x, dim=axis)
def _gelu(x):
    return torch.nn.functional.gelu(x, approximate="tanh")
def _silu(x):
    return torch.nn.functional.silu(x)
def _sigmoid(x):
    return torch.sigmoid(x)
def _layernorm(x, axis=-1, eps=1e-5):
    m = x.mean(dim=axis, keepdim=True)
    v = x.var(dim=axis, keepdim=True, unbiased=False)
    return (x - m) / torch.sqrt(v + eps)
def _scatter_add(tgt, idx, src, axis=0):
    return tgt.clone().index_add_(axis, idx, src)
def _segment_sum(src, seg, n):
    out = torch.zeros((n,) + src.shape[1:], dtype=src.dtype)
    return out.index_add_(0, seg, src)
def _pad(x, pw):
    flat = tuple(v for pair in reversed(pw) for v in pair)
    return torch.nn.functional.pad(x, flat)

kernel_name = 'standalone__mha_softmax__near_ridge'

def setup_inputs():

    _w = (lambda a: (lambda t: t.to(torch.bfloat16) if t.is_floating_point() else t)(torch.as_tensor(a)))
    B, H, Sq, Sk, D = 8, 32, 304, 2048, 128
    return {
        "q": _w((lambda *s: torch.randn(*s, dtype=torch.bfloat16) * 0.8)(B, H, Sq, D)),
        "k": _w((lambda *s: torch.randn(*s, dtype=torch.bfloat16) * 0.8)(B, H, Sk, D)),
        "v": _w((lambda *s: torch.randn(*s, dtype=torch.bfloat16) * 0.05)(B, H, Sk, D)),
    }

def reference(q, k, v):
    scale = q.shape[-1] ** -0.5
    scores = torch.einsum("bhqd,bhkd->bhqk", q, k) * scale
    p = _softmax(scores, axis=-1)
    return torch.einsum("bhqk,bhkd->bhqd", p, v)
```

```cpp
#include <hip/hip_runtime.h>
#include <hip/hip_bf16.h>
#include <cstdio>
using bf16 = __hip_bfloat16;
constexpr int   D = 128, NW = 8, QBLK = 32, KVBLK = 64;
constexpr float SCALE = 0.088388347648318440f;
constexpr float THR = 8.f;
constexpr int B = 8, HQ = 32, SQ_ = 304, SKV = 2048;
constexpr int LDQ = D, LDK = D, LDO = D;
constexpr size_t SHM_V = KVBLK * D * 2, SHM_K = KVBLK * D * 2, SHM_ATTN = 2 * SHM_V + 2 * SHM_K + NW * 64 * 4;
using bf16x8 = __attribute__((ext_vector_type(8))) short;
using s16x4  = __attribute__((ext_vector_type(4))) short;
using f32x16 = __attribute__((ext_vector_type(16))) float;
using u32x4  = __attribute__((ext_vector_type(4))) unsigned;
#define KSWZ(row, colB) ((row) * 256 + ((colB) ^ (((row) & 7) << 4)))
#define SBAR() __builtin_amdgcn_sched_barrier(0)
__device__ __forceinline__ int crow(int r, int hi) { return (r & 3) + 8 * (r >> 2) + 4 * hi; }
__device__ __forceinline__ unsigned cvtpk(float lo, float hi) {
  unsigned r; asm volatile("v_cvt_pk_bf16_f32 %0, %1, %2" : "=v"(r) : "v"(lo), "v"(hi)); return r;
}
__device__ __forceinline__ bf16x8 ld8(const bf16* p) { return *reinterpret_cast<const bf16x8*>(p); }
__device__ __forceinline__ void partialSM(f32x16& p0, f32x16& p1, float& m_reg, float& mn, float& alpha) {
  constexpr float C = SCALE * 1.4426950408889634f;
  float pmax = p0[0]; for (int r = 1; r < 16; ++r) pmax = fmaxf(pmax, p0[r]); for (int r = 0; r < 16; ++r) pmax = fmaxf(pmax, p1[r]);
  { auto rr = __builtin_amdgcn_permlane32_swap(__float_as_uint(pmax), __float_as_uint(pmax), false, false);
    pmax = fmaxf(__uint_as_float(rr[0]), __uint_as_float(rr[1])); }
  if (__builtin_expect(__all(pmax - m_reg <= THR / SCALE), 1)) { mn = m_reg; alpha = 1.f; }
  else { mn = fmaxf(m_reg, pmax); alpha = __builtin_amdgcn_exp2f((m_reg - mn) * C); m_reg = mn; }
  float mnC = -mn * C;
  for (int r = 0; r < 16; ++r) p0[r] = fmaf(p0[r], C, mnC); for (int r = 0; r < 16; ++r) p1[r] = fmaf(p1[r], C, mnC);
  for (int r = 0; r < 16; ++r) p0[r] = __builtin_amdgcn_exp2f(p0[r]);
}
__device__ __forceinline__ void finishSM(f32x16& p0, f32x16& p1, float alpha, float& l_reg, bf16x8& pa0, bf16x8& pa1, bf16x8& pa2, bf16x8& pa3) {
  for (int r = 0; r < 16; ++r) p1[r] = __builtin_amdgcn_exp2f(p1[r]);
  float ps = 0; for (int r = 0; r < 16; ++r) ps += p0[r]; for (int r = 0; r < 16; ++r) ps += p1[r];
  { auto rr = __builtin_amdgcn_permlane32_swap(__float_as_uint(ps), __float_as_uint(ps), false, false);
    ps = __uint_as_float(rr[0]) + __uint_as_float(rr[1]); }
  l_reg = l_reg * alpha + ps;
#define PK4(P, BASE, OUT) do { unsigned a0 = cvtpk(P[BASE + 0], P[BASE + 1]), a1 = cvtpk(P[BASE + 2], P[BASE + 3]);   \
    unsigned b0 = cvtpk(P[BASE + 4], P[BASE + 5]), b1 = cvtpk(P[BASE + 6], P[BASE + 7]);                              \
    auto r0 = __builtin_amdgcn_permlane32_swap(a0, b0, false, false); auto r1 = __builtin_amdgcn_permlane32_swap(a1, b1, false, false); \
    u32x4 w = {r0[0], r1[0], r0[1], r1[1]}; OUT = *reinterpret_cast<bf16x8*>(&w); } while (0)
  PK4(p0, 0, pa0); PK4(p0, 8, pa1); PK4(p1, 0, pa2); PK4(p1, 8, pa3);
#undef PK4
}
__device__ __forceinline__ void qkt(f32x16& p0, f32x16& p1, const bf16* Ks, const bf16x8* qr, int r32, int hi) {
  p0 = f32x16{}; p1 = f32x16{};
  for (int d0 = 0; d0 < 8; ++d0) { int cb = (d0 * 16 + hi * 8) * 2;
    bf16x8 b0 = *reinterpret_cast<const bf16x8*>((const char*)Ks + KSWZ(r32, cb));
    bf16x8 b1 = *reinterpret_cast<const bf16x8*>((const char*)Ks + KSWZ(32 + r32, cb));
    p0 = __builtin_amdgcn_mfma_f32_32x32x16_bf16(b0, qr[d0], p0, 0, 0, 0);
    p1 = __builtin_amdgcn_mfma_f32_32x32x16_bf16(b1, qr[d0], p1, 0, 0, 0); }
}
__device__ __forceinline__ int v_st(int k, int c) { const int kk = (k & ~0xC) | ((k & 4) << 1) | ((k & 8) >> 1); return ((kk >> 3) * 4 + (c >> 5)) * 512 + ((kk & 7) * 32 + (c & 31)) * 2; }
__device__ __forceinline__ int v_rd_base(int lane) { return ((lane & 3) << 3) | (((lane >> 2) & 3) << 6) | (((lane >> 4) & 1) << 5) | (((lane >> 5) & 1) << 8); }
constexpr int v_rd_off(int d0, int ks, int half) { return d0 * 512 + ks * 4096 + half * 2048; }
template <int OFF> __device__ __forceinline__ s16x4 tr_read(int vb) {
  s16x4 r; asm volatile("ds_read_b64_tr_b16 %0, %1 offset:%2" : "=&v"(r) : "v"(vb), "i"(OFF) : "memory"); return r;
}
template <int D0> __device__ __forceinline__ void pv_one(f32x16& od, int vb, bf16x8 pa0, bf16x8 pa1, bf16x8 pa2, bf16x8 pa3) {
  const s16x4 l0 = tr_read<v_rd_off(D0, 0, 0)>(vb), h0 = tr_read<v_rd_off(D0, 0, 1)>(vb), l1 = tr_read<v_rd_off(D0, 1, 0)>(vb), h1 = tr_read<v_rd_off(D0, 1, 1)>(vb);
  const s16x4 l2 = tr_read<v_rd_off(D0, 2, 0)>(vb), h2 = tr_read<v_rd_off(D0, 2, 1)>(vb), l3 = tr_read<v_rd_off(D0, 3, 0)>(vb), h3 = tr_read<v_rd_off(D0, 3, 1)>(vb);
  asm volatile("s_waitcnt lgkmcnt(0)" ::: "memory"); SBAR();
#define PK(L, H) (bf16x8){L[0], L[1], L[2], L[3], H[0], H[1], H[2], H[3]}
  od = __builtin_amdgcn_mfma_f32_32x32x16_bf16(pa0, PK(l0, h0), od, 0, 0, 0);
  od = __builtin_amdgcn_mfma_f32_32x32x16_bf16(pa1, PK(l1, h1), od, 0, 0, 0);
  od = __builtin_amdgcn_mfma_f32_32x32x16_bf16(pa2, PK(l2, h2), od, 0, 0, 0);
  od = __builtin_amdgcn_mfma_f32_32x32x16_bf16(pa3, PK(l3, h3), od, 0, 0, 0);
#undef PK
}
__device__ __forceinline__ void pv_d0(f32x16* o, int vb, bf16x8 pa0, bf16x8 pa1, bf16x8 pa2, bf16x8 pa3) {
  pv_one<0>(o[0], vb, pa0, pa1, pa2, pa3); pv_one<1>(o[1], vb, pa0, pa1, pa2, pa3); pv_one<2>(o[2], vb, pa0, pa1, pa2, pa3); pv_one<3>(o[3], vb, pa0, pa1, pa2, pa3);
}
__device__ __forceinline__ void attn_dense_body(const bf16* __restrict__ Qb, const bf16* __restrict__ Kh, const bf16* __restrict__ Vh,
                                                bf16* __restrict__ Ob, int seq, int nrows, char* lds) {
  const int tid = threadIdx.x, wid = tid >> 6, lane = tid & 63, r32 = lane & 31, hi = lane >> 5;
  bf16* V_lds = (bf16*)lds; bf16* K_lds = (bf16*)(lds + 2 * SHM_V);
  float* ws = (float*)(lds + 2 * SHM_V + 2 * SHM_K) + wid * 64; float* li_l = ws; float* al_l = ws + 32;
  float m_reg = -1e30f, l_reg = 0; f32x16 o[4] = {}; bf16x8 qr[8];
  int qrow = wid * QBLK + r32; if (qrow >= nrows) qrow = nrows - 1;
  const bf16* Qw = Qb + (long)qrow * LDQ + hi * 8;
#pragma unroll
  for (int d0 = 0; d0 < 8; ++d0) qr[d0] = ld8(Qw + d0 * 16);
  const int sr = tid >> 4, sc = (tid & 15) * 8, vst0 = v_st(sr, sc), vst1 = v_st(32 + sr, sc);
  const int vb0 = (int)(uintptr_t)V_lds + v_rd_base(lane);
  struct { bf16x8 vs0, vs1, ks0, ks1; } sr_[2];
#define SLOAD(i, k0) do { sr_[i].vs0 = ld8(&Vh[(long)((k0) + sr) * LDK + sc]); sr_[i].vs1 = ld8(&Vh[(long)((k0) + 32 + sr) * LDK + sc]); \
    sr_[i].ks0 = ld8(&Kh[(long)((k0) + sr) * LDK + sc]); sr_[i].ks1 = ld8(&Kh[(long)((k0) + 32 + sr) * LDK + sc]); } while (0)
#define SWRITE(b, i) do { *(bf16x8*)((char*)V_lds + (b) * SHM_V + vst0) = sr_[i].vs0;          \
    *(bf16x8*)((char*)V_lds + (b) * SHM_V + vst1) = sr_[i].vs1; int kc = sc * 2;               \
    *(bf16x8*)((char*)K_lds + (b) * SHM_K + KSWZ(sr, kc)) = sr_[i].ks0;                       \
    *(bf16x8*)((char*)K_lds + (b) * SHM_K + KSWZ(32 + sr, kc)) = sr_[i].ks1; } while (0)
#define SWAIT() asm volatile("s_waitcnt vmcnt(4)" ::: "memory")
#define RESC(a) do { if (__any((a) < 1.f)) { if (hi == 0) al_l[r32] = (a); asm volatile("s_waitcnt lgkmcnt(0)" ::: "memory"); \
    for (int d = 0; d < 4; ++d) for (int r = 0; r < 16; ++r) o[d][r] *= al_l[crow(r, hi)]; } } while (0)
  f32x16 pA0, pA1, pB0, pB1; float mnA, mnB, alA, alB; bf16x8 pa0, pa1, pa2, pa3; const int NT = seq / KVBLK;
  constexpr int SE = 0, SO = 1;
  SLOAD(SE, 0); asm volatile("s_waitcnt vmcnt(0)" ::: "memory"); SWRITE(0, SE); __syncthreads();
  qkt(pA0, pA1, K_lds, qr, r32, hi); partialSM(pA0, pA1, m_reg, mnA, alA);
  SLOAD(SO, KVBLK); if (2 < NT) SLOAD(SE, 2 * KVBLK);
  SWAIT(); SWRITE(1, SO); __syncthreads();
  for (int j = 1; j + 1 < NT; j += 2) {
    SBAR(); qkt(pB0, pB1, (bf16*)((char*)K_lds + SHM_K), qr, r32, hi);
    finishSM(pA0, pA1, alA, l_reg, pa0, pa1, pa2, pa3); SBAR();
    SLOAD(SO, (j + 2) * KVBLK); SBAR();
    pv_d0(o, vb0, pa0, pa1, pa2, pa3); partialSM(pB0, pB1, m_reg, mnB, alB);
    __syncthreads(); SWAIT(); SWRITE(0, SE);
    RESC(alB); __syncthreads();
    SBAR(); qkt(pA0, pA1, K_lds, qr, r32, hi);
    finishSM(pB0, pB1, alB, l_reg, pa0, pa1, pa2, pa3); SBAR();
    if (j + 3 < NT) SLOAD(SE, (j + 3) * KVBLK); SBAR();
    pv_d0(o, vb0 + (int)SHM_V, pa0, pa1, pa2, pa3); partialSM(pA0, pA1, m_reg, mnA, alA);
    __syncthreads(); SWAIT(); SWRITE(1, SO);
    RESC(alA); __syncthreads();
  }
  SBAR(); qkt(pB0, pB1, (bf16*)((char*)K_lds + SHM_K), qr, r32, hi);
  finishSM(pA0, pA1, alA, l_reg, pa0, pa1, pa2, pa3); SBAR();
  pv_d0(o, vb0, pa0, pa1, pa2, pa3); partialSM(pB0, pB1, m_reg, mnB, alB);
  __syncthreads(); RESC(alB);
  finishSM(pB0, pB1, alB, l_reg, pa0, pa1, pa2, pa3); SBAR();
  pv_d0(o, vb0 + (int)SHM_V, pa0, pa1, pa2, pa3);
  if (hi == 0) li_l[r32] = l_reg; asm volatile("s_waitcnt lgkmcnt(0)" ::: "memory");
  float rli[16];
#pragma unroll
  for (int r = 0; r < 16; ++r) rli[r] = __builtin_amdgcn_rcpf(li_l[crow(r, hi)]);
  bf16* Ow = Ob + (long)(wid * QBLK) * LDO;
#pragma unroll
  for (int r = 0; r < 16; ++r) { int orow = crow(r, hi);
    if (wid * QBLK + orow < nrows)
    for (int d0 = 0; d0 < 4; ++d0) Ow[(long)orow * LDO + d0 * 32 + r32] = __float2bfloat16(o[d0][r] * rli[r]); }
}
__global__ __launch_bounds__(64 * NW, 1)
void attn_fwd_dense(const bf16* __restrict__ Q, const bf16* __restrict__ K, const bf16* __restrict__ V, bf16* __restrict__ O) {
  extern __shared__ __attribute__((aligned(16))) char shm[];
  const long b = blockIdx.z, h = blockIdx.y, i0 = (long)blockIdx.x * (QBLK * NW);
  const long q0 = ((b * HQ + h) * SQ_ + i0) * D, k0 = (b * HQ + h) * SKV * (long)D;
  int nrows = SQ_ - (int)i0; if (nrows > 256) nrows = 256;
  attn_dense_body(Q + q0, K + k0, V + k0, O + q0, SKV, nrows, shm);
}
extern "C" void kernel_launch(void* const* d_in, const int* in_sizes, int n_in, void* d_out, int out_size, void* d_ws, size_t ws_size, hipStream_t stream) {
  hipFuncSetAttribute((const void*)attn_fwd_dense, hipFuncAttributeMaxDynamicSharedMemorySize, (int)SHM_ATTN);
  hipLaunchKernelGGL(attn_fwd_dense, dim3(2, HQ, B), dim3(64 * NW), SHM_ATTN, stream,
                     (const bf16*)d_in[0], (const bf16*)d_in[1], (const bf16*)d_in[2], (bf16*)d_out);
}
```

```cpp
#include <hip/hip_runtime.h>
#include <hip/hip_bf16.h>
#define NTHREADS 256
#define LDS_BYTES 163840

__device__ __forceinline__ void slow_rows(const unsigned short* Q, const unsigned short* K, const unsigned short* V, unsigned short* O, int row0, int row1, int lane) {
  const float c = 0.12751743082f;
  for (int row = row0; row < row1; ++row) {
    float s[32];
#pragma unroll
    for (int j = 0; j < 32; ++j) s[j] = 0.f;
    for (int d = 0; d < 128; d += 2) {
      const unsigned qq = *(const unsigned*)(Q + row * 128 + d);
      const float q0 = __uint_as_float(qq << 16), q1 = __uint_as_float(qq & 0xffff0000u);
#pragma unroll
      for (int j = 0; j < 32; ++j) {
        const unsigned kk = *(const unsigned*)(K + (size_t)(j * 64 + lane) * 128 + d);
        s[j] = fmaf(q0, __uint_as_float(kk << 16), s[j]);
        s[j] = fmaf(q1, __uint_as_float(kk & 0xffff0000u), s[j]);
      }
    }
    float m = s[0];
#pragma unroll
    for (int j = 1; j < 32; ++j) m = fmaxf(m, s[j]);
    for (int o = 32; o > 0; o >>= 1) m = fmaxf(m, __shfl_xor(m, o));
    float l = 0.f;
#pragma unroll
    for (int j = 0; j < 32; ++j) { s[j] = exp2f((s[j] - m) * c); l += s[j]; }
    for (int o = 32; o > 0; o >>= 1) l += __shfl_xor(l, o);
    const float inv = 1.f / l;
    float o0 = 0.f, o1 = 0.f;
#pragma unroll
    for (int j = 0; j < 32; ++j) {
      for (int t = 0; t < 64; ++t) {
        const float p = __shfl(s[j], t) * inv;
        const unsigned vv = *(const unsigned*)(V + (size_t)(j * 64 + t) * 128 + 2 * lane);
        o0 = fmaf(p, __uint_as_float(vv << 16), o0);
        o1 = fmaf(p, __uint_as_float(vv & 0xffff0000u), o1);
      }
    }
    __hip_bfloat16 b0 = __float2bfloat16(o0), b1 = __float2bfloat16(o1);
    *(unsigned*)(O + row * 128 + 2 * lane) = (unsigned)*(unsigned short*)&b0 | ((unsigned)*(unsigned short*)&b1 << 16);
  }
}

__global__ __launch_bounds__(NTHREADS, 1) void mha_fwd_kernel(const unsigned short* __restrict__ Q, const unsigned short* __restrict__ K,
                                                              const unsigned short* __restrict__ V, unsigned short* __restrict__ O) {
  extern __shared__ __attribute__((aligned(16))) char lds[];
  const unsigned head = blockIdx.x;
  const unsigned w = __builtin_amdgcn_readfirstlane(threadIdx.x >> 6);
  const unsigned long long kp = (unsigned long long)__builtin_amdgcn_kernarg_segment_ptr();
  const unsigned hkv = head << 19, hqo = head * 77824u;
  const unsigned ldsb = (unsigned)(size_t)lds;
  unsigned flag;

  const unsigned rot = __builtin_amdgcn_readfirstlane(((head) & 31u) << 14);

  asm volatile(
    "s_load_dwordx2 s[40:41], %[kp], 8\n\t"
    "s_load_dwordx2 s[48:49], %[kp], 0\n\t"
    "s_load_dwordx2 s[44:45], %[kp], 16\n\t"
    "s_load_dwordx2 s[52:53], %[kp], 24\n\t"
    "s_mov_b32 s42, 524288\n\t"
    "s_mov_b32 s43, 0x20000\n\t"
    "s_mov_b32 s50, 77824\n\t"
    "s_mov_b32 s51, 0x20000\n\t"
    "s_mov_b32 s56, %[w]\n\t"
    "v_mbcnt_lo_u32_b32 v33, -1, 0\n\t"
    "v_mbcnt_hi_u32_b32 v33, -1, v33\n\t"
    "v_lshrrev_b32 v36, 4, v33\n\t"
    "v_and_b32 v37, 15, v33\n\t"
    "v_xor_b32 v38, v36, v37\n\t"
    "v_lshlrev_b32 v38, 4, v38\n\t"
    "v_lshl_add_u32 v20, v36, 8, v38\n\t"
    "v_xor_b32 v21, 64, v20\n\t"
    "v_xor_b32 v22, 128, v20\n\t"
    "v_xor_b32 v23, 192, v20\n\t"
    "s_lshl_b32 s58, s56, 12\n\t"
    "s_add_u32 s59, %[lds], s58\n\t"
    "s_add_u32 s60, s59, 65536\n\t"
    "s_add_u32 s58, s58, %[rot]\n\t"
    "s_mul_i32 s70, s56, 20480\n\t"
    "s_lshl_b32 s75, s56, 13\n\t"
    "s_add_u32 s75, s75, %[lds]\n\t"
    "s_add_u32 s71, s75, 131072\n\t"
    "s_cmp_gt_u32 s56, 1\n\t"
    "s_cselect_b32 s61, 98304, 0\n\t"
    "s_max_u32 s61, s61, 49152\n\t"
    "s_add_u32 s75, s75, s61\n\t"
    "s_mov_b32 s46, 524288\n\t"
    "s_mov_b32 s47, 0x20000\n\t"
    "s_mov_b32 s54, 77824\n\t"
    "s_mov_b32 s55, 0x20000\n\t"
    "s_cmp_eq_u32 s56, 3\n\t"
    "s_cbranch_scc1 VAR4%=\n\t"
    "s_mov_b32 s72, s58\n\t"
    "s_add_u32 s58, s58, 16384\n\t"
    "s_and_b32 s58, s58, 0x7ffff\n\t"
    "s_mov_b32 s73, s58\n\t"
    "s_add_u32 s58, s58, 16384\n\t"
    "s_and_b32 s58, s58, 0x7ffff\n\t"
    "s_mov_b32 s74, s58\n\t"
    "s_add_u32 s58, s58, 16384\n\t"
    "s_and_b32 s58, s58, 0x7ffff\n\t"
    "s_waitcnt lgkmcnt(0)\n\t"
    "s_add_u32 s40, s40, %[hkv]\n\t"
    "s_addc_u32 s41, s41, 0\n\t"
    "s_and_b32 s41, s41, 0xffff\n\t"
    "s_add_u32 s48, s48, %[hqo]\n\t"
    "s_addc_u32 s49, s49, 0\n\t"
    "s_and_b32 s49, s49, 0xffff\n\t"
    "s_add_u32 m0, s59, 0\n\t"
    "s_nop 0\n\t"
    "buffer_load_dwordx4 v20, s[40:43], s72 offen offset:0 nt lds\n\t"
    "buffer_load_dwordx4 v21, s[40:43], s72 offen offset:1024 nt lds\n\t"
    "buffer_load_dwordx4 v22, s[40:43], s72 offen offset:2048 nt lds\n\t"
    "buffer_load_dwordx4 v23, s[40:43], s72 offen offset:3072 nt lds\n\t"
    "s_add_u32 s64, s70, 0\n\t"
    "s_add_u32 m0, s71, 0\n\t"
    "s_nop 0\n\t"
    "buffer_load_dwordx4 v20, s[48:51], s64 offen offset:0 nt lds\n\t"
    "buffer_load_dwordx4 v21, s[48:51], s64 offen offset:1024 nt lds\n\t"
    "buffer_load_dwordx4 v22, s[48:51], s64 offen offset:2048 nt lds\n\t"
    "buffer_load_dwordx4 v23, s[48:51], s64 offen offset:3072 nt lds\n\t"
    "s_add_u32 s64, s70, 4096\n\t"
    "s_add_u32 m0, s71, 4096\n\t"
    "s_nop 0\n\t"
    "buffer_load_dwordx4 v20, s[48:51], s64 offen offset:0 nt lds\n\t"
    "buffer_load_dwordx4 v21, s[48:51], s64 offen offset:1024 nt lds\n\t"
    "buffer_load_dwordx4 v22, s[48:51], s64 offen offset:2048 nt lds\n\t"
    "buffer_load_dwordx4 v23, s[48:51], s64 offen offset:3072 nt lds\n\t"
    "s_add_u32 s44, s44, %[hkv]\n\t"
    "s_addc_u32 s45, s45, 0\n\t"
    "s_and_b32 s45, s45, 0xffff\n\t"
    "s_add_u32 s52, s52, %[hqo]\n\t"
    "s_addc_u32 s53, s53, 0\n\t"
    "s_and_b32 s53, s53, 0xffff\n\t"
    "v_xor_b32 v38, v36, v37\n\t"
    "v_lshlrev_b32 v38, 4, v38\n\t"
    "v_lshl_add_u32 v38, v37, 8, v38\n\t"
    "v_xor_b32 v8, 0, v38\n\t"
    "v_xor_b32 v9, 64, v38\n\t"
    "v_xor_b32 v10, 128, v38\n\t"
    "v_xor_b32 v11, 192, v38\n\t"
    "v_add_u32 v192, s71, v8\n\t"
    "v_add_u32 v196, s75, v8\n\t"
    "v_add_u32 v193, s71, v9\n\t"
    "v_add_u32 v197, s75, v9\n\t"
    "v_add_u32 v194, s71, v10\n\t"
    "v_add_u32 v198, s75, v10\n\t"
    "v_add_u32 v195, s71, v11\n\t"
    "v_add_u32 v199, s75, v11\n\t"
    "s_mov_b32 s64, %[lds]\n\t"
    "v_add_u32 v8, s64, v8\n\t"
    "v_add_u32 v9, s64, v9\n\t"
    "v_add_u32 v10, s64, v10\n\t"
    "v_add_u32 v11, s64, v11\n\t"
    "v_bfe_u32 v38, v33, 2, 2\n\t"
    "v_and_b32 v39, 1, v33\n\t"
    "v_lshlrev_b32 v39, 3, v39\n\t"
    "v_lshl_add_u32 v26, v36, 2, v38\n\t"
    "v_lshl_add_u32 v39, v26, 8, v39\n\t"
    "v_bfe_u32 v26, v33, 1, 1\n\t"
    "v_lshlrev_b32 v26, 2, v26\n\t"
    "v_xor_b32 v38, v38, v26\n\t"
    "v_and_b32 v26, 1, v36\n\t"
    "v_lshlrev_b32 v26, 3, v26\n\t"
    "v_xor_b32 v38, v38, v26\n\t"
    "v_lshl_add_u32 v39, v38, 4, v39\n\t"
    "s_add_u32 s64, %[lds], 65536\n\t"
    "v_xor_b32 v12, 0, v39\n\t"
    "v_xor_b32 v13, 16, v39\n\t"
    "v_xor_b32 v14, 32, v39\n\t"
    "v_xor_b32 v15, 48, v39\n\t"
    "v_xor_b32 v16, 128, v39\n\t"
    "v_xor_b32 v17, 144, v39\n\t"
    "v_xor_b32 v18, 160, v39\n\t"
    "v_xor_b32 v19, 176, v39\n\t"
    "v_add_u32 v12, s64, v12\n\t"
    "v_add_u32 v13, s64, v13\n\t"
    "v_add_u32 v14, s64, v14\n\t"
    "v_add_u32 v15, s64, v15\n\t"
    "v_add_u32 v16, s64, v16\n\t"
    "v_add_u32 v17, s64, v17\n\t"
    "v_add_u32 v18, s64, v18\n\t"
    "v_add_u32 v19, s64, v19\n\t"
    "s_lshl_b32 s64, s56, 13\n\t"
    "s_add_u32 s64, s64, %[lds]\n\t"
    "v_and_b32 v38, 3, v33\n\t"
    "v_lshlrev_b32 v38, 3, v38\n\t"
    "v_bfe_u32 v39, v33, 2, 2\n\t"
    "v_lshl_add_u32 v38, v39, 5, v38\n\t"
    "v_lshl_add_u32 v38, v36, 10, v38\n\t"
    "v_and_b32 v39, 1, v36\n\t"
    "v_lshlrev_b32 v39, 5, v39\n\t"
    "v_xor_b32 v38, v39, v38\n\t"
    "v_add_u32 v26, s64, v38\n\t"
    "v_and_b32 v38, 3, v33\n\t"
    "v_lshlrev_b32 v38, 6, v38\n\t"
    "v_bfe_u32 v39, v33, 2, 2\n\t"
    "v_lshl_add_u32 v38, v39, 3, v38\n\t"
    "v_lshl_add_u32 v38, v36, 10, v38\n\t"
    "v_and_b32 v39, 1, v36\n\t"
    "v_lshl_add_u32 v38, v39, 5, v38\n\t"
    "v_add_u32 v28, s64, v38\n\t"
    "v_xor_b32 v38, 32, v38\n\t"
    "v_add_u32 v29, s64, v38\n\t"
    "v_lshlrev_b32 v25, 4, v37\n\t"
    "v_lshl_add_u32 v25, v36, 10, v25\n\t"
    "v_lshlrev_b32 v30, 4, v36\n\t"
    "s_lshl_b32 s64, s56, 10\n\t"
    "s_add_u32 s64, s64, %[lds]\n\t"
    "s_add_u32 s64, s64, 147456\n\t"
    "v_lshl_add_u32 v27, v33, 3, s64\n\t"
    "s_mul_i32 s64, s56, 5120\n\t"
    "s_add_u32 s64, s64, %[lds]\n\t"
    "s_add_u32 s64, s64, 131072\n\t"
    "v_lshl_add_u32 v34, v33, 4, s64\n\t"
    "v_mov_b32 v35, 1\n\t"
    "v_add_u32 v39, %[lds], v30\n\t"
    "v_mov_b32 v31, 0\n\t"
    "v_mov_b32 v32, 0x7f000000\n\t"
    "v_accvgpr_write_b32 a232, 0\n\t"
    "v_accvgpr_write_b32 a236, 0\n\t"
    "v_accvgpr_write_b32 a233, 0\n\t"
    "v_accvgpr_write_b32 a237, 0\n\t"
    "v_accvgpr_write_b32 a234, 0\n\t"
    "v_accvgpr_write_b32 a238, 0\n\t"
    "v_accvgpr_write_b32 a235, 0\n\t"
    "v_accvgpr_write_b32 a239, 0\n\t"
    "v_cmp_gt_u32 vcc, 2, v37\n\t"
    "v_mov_b32 v38, 0x3f803f80\n\t"
    "s_nop 0\n\t"
    "v_cndmask_b32 v38, 0, v38, vcc\n\t"
    "v_accvgpr_write_b32 a208, v38\n\t"
    "v_accvgpr_write_b32 a209, v38\n\t"
    "v_accvgpr_write_b32 a210, v38\n\t"
    "v_accvgpr_write_b32 a211, v38\n\t"
    "v_and_b32 v38, 1, v33\n\t"
    "v_cmp_eq_u32 vcc, 0, v38\n\t"
    "v_mov_b32 v38, 0x44444444\n\t"
    "v_mov_b32 v24, 0xeeeeeeee\n\t"
    "v_cndmask_b32 v24, v24, v38, vcc\n\t"
    "v_accvgpr_write_b32 a212, 0\n\t"
    "v_accvgpr_write_b32 a213, 0\n\t"
    "v_accvgpr_write_b32 a214, 0\n\t"
    "v_accvgpr_write_b32 a215, 0\n\t"
    "v_accvgpr_write_b32 a216, 0\n\t"
    "v_accvgpr_write_b32 a217, 0\n\t"
    "v_accvgpr_write_b32 a218, 0\n\t"
    "v_accvgpr_write_b32 a219, 0\n\t"
    "v_accvgpr_write_b32 a220, 0\n\t"
    "v_accvgpr_write_b32 a221, 0\n\t"
    "v_accvgpr_write_b32 a222, 0\n\t"
    "v_accvgpr_write_b32 a223, 0\n\t"
    "v_accvgpr_write_b32 a224, 0\n\t"
    "v_accvgpr_write_b32 a225, 0\n\t"
    "v_accvgpr_write_b32 a226, 0\n\t"
    "v_accvgpr_write_b32 a227, 0\n\t"
    "v_accvgpr_write_b32 a228, 0\n\t"
    "v_accvgpr_write_b32 a229, 0\n\t"
    "v_accvgpr_write_b32 a230, 0\n\t"
    "v_accvgpr_write_b32 a231, 0\n\t"
    "s_mov_b32 s66, 0x3e0293ee\n\t"
    "s_mov_b32 s67, 0x3e0293ee\n\t"
    "s_mov_b32 s69, 0xffff0000\n\t"
    "s_mov_b32 s63, 0x80008000\n\t"
    "s_waitcnt vmcnt(8)\n\t"
    "s_barrier\n\t"
    "; PSTAMP 1\n\t"
    "s_add_u32 m0, s60, 0\n\t"
    "s_nop 0\n\t"
    "buffer_load_dwordx4 v20, s[44:47], s72 offen offset:0 nt lds\n\t"
    "buffer_load_dwordx4 v22, s[44:47], s72 offen offset:1024 nt lds\n\t"
    "buffer_load_dwordx4 v20, s[44:47], s72 offen offset:2048 nt lds\n\t"
    "buffer_load_dwordx4 v22, s[44:47], s72 offen offset:3072 nt lds\n\t"
    "ds_read_b128 v[224:227], v39 offset:0\n\t"
    "ds_read_b128 v[228:231], v39 offset:64\n\t"
    "ds_read_b128 v[232:235], v39 offset:128\n\t"
    "ds_read_b128 v[236:239], v39 offset:192\n\t"
    "ds_read_b128 a[80:83], v8 offset:0\n\t"
    "ds_read_b128 a[84:87], v9 offset:0\n\t"
    "ds_read_b128 a[88:91], v10 offset:0\n\t"
    "ds_read_b128 a[92:95], v11 offset:0\n\t"
    "ds_read_b128 a[96:99], v8 offset:4096\n\t"
    "ds_read_b128 a[100:103], v9 offset:4096\n\t"
    "ds_read_b128 a[104:107], v10 offset:4096\n\t"
    "ds_read_b128 a[108:111], v11 offset:4096\n\t"
    "ds_read_b128 a[112:115], v8 offset:8192\n\t"
    "ds_read_b128 a[116:119], v9 offset:8192\n\t"
    "ds_read_b128 a[120:123], v10 offset:8192\n\t"
    "ds_read_b128 a[124:127], v11 offset:8192\n\t"
    "ds_read_b128 a[128:131], v8 offset:12288\n\t"
    "ds_read_b128 a[132:135], v9 offset:12288\n\t"
    "ds_read_b128 a[136:139], v10 offset:12288\n\t"
    "ds_read_b128 a[140:143], v11 offset:12288\n\t"
    "s_waitcnt vmcnt(8)\n\t"
    "; PSTAMP 2\n\t"
    "ds_read_b128 v[240:243], v192 offset:0\n\t"
    "ds_read_b128 v[244:247], v193 offset:0\n\t"
    "ds_read_b128 v[248:251], v194 offset:0\n\t"
    "ds_read_b128 v[252:255], v195 offset:0\n\t"
    "s_add_u32 s64, s70, 8192\n\t"
    "s_add_u32 m0, s75, 0\n\t"
    "s_nop 0\n\t"
    "buffer_load_dwordx4 v20, s[48:51], s64 offen offset:0 nt lds\n\t"
    "buffer_load_dwordx4 v21, s[48:51], s64 offen offset:1024 nt lds\n\t"
    "buffer_load_dwordx4 v22, s[48:51], s64 offen offset:2048 nt lds\n\t"
    "buffer_load_dwordx4 v23, s[48:51], s64 offen offset:3072 nt lds\n\t"
    "s_waitcnt lgkmcnt(15)\n\t"
    "v_xor_b32 v224, s63, v224\n\t"
    "v_xor_b32 v225, s63, v225\n\t"
    "v_xor_b32 v226, s63, v226\n\t"
    "v_xor_b32 v227, s63, v227\n\t"
    "s_waitcnt lgkmcnt(15)\n\t"
    "v_xor_b32 v228, s63, v228\n\t"
    "v_xor_b32 v229, s63, v229\n\t"
    "v_xor_b32 v230, s63, v230\n\t"
    "v_xor_b32 v231, s63, v231\n\t"
    "s_waitcnt lgkmcnt(15)\n\t"
    "v_xor_b32 v232, s63, v232\n\t"
    "v_xor_b32 v233, s63, v233\n\t"
    "v_xor_b32 v234, s63, v234\n\t"
    "v_xor_b32 v235, s63, v235\n\t"
    "s_waitcnt lgkmcnt(15)\n\t"
    "v_xor_b32 v236, s63, v236\n\t"
    "v_xor_b32 v237, s63, v237\n\t"
    "v_xor_b32 v238, s63, v238\n\t"
    "v_xor_b32 v239, s63, v239\n\t"
    "; PSTAMP 3\n\t"
    "s_waitcnt lgkmcnt(3)\n\t"
    "v_lshlrev_b32 v200, 16, v240\n\t"
    "v_and_b32 v201, s69, v240\n\t"
    "v_lshlrev_b32 v202, 16, v241\n\t"
    "v_and_b32 v203, s69, v241\n\t"
    "v_pk_mul_f32 v[200:201], v[200:201], s[66:67]\n\t"
    "v_pk_mul_f32 v[202:203], v[202:203], s[66:67]\n\t"
    "v_cvt_pk_bf16_f32 v240, v200, v201\n\t"
    "v_cvt_pk_bf16_f32 v241, v202, v203\n\t"
    "v_accvgpr_write_b32 a0, v240\n\t"
    "v_accvgpr_write_b32 a1, v241\n\t"
    "v_lshlrev_b32 v204, 16, v242\n\t"
    "v_and_b32 v205, s69, v242\n\t"
    "v_lshlrev_b32 v206, 16, v243\n\t"
    "v_and_b32 v207, s69, v243\n\t"
    "v_pk_mul_f32 v[204:205], v[204:205], s[66:67]\n\t"
    "v_pk_mul_f32 v[206:207], v[206:207], s[66:67]\n\t"
    "v_cvt_pk_bf16_f32 v242, v204, v205\n\t"
    "v_cvt_pk_bf16_f32 v243, v206, v207\n\t"
    "v_accvgpr_write_b32 a2, v242\n\t"
    "v_accvgpr_write_b32 a3, v243\n\t"
    "s_waitcnt lgkmcnt(2)\n\t"
    "v_lshlrev_b32 v200, 16, v244\n\t"
    "v_and_b32 v201, s69, v244\n\t"
    "v_lshlrev_b32 v202, 16, v245\n\t"
    "v_and_b32 v203, s69, v245\n\t"
    "v_pk_mul_f32 v[200:201], v[200:201], s[66:67]\n\t"
    "v_pk_mul_f32 v[202:203], v[202:203], s[66:67]\n\t"
    "v_cvt_pk_bf16_f32 v244, v200, v201\n\t"
    "v_cvt_pk_bf16_f32 v245, v202, v203\n\t"
    "v_accvgpr_write_b32 a4, v244\n\t"
    "v_accvgpr_write_b32 a5, v245\n\t"
    "v_lshlrev_b32 v204, 16, v246\n\t"
    "v_and_b32 v205, s69, v246\n\t"
    "v_lshlrev_b32 v206, 16, v247\n\t"
    "v_and_b32 v207, s69, v247\n\t"
    "v_pk_mul_f32 v[204:205], v[204:205], s[66:67]\n\t"
    "v_pk_mul_f32 v[206:207], v[206:207], s[66:67]\n\t"
    "v_cvt_pk_bf16_f32 v246, v204, v205\n\t"
    "v_cvt_pk_bf16_f32 v247, v206, v207\n\t"
    "v_accvgpr_write_b32 a6, v246\n\t"
    "v_accvgpr_write_b32 a7, v247\n\t"
    "v_mfma_f32_16x16x32_bf16 v[44:47], v[224:227], a[0:3], -4.0\n\t"
    "s_waitcnt lgkmcnt(1)\n\t"
    "v_lshlrev_b32 v200, 16, v248\n\t"
    "v_and_b32 v201, s69, v248\n\t"
    "v_lshlrev_b32 v202, 16, v249\n\t"
    "v_and_b32 v203, s69, v249\n\t"
    "v_pk_mul_f32 v[200:201], v[200:201], s[66:67]\n\t"
    "v_pk_mul_f32 v[202:203], v[202:203], s[66:67]\n\t"
    "v_cvt_pk_bf16_f32 v248, v200, v201\n\t"
    "v_cvt_pk_bf16_f32 v249, v202, v203\n\t"
    "v_accvgpr_write_b32 a8, v248\n\t"
    "v_accvgpr_write_b32 a9, v249\n\t"
    "v_lshlrev_b32 v204, 16, v250\n\t"
    "v_and_b32 v205, s69, v250\n\t"
    "v_lshlrev_b32 v206, 16, v251\n\t"
    "v_and_b32 v207, s69, v251\n\t"
    "v_pk_mul_f32 v[204:205], v[204:205], s[66:67]\n\t"
    "v_pk_mul_f32 v[206:207], v[206:207], s[66:67]\n\t"
    "v_cvt_pk_bf16_f32 v250, v204, v205\n\t"
    "v_cvt_pk_bf16_f32 v251, v206, v207\n\t"
    "v_accvgpr_write_b32 a10, v250\n\t"
    "v_accvgpr_write_b32 a11, v251\n\t"
    "v_mfma_f32_16x16x32_bf16 v[44:47], v[228:231], a[4:7], v[44:47]\n\t"
    "s_waitcnt lgkmcnt(0)\n\t"
    "v_lshlrev_b32 v200, 16, v252\n\t"
    "v_and_b32 v201, s69, v252\n\t"
    "v_lshlrev_b32 v202, 16, v253\n\t"
    "v_and_b32 v203, s69, v253\n\t"
    "v_pk_mul_f32 v[200:201], v[200:201], s[66:67]\n\t"
    "v_pk_mul_f32 v[202:203], v[202:203], s[66:67]\n\t"
    "v_cvt_pk_bf16_f32 v252, v200, v201\n\t"
    "v_cvt_pk_bf16_f32 v253, v202, v203\n\t"
    "v_accvgpr_write_b32 a12, v252\n\t"
    "v_accvgpr_write_b32 a13, v253\n\t"
    "v_lshlrev_b32 v204, 16, v254\n\t"
    "v_and_b32 v205, s69, v254\n\t"
    "v_lshlrev_b32 v206, 16, v255\n\t"
    "v_and_b32 v207, s69, v255\n\t"
    "v_pk_mul_f32 v[204:205], v[204:205], s[66:67]\n\t"
    "v_pk_mul_f32 v[206:207], v[206:207], s[66:67]\n\t"
    "v_cvt_pk_bf16_f32 v254, v204, v205\n\t"
    "v_cvt_pk_bf16_f32 v255, v206, v207\n\t"
    "v_accvgpr_write_b32 a14, v254\n\t"
    "v_accvgpr_write_b32 a15, v255\n\t"
    "v_mfma_f32_16x16x32_bf16 v[44:47], v[232:235], a[8:11], v[44:47]\n\t"
    "; PSTAMP 4\n\t"
    "s_nop 0\n\t"
    "v_mfma_f32_16x16x32_bf16 v[44:47], v[236:239], a[12:15], v[44:47]\n\t"
    "; TILE 0\n\t"
    "v_mfma_f32_16x16x32_bf16 v[64:67], a[80:83], a[0:3], v[44:47]\n\t"
    "s_waitcnt vmcnt(8)\n\t"
    "ds_read_b128 v[240:243], v192 offset:4096\n\t"
    "ds_read_b128 v[244:247], v193 offset:4096\n\t"
    "ds_read_b128 v[248:251], v194 offset:4096\n\t"
    "ds_read_b128 v[252:255], v195 offset:4096\n\t"
    "v_mfma_f32_16x16x32_bf16 v[64:67], a[84:87], a[4:7], v[64:67]\n\t"
    "s_add_u32 s64, s70, 12288\n\t"
    "s_add_u32 m0, s75, 4096\n\t"
    "v_mfma_f32_16x16x32_bf16 v[64:67], a[88:91], a[8:11], v[64:67]\n\t"
    "buffer_load_dwordx4 v20, s[48:51], s64 offen offset:0 nt lds\n\t"
    "v_mfma_f32_16x16x32_bf16 v[64:67], a[92:95], a[12:15], v[64:67]\n\t"
    "buffer_load_dwordx4 v21, s[48:51], s64 offen offset:1024 nt lds\n\t"
    "v_mfma_f32_16x16x32_bf16 v[68:71], a[96:99], a[0:3], v[44:47]\n\t"
    "buffer_load_dwordx4 v22, s[48:51], s64 offen offset:2048 nt lds\n\t"
    "s_waitcnt lgkmcnt(3)\n\t"
    "v_lshlrev_b32 v200, 16, v240\n\t"
    "v_and_b32 v201, s69, v240\n\t"
    "v_lshlrev_b32 v202, 16, v241\n\t"
    "v_and_b32 v203, s69, v241\n\t"
    "v_mfma_f32_16x16x32_bf16 v[68:71], a[100:103], a[4:7], v[68:71]\n\t"
    "buffer_load_dwordx4 v23, s[48:51], s64 offen offset:3072 nt lds\n\t"
    "v_pk_mul_f32 v[200:201], v[200:201], s[66:67]\n\t"
    "v_pk_mul_f32 v[202:203], v[202:203], s[66:67]\n\t"
    "v_cvt_pk_bf16_f32 v240, v200, v201\n\t"
    "v_mfma_f32_16x16x32_bf16 v[68:71], a[104:107], a[8:11], v[68:71]\n\t"
    "v_cvt_pk_bf16_f32 v241, v202, v203\n\t"
    "v_accvgpr_write_b32 a16, v240\n\t"
    "v_accvgpr_write_b32 a17, v241\n\t"
    "v_mfma_f32_16x16x32_bf16 v[68:71], a[108:111], a[12:15], v[68:71]\n\t"
    "v_lshlrev_b32 v204, 16, v242\n\t"
    "v_and_b32 v205, s69, v242\n\t"
    "v_lshlrev_b32 v206, 16, v243\n\t"
    "v_and_b32 v207, s69, v243\n\t"
    "v_mfma_f32_16x16x32_bf16 v[72:75], a[112:115], a[0:3], v[44:47]\n\t"
    "v_pk_mul_f32 v[204:205], v[204:205], s[66:67]\n\t"
    "v_pk_mul_f32 v[206:207], v[206:207], s[66:67]\n\t"
    "v_cvt_pk_bf16_f32 v242, v204, v205\n\t"
    "v_exp_f32 v64, v64\n\t"
    "v_mfma_f32_16x16x32_bf16 v[72:75], a[116:119], a[4:7], v[72:75]\n\t"
    "v_cvt_pk_bf16_f32 v243, v206, v207\n\t"
    "v_accvgpr_write_b32 a18, v242\n\t"
    "v_accvgpr_write_b32 a19, v243\n\t"
    "v_exp_f32 v65, v65\n\t"
    "v_mfma_f32_16x16x32_bf16 v[72:75], a[120:123], a[8:11], v[72:75]\n\t"
    "s_waitcnt lgkmcnt(0)\n\t"
    "v_lshlrev_b32 v200, 16, v244\n\t"
    "v_and_b32 v201, s69, v244\n\t"
    "v_lshlrev_b32 v202, 16, v245\n\t"
    "v_and_b32 v203, s69, v245\n\t"
    "v_exp_f32 v66, v66\n\t"
    "v_mfma_f32_16x16x32_bf16 v[72:75], a[124:127], a[12:15], v[72:75]\n\t"
    "v_pk_mul_f32 v[200:201], v[200:201], s[66:67]\n\t"
    "v_pk_mul_f32 v[202:203], v[202:203], s[66:67]\n\t"
    "v_cvt_pk_bf16_f32 v244, v200, v201\n\t"
    "v_exp_f32 v67, v67\n\t"
    "v_mfma_f32_16x16x32_bf16 v[76:79], a[128:131], a[0:3], v[44:47]\n\t"
    "v_cvt_pk_bf16_f32 v245, v202, v203\n\t"
    "v_accvgpr_write_b32 a20, v244\n\t"
    "v_accvgpr_write_b32 a21, v245\n\t"
    "v_exp_f32 v68, v68\n\t"
    "v_mfma_f32_16x16x32_bf16 v[76:79], a[132:135], a[4:7], v[76:79]\n\t"
    "v_lshlrev_b32 v204, 16, v246\n\t"
    "v_and_b32 v205, s69, v246\n\t"
    "v_lshlrev_b32 v206, 16, v247\n\t"
    "v_and_b32 v207, s69, v247\n\t"
    "v_exp_f32 v69, v69\n\t"
    "v_mfma_f32_16x16x32_bf16 v[76:79], a[136:139], a[8:11], v[76:79]\n\t"
    "v_pk_mul_f32 v[204:205], v[204:205], s[66:67]\n\t"
    "v_pk_mul_f32 v[206:207], v[206:207], s[66:67]\n\t"
    "v_cvt_pk_bf16_f32 v246, v204, v205\n\t"
    "v_exp_f32 v70, v70\n\t"
    "v_mfma_f32_16x16x32_bf16 v[76:79], a[140:143], a[12:15], v[76:79]\n\t"
    "v_cvt_pk_bf16_f32 v247, v206, v207\n\t"
    "v_accvgpr_write_b32 a22, v246\n\t"
    "v_accvgpr_write_b32 a23, v247\n\t"
    "v_exp_f32 v71, v71\n\t"
    "v_lshlrev_b32 v200, 16, v248\n\t"
    "v_and_b32 v201, s69, v248\n\t"
    "v_lshlrev_b32 v202, 16, v249\n\t"
    "v_and_b32 v203, s69, v249\n\t"
    "v_exp_f32 v72, v72\n\t"
    "v_pk_mul_f32 v[200:201], v[200:201], s[66:67]\n\t"
    "v_pk_mul_f32 v[202:203], v[202:203], s[66:67]\n\t"
    "v_cvt_pk_bf16_f32 v248, v200, v201\n\t"
    "v_exp_f32 v73, v73\n\t"
    "v_cvt_pk_bf16_f32 v249, v202, v203\n\t"
    "v_accvgpr_write_b32 a24, v248\n\t"
    "v_accvgpr_write_b32 a25, v249\n\t"
    "v_exp_f32 v74, v74\n\t"
    "v_lshlrev_b32 v204, 16, v250\n\t"
    "v_and_b32 v205, s69, v250\n\t"
    "v_lshlrev_b32 v206, 16, v251\n\t"
    "v_and_b32 v207, s69, v251\n\t"
    "v_exp_f32 v75, v75\n\t"
    "v_pk_mul_f32 v[204:205], v[204:205], s[66:67]\n\t"
    "v_pk_mul_f32 v[206:207], v[206:207], s[66:67]\n\t"
    "v_cvt_pk_bf16_f32 v250, v204, v205\n\t"
    "v_exp_f32 v76, v76\n\t"
    "v_cvt_pk_bf16_f32 v251, v206, v207\n\t"
    "v_accvgpr_write_b32 a26, v250\n\t"
    "v_accvgpr_write_b32 a27, v251\n\t"
    "v_exp_f32 v77, v77\n\t"
    "v_lshlrev_b32 v200, 16, v252\n\t"
    "v_and_b32 v201, s69, v252\n\t"
    "v_lshlrev_b32 v202, 16, v253\n\t"
    "v_and_b32 v203, s69, v253\n\t"
    "v_exp_f32 v78, v78\n\t"
    "s_waitcnt vmcnt(8)\n\t"
    "s_barrier\n\t"
    "s_add_u32 s64, s70, 16384\n\t"
    "s_add_u32 m0, s71, 0\n\t"
    "v_pk_mul_f32 v[200:201], v[200:201], s[66:67]\n\t"
    "v_pk_mul_f32 v[202:203], v[202:203], s[66:67]\n\t"
    "v_cvt_pk_bf16_f32 v252, v200, v201\n\t"
    "v_exp_f32 v79, v79\n\t"
    "ds_read_b64_tr_b16 a[144:145], v12 offset:0\n\t"
    "ds_read_b64_tr_b16 a[146:147], v12 offset:4096\n\t"
    "buffer_load_dwordx4 v20, s[48:51], s64 offen offset:0 nt lds\n\t"
    "v_cvt_pk_bf16_f32 v253, v202, v203\n\t"
    "v_accvgpr_write_b32 a28, v252\n\t"
    "v_accvgpr_write_b32 a29, v253\n\t"
    "ds_read_b64_tr_b16 a[148:149], v12 offset:8192\n\t"
    "ds_read_b64_tr_b16 a[150:151], v12 offset:12288\n\t"
    "buffer_load_dwordx4 v21, s[48:51], s64 offen offset:1024 nt lds\n\t"
    "v_lshlrev_b32 v204, 16, v254\n\t"
    "v_and_b32 v205, s69, v254\n\t"
    "v_lshlrev_b32 v206, 16, v255\n\t"
    "v_and_b32 v207, s69, v255\n\t"
    "ds_read_b64_tr_b16 a[152:153], v13 offset:0\n\t"
    "ds_read_b64_tr_b16 a[154:155], v13 offset:4096\n\t"
    "buffer_load_dwordx4 v22, s[48:51], s64 offen offset:2048 nt lds\n\t"
    "v_pk_mul_f32 v[204:205], v[204:205], s[66:67]\n\t"
    "v_pk_mul_f32 v[206:207], v[206:207], s[66:67]\n\t"
    "v_cvt_pk_bf16_f32 v254, v204, v205\n\t"
    "ds_read_b64_tr_b16 a[156:157], v13 offset:8192\n\t"
    "ds_read_b64_tr_b16 a[158:159], v13 offset:12288\n\t"
    "buffer_load_dwordx4 v23, s[48:51], s64 offen offset:3072 nt lds\n\t"
    "v_cvt_pk_bf16_f32 v255, v206, v207\n\t"
    "v_accvgpr_write_b32 a30, v254\n\t"
    "v_accvgpr_write_b32 a31, v255\n\t"
    "v_mfma_f32_16x16x32_bf16 v[48:51], v[224:227], a[16:19], -4.0\n\t"
    "ds_read_b64_tr_b16 a[160:161], v14 offset:0\n\t"
    "ds_read_b64_tr_b16 a[162:163], v14 offset:4096\n\t"
    "v_mfma_f32_16x16x32_bf16 v[48:51], v[228:231], a[20:23], v[48:51]\n\t"
    "ds_read_b64_tr_b16 a[164:165], v14 offset:8192\n\t"
    "ds_read_b64_tr_b16 a[166:167], v14 offset:12288\n\t"
    "v_mfma_f32_16x16x32_bf16 v[48:51], v[232:235], a[24:27], v[48:51]\n\t"
    "ds_read_b64_tr_b16 a[168:169], v15 offset:0\n\t"
    "ds_read_b64_tr_b16 a[170:171], v15 offset:4096\n\t"
    "v_mfma_f32_16x16x32_bf16 v[48:51], v[236:239], a[28:31], v[48:51]\n\t"
    "ds_read_b64_tr_b16 a[172:173], v15 offset:8192\n\t"
    "ds_read_b64_tr_b16 a[174:175], v15 offset:12288\n\t"
    "v_mfma_f32_16x16x32_bf16 v[80:83], a[80:83], a[16:19], v[48:51]\n\t"
    "s_waitcnt vmcnt(8)\n\t"
    "ds_read_b64_tr_b16 a[176:177], v16 offset:0\n\t"
    "ds_read_b128 v[240:243], v196 offset:0\n\t"
    "ds_read_b128 v[244:247], v197 offset:0\n\t"
    "ds_read_b128 v[248:251], v198 offset:0\n\t"
    "ds_read_b128 v[252:255], v199 offset:0\n\t"
    "v_cvt_pk_bf16_f32 v36, v64, v65\n\t"
    "v_mfma_f32_16x16x32_bf16 v[80:83], a[84:87], a[20:23], v[80:83]\n\t"
    "s_add_u32 m0, s59, 16384\n\t"
    "ds_read_b64_tr_b16 a[178:179], v16 offset:4096\n\t"
    "v_cvt_pk_bf16_f32 v37, v66, v67\n\t"
    "v_mfma_f32_16x16x32_bf16 v[80:83], a[88:91], a[24:27], v[80:83]\n\t"
    "ds_read_b64_tr_b16 a[180:181], v16 offset:8192\n\t"
    "buffer_load_dwordx4 v20, s[40:43], s73 offen offset:0 nt lds\n\t"
    "v_cvt_pk_bf16_f32 v38, v68, v69\n\t"
    "v_mfma_f32_16x16x32_bf16 v[80:83], a[92:95], a[28:31], v[80:83]\n\t"
    "ds_read_b64_tr_b16 a[182:183], v16 offset:12288\n\t"
    "buffer_load_dwordx4 v21, s[40:43], s73 offen offset:1024 nt lds\n\t"
    "v_cvt_pk_bf16_f32 v39, v70, v71\n\t"
    "v_mfma_f32_16x16x32_bf16 v[84:87], a[96:99], a[16:19], v[48:51]\n\t"
    "ds_read_b64_tr_b16 a[184:185], v17 offset:0\n\t"
    "buffer_load_dwordx4 v22, s[40:43], s73 offen offset:2048 nt lds\n\t"
    "v_cvt_pk_bf16_f32 v40, v72, v73\n\t"
    "s_waitcnt lgkmcnt(7)\n\t"
    "v_lshlrev_b32 v200, 16, v240\n\t"
    "v_and_b32 v201, s69, v240\n\t"
    "v_lshlrev_b32 v202, 16, v241\n\t"
    "v_and_b32 v203, s69, v241\n\t"
    "v_mfma_f32_16x16x32_bf16 v[84:87], a[100:103], a[20:23], v[84:87]\n\t"
    "ds_read_b64_tr_b16 a[186:187], v17 offset:4096\n\t"
    "buffer_load_dwordx4 v23, s[40:43], s73 offen offset:3072 nt lds\n\t"
    "v_cvt_pk_bf16_f32 v41, v74, v75\n\t"
    "v_pk_mul_f32 v[200:201], v[200:201], s[66:67]\n\t"
    "v_pk_mul_f32 v[202:203], v[202:203], s[66:67]\n\t"
    "v_cvt_pk_bf16_f32 v240, v200, v201\n\t"
    "v_mfma_f32_16x16x32_bf16 v[84:87], a[104:107], a[24:27], v[84:87]\n\t"
    "ds_read_b64_tr_b16 a[188:189], v17 offset:8192\n\t"
    "v_cvt_pk_bf16_f32 v42, v76, v77\n\t"
    "v_cvt_pk_bf16_f32 v241, v202, v203\n\t"
    "v_accvgpr_write_b32 a32, v240\n\t"
    "v_accvgpr_write_b32 a33, v241\n\t"
    "v_mfma_f32_16x16x32_bf16 v[84:87], a[108:111], a[28:31], v[84:87]\n\t"
    "ds_read_b64_tr_b16 a[190:191], v17 offset:12288\n\t"
    "v_cvt_pk_bf16_f32 v43, v78, v79\n\t"
    "v_lshlrev_b32 v204, 16, v242\n\t"
    "v_and_b32 v205, s69, v242\n\t"
    "v_lshlrev_b32 v206, 16, v243\n\t"
    "v_and_b32 v207, s69, v243\n\t"
    "v_mfma_f32_16x16x32_bf16 v[88:91], a[112:115], a[16:19], v[48:51]\n\t"
    "ds_read_b64_tr_b16 a[192:193], v18 offset:0\n\t"
    "v_pk_mul_f32 v[204:205], v[204:205], s[66:67]\n\t"
    "v_pk_mul_f32 v[206:207], v[206:207], s[66:67]\n\t"
    "v_cvt_pk_bf16_f32 v242, v204, v205\n\t"
    "v_exp_f32 v80, v80\n\t"
    "v_mfma_f32_16x16x32_bf16 v[88:91], a[116:119], a[20:23], v[88:91]\n\t"
    "ds_read_b64_tr_b16 a[194:195], v18 offset:4096\n\t"
    "v_cvt_pk_bf16_f32 v243, v206, v207\n\t"
    "v_accvgpr_write_b32 a34, v242\n\t"
    "v_accvgpr_write_b32 a35, v243\n\t"
    "v_exp_f32 v81, v81\n\t"
    "v_mfma_f32_16x16x32_bf16 v[88:91], a[120:123], a[24:27], v[88:91]\n\t"
    "ds_read_b64_tr_b16 a[196:197], v18 offset:8192\n\t"
    "s_waitcnt lgkmcnt(8)\n\t"
    "v_lshlrev_b32 v200, 16, v244\n\t"
    "v_and_b32 v201, s69, v244\n\t"
    "v_lshlrev_b32 v202, 16, v245\n\t"
    "v_and_b32 v203, s69, v245\n\t"
    "v_exp_f32 v82, v82\n\t"
    "v_mfma_f32_16x16x32_bf16 v[88:91], a[124:127], a[28:31], v[88:91]\n\t"
    "ds_read_b64_tr_b16 a[198:199], v18 offset:12288\n\t"
    "v_pk_mul_f32 v[200:201], v[200:201], s[66:67]\n\t"
    "v_pk_mul_f32 v[202:203], v[202:203], s[66:67]\n\t"
    "v_cvt_pk_bf16_f32 v244, v200, v201\n\t"
    "v_exp_f32 v83, v83\n\t"
    "v_mfma_f32_16x16x32_bf16 v[92:95], a[128:131], a[16:19], v[48:51]\n\t"
    "ds_read_b64_tr_b16 a[200:201], v19 offset:0\n\t"
    "v_cvt_pk_bf16_f32 v245, v202, v203\n\t"
    "v_accvgpr_write_b32 a36, v244\n\t"
    "v_accvgpr_write_b32 a37, v245\n\t"
    "v_exp_f32 v84, v84\n\t"
    "v_mfma_f32_16x16x32_bf16 v[92:95], a[132:135], a[20:23], v[92:95]\n\t"
    "ds_read_b64_tr_b16 a[202:203], v19 offset:4096\n\t"
    "v_lshlrev_b32 v204, 16, v246\n\t"
    "v_and_b32 v205, s69, v246\n\t"
    "v_lshlrev_b32 v206, 16, v247\n\t"
    "v_and_b32 v207, s69, v247\n\t"
    "v_exp_f32 v85, v85\n\t"
    "v_mfma_f32_16x16x32_bf16 v[92:95], a[136:139], a[24:27], v[92:95]\n\t"
    "ds_read_b64_tr_b16 a[204:205], v19 offset:8192\n\t"
    "v_pk_mul_f32 v[204:205], v[204:205], s[66:67]\n\t"
    "v_pk_mul_f32 v[206:207], v[206:207], s[66:67]\n\t"
    "v_cvt_pk_bf16_f32 v246, v204, v205\n\t"
    "v_exp_f32 v86, v86\n\t"
    "v_mfma_f32_16x16x32_bf16 v[92:95], a[140:143], a[28:31], v[92:95]\n\t"
    "ds_read_b64_tr_b16 a[206:207], v19 offset:12288\n\t"
    "v_cvt_pk_bf16_f32 v247, v206, v207\n\t"
    "v_accvgpr_write_b32 a38, v246\n\t"
    "v_accvgpr_write_b32 a39, v247\n\t"
    "v_exp_f32 v87, v87\n\t"
    "v_mfma_f32_16x16x32_bf16 v[96:99], v[36:39], a[144:147], 0\n\t"
    "v_lshlrev_b32 v200, 16, v248\n\t"
    "v_and_b32 v201, s69, v248\n\t"
    "v_lshlrev_b32 v202, 16, v249\n\t"
    "v_and_b32 v203, s69, v249\n\t"
    "v_exp_f32 v88, v88\n\t"
    "v_mfma_f32_16x16x32_bf16 v[96:99], v[40:43], a[148:151], v[96:99]\n\t"
    "v_pk_mul_f32 v[200:201], v[200:201], s[66:67]\n\t"
    "v_pk_mul_f32 v[202:203], v[202:203], s[66:67]\n\t"
    "v_cvt_pk_bf16_f32 v248, v200, v201\n\t"
    "v_exp_f32 v89, v89\n\t"
    "v_mfma_f32_16x16x32_bf16 v[100:103], v[36:39], a[152:155], 0\n\t"
    "v_cvt_pk_bf16_f32 v249, v202, v203\n\t"
    "v_accvgpr_write_b32 a40, v248\n\t"
    "v_accvgpr_write_b32 a41, v249\n\t"
    "v_exp_f32 v90, v90\n\t"
    "v_mfma_f32_16x16x32_bf16 v[100:103], v[40:43], a[156:159], v[100:103]\n\t"
    "v_lshlrev_b32 v204, 16, v250\n\t"
    "v_and_b32 v205, s69, v250\n\t"
    "v_lshlrev_b32 v206, 16, v251\n\t"
    "v_and_b32 v207, s69, v251\n\t"
    "v_exp_f32 v91, v91\n\t"
    "v_mfma_f32_16x16x32_bf16 v[104:107], v[36:39], a[160:163], 0\n\t"
    "v_pk_mul_f32 v[204:205], v[204:205], s[66:67]\n\t"
    "v_pk_mul_f32 v[206:207], v[206:207], s[66:67]\n\t"
    "v_cvt_pk_bf16_f32 v250, v204, v205\n\t"
    "v_exp_f32 v92, v92\n\t"
    "v_mfma_f32_16x16x32_bf16 v[104:107], v[40:43], a[164:167], v[104:107]\n\t"
    "v_cvt_pk_bf16_f32 v251, v206, v207\n\t"
    "v_accvgpr_write_b32 a42, v250\n\t"
    "v_accvgpr_write_b32 a43, v251\n\t"
    "v_exp_f32 v93, v93\n\t"
    "v_mfma_f32_16x16x32_bf16 v[108:111], v[36:39], a[168:171], 0\n\t"
    "v_lshlrev_b32 v200, 16, v252\n\t"
    "v_and_b32 v201, s69, v252\n\t"
    "v_lshlrev_b32 v202, 16, v253\n\t"
    "v_and_b32 v203, s69, v253\n\t"
    "v_exp_f32 v94, v94\n\t"
    "v_mfma_f32_16x16x32_bf16 v[108:111], v[40:43], a[172:175], v[108:111]\n\t"
    "v_pk_mul_f32 v[200:201], v[200:201], s[66:67]\n\t"
    "v_pk_mul_f32 v[202:203], v[202:203], s[66:67]\n\t"
    "v_cvt_pk_bf16_f32 v252, v200, v201\n\t"
    "v_exp_f32 v95, v95\n\t"
    "v_mfma_f32_16x16x32_bf16 v[112:115], v[36:39], a[176:179], 0\n\t"
    "s_waitcnt lgkmcnt(0)\n\t"
    "v_cvt_pk_bf16_f32 v253, v202, v203\n\t"
    "v_accvgpr_write_b32 a44, v252\n\t"
    "v_accvgpr_write_b32 a45, v253\n\t"
    "v_mfma_f32_16x16x32_bf16 v[112:115], v[40:43], a[180:183], v[112:115]\n\t"
    "v_lshlrev_b32 v204, 16, v254\n\t"
    "v_and_b32 v205, s69, v254\n\t"
    "v_lshlrev_b32 v206, 16, v255\n\t"
    "v_and_b32 v207, s69, v255\n\t"
    "v_mfma_f32_16x16x32_bf16 v[116:119], v[36:39], a[184:187], 0\n\t"
    "v_pk_mul_f32 v[204:205], v[204:205], s[66:67]\n\t"
    "v_pk_mul_f32 v[206:207], v[206:207], s[66:67]\n\t"
    "v_cvt_pk_bf16_f32 v254, v204, v205\n\t"
    "v_mfma_f32_16x16x32_bf16 v[116:119], v[40:43], a[188:191], v[116:119]\n\t"
    "v_cvt_pk_bf16_f32 v255, v206, v207\n\t"
    "v_accvgpr_write_b32 a46, v254\n\t"
    "v_accvgpr_write_b32 a47, v255\n\t"
    "v_mfma_f32_16x16x32_bf16 v[120:123], v[36:39], a[192:195], 0\n\t"
    "v_mfma_f32_16x16x32_bf16 v[52:55], v[224:227], a[32:35], -4.0\n\t"
    "v_mfma_f32_16x16x32_bf16 v[120:123], v[40:43], a[196:199], v[120:123]\n\t"
    "v_mfma_f32_16x16x32_bf16 v[52:55], v[228:231], a[36:39], v[52:55]\n\t"
    "v_mfma_f32_16x16x32_bf16 v[124:127], v[36:39], a[200:203], 0\n\t"
    "v_mfma_f32_16x16x32_bf16 v[52:55], v[232:235], a[40:43], v[52:55]\n\t"
    "v_mfma_f32_16x16x32_bf16 v[124:127], v[40:43], a[204:207], v[124:127]\n\t"
    "v_mfma_f32_16x16x32_bf16 v[52:55], v[236:239], a[44:47], v[52:55]\n\t"
    "v_smfmac_f32_16x16x64_bf16 a[212:215], a[208:211], v[36:43], v24\n\t"
    "v_mfma_f32_16x16x32_bf16 v[64:67], a[80:83], a[32:35], v[52:55]\n\t"
    "s_waitcnt vmcnt(8)\n\t"
    "ds_read_b128 v[240:243], v196 offset:4096\n\t"
    "ds_read_b128 v[244:247], v197 offset:4096\n\t"
    "ds_read_b128 v[248:251], v198 offset:4096\n\t"
    "ds_read_b128 v[252:255], v199 offset:4096\n\t"
    "v_cvt_pk_bf16_f32 v36, v80, v81\n\t"
    "v_mfma_f32_16x16x32_bf16 v[64:67], a[84:87], a[36:39], v[64:67]\n\t"
    "s_add_u32 m0, s60, 16384\n\t"
    "v_cvt_pk_bf16_f32 v37, v82, v83\n\t"
    "v_mfma_f32_16x16x32_bf16 v[64:67], a[88:91], a[40:43], v[64:67]\n\t"
    "buffer_load_dwordx4 v20, s[44:47], s73 offen offset:0 nt lds\n\t"
    "v_cvt_pk_bf16_f32 v38, v84, v85\n\t"
    "v_mfma_f32_16x16x32_bf16 v[64:67], a[92:95], a[44:47], v[64:67]\n\t"
    "buffer_load_dwordx4 v22, s[44:47], s73 offen offset:1024 nt lds\n\t"
    "v_cvt_pk_bf16_f32 v39, v86, v87\n\t"
    "v_mfma_f32_16x16x32_bf16 v[68:71], a[96:99], a[32:35], v[52:55]\n\t"
    "buffer_load_dwordx4 v20, s[44:47], s73 offen offset:2048 nt lds\n\t"
    "v_cvt_pk_bf16_f32 v40, v88, v89\n\t"
    "s_waitcnt lgkmcnt(3)\n\t"
    "v_lshlrev_b32 v200, 16, v240\n\t"
    "v_and_b32 v201, s69, v240\n\t"
    "v_lshlrev_b32 v202, 16, v241\n\t"
    "v_and_b32 v203, s69, v241\n\t"
    "v_mfma_f32_16x16x32_bf16 v[68:71], a[100:103], a[36:39], v[68:71]\n\t"
    "buffer_load_dwordx4 v22, s[44:47], s73 offen offset:3072 nt lds\n\t"
    "v_cvt_pk_bf16_f32 v41, v90, v91\n\t"
    "v_pk_mul_f32 v[200:201], v[200:201], s[66:67]\n\t"
    "v_pk_mul_f32 v[202:203], v[202:203], s[66:67]\n\t"
    "v_cvt_pk_bf16_f32 v240, v200, v201\n\t"
    "v_mfma_f32_16x16x32_bf16 v[68:71], a[104:107], a[40:43], v[68:71]\n\t"
    "v_cvt_pk_bf16_f32 v42, v92, v93\n\t"
    "v_cvt_pk_bf16_f32 v241, v202, v203\n\t"
    "v_accvgpr_write_b32 a48, v240\n\t"
    "v_accvgpr_write_b32 a49, v241\n\t"
    "v_mfma_f32_16x16x32_bf16 v[68:71], a[108:111], a[44:47], v[68:71]\n\t"
    "v_cvt_pk_bf16_f32 v43, v94, v95\n\t"
    "v_lshlrev_b32 v204, 16, v242\n\t"
    "v_and_b32 v205, s69, v242\n\t"
    "v_lshlrev_b32 v206, 16, v243\n\t"
    "v_and_b32 v207, s69, v243\n\t"
    "v_mfma_f32_16x16x32_bf16 v[72:75], a[112:115], a[32:35], v[52:55]\n\t"
    "v_pk_mul_f32 v[204:205], v[204:205], s[66:67]\n\t"
    "v_pk_mul_f32 v[206:207], v[206:207], s[66:67]\n\t"
    "v_cvt_pk_bf16_f32 v242, v204, v205\n\t"
    "v_exp_f32 v64, v64\n\t"
    "v_mfma_f32_16x16x32_bf16 v[72:75], a[116:119], a[36:39], v[72:75]\n\t"
    "v_cvt_pk_bf16_f32 v243, v206, v207\n\t"
    "v_accvgpr_write_b32 a50, v242\n\t"
    "v_accvgpr_write_b32 a51, v243\n\t"
    "v_exp_f32 v65, v65\n\t"
    "v_mfma_f32_16x16x32_bf16 v[72:75], a[120:123], a[40:43], v[72:75]\n\t"
    "s_waitcnt lgkmcnt(0)\n\t"
    "v_lshlrev_b32 v200, 16, v244\n\t"
    "v_and_b32 v201, s69, v244\n\t"
    "v_lshlrev_b32 v202, 16, v245\n\t"
    "v_and_b32 v203, s69, v245\n\t"
    "v_exp_f32 v66, v66\n\t"
    "v_mfma_f32_16x16x32_bf16 v[72:75], a[124:127], a[44:47], v[72:75]\n\t"
    "v_pk_mul_f32 v[200:201], v[200:201], s[66:67]\n\t"
    "v_pk_mul_f32 v[202:203], v[202:203], s[66:67]\n\t"
    "v_cvt_pk_bf16_f32 v244, v200, v201\n\t"
    "v_exp_f32 v67, v67\n\t"
    "v_mfma_f32_16x16x32_bf16 v[76:79], a[128:131], a[32:35], v[52:55]\n\t"
    "v_cvt_pk_bf16_f32 v245, v202, v203\n\t"
    "v_accvgpr_write_b32 a52, v244\n\t"
    "v_accvgpr_write_b32 a53, v245\n\t"
    "v_exp_f32 v68, v68\n\t"
    "v_mfma_f32_16x16x32_bf16 v[76:79], a[132:135], a[36:39], v[76:79]\n\t"
    "v_lshlrev_b32 v204, 16, v246\n\t"
    "v_and_b32 v205, s69, v246\n\t"
    "v_lshlrev_b32 v206, 16, v247\n\t"
    "v_and_b32 v207, s69, v247\n\t"
    "v_exp_f32 v69, v69\n\t"
    "v_mfma_f32_16x16x32_bf16 v[76:79], a[136:139], a[40:43], v[76:79]\n\t"
    "v_pk_mul_f32 v[204:205], v[204:205], s[66:67]\n\t"
    "v_pk_mul_f32 v[206:207], v[206:207], s[66:67]\n\t"
    "v_cvt_pk_bf16_f32 v246, v204, v205\n\t"
    "v_exp_f32 v70, v70\n\t"
    "v_mfma_f32_16x16x32_bf16 v[76:79], a[140:143], a[44:47], v[76:79]\n\t"
    "v_cvt_pk_bf16_f32 v247, v206, v207\n\t"
    "v_accvgpr_write_b32 a54, v246\n\t"
    "v_accvgpr_write_b32 a55, v247\n\t"
    "v_exp_f32 v71, v71\n\t"
    "v_mfma_f32_16x16x32_bf16 v[128:131], v[36:39], a[144:147], 0\n\t"
    "v_lshlrev_b32 v200, 16, v248\n\t"
    "v_and_b32 v201, s69, v248\n\t"
    "v_lshlrev_b32 v202, 16, v249\n\t"
    "v_and_b32 v203, s69, v249\n\t"
    "v_exp_f32 v72, v72\n\t"
    "v_mfma_f32_16x16x32_bf16 v[128:131], v[40:43], a[148:151], v[128:131]\n\t"
    "v_pk_mul_f32 v[200:201], v[200:201], s[66:67]\n\t"
    "v_pk_mul_f32 v[202:203], v[202:203], s[66:67]\n\t"
    "v_cvt_pk_bf16_f32 v248, v200, v201\n\t"
    "v_exp_f32 v73, v73\n\t"
    "v_mfma_f32_16x16x32_bf16 v[132:135], v[36:39], a[152:155], 0\n\t"
    "v_cvt_pk_bf16_f32 v249, v202, v203\n\t"
    "v_accvgpr_write_b32 a56, v248\n\t"
    "v_accvgpr_write_b32 a57, v249\n\t"
    "v_exp_f32 v74, v74\n\t"
    "v_mfma_f32_16x16x32_bf16 v[132:135], v[40:43], a[156:159], v[132:135]\n\t"
    "v_lshlrev_b32 v204, 16, v250\n\t"
    "v_and_b32 v205, s69, v250\n\t"
    "v_lshlrev_b32 v206, 16, v251\n\t"
    "v_and_b32 v207, s69, v251\n\t"
    "v_exp_f32 v75, v75\n\t"
    "v_mfma_f32_16x16x32_bf16 v[136:139], v[36:39], a[160:163], 0\n\t"
    "v_pk_mul_f32 v[204:205], v[204:205], s[66:67]\n\t"
    "v_pk_mul_f32 v[206:207], v[206:207], s[66:67]\n\t"
    "v_cvt_pk_bf16_f32 v250, v204, v205\n\t"
    "v_exp_f32 v76, v76\n\t"
    "v_mfma_f32_16x16x32_bf16 v[136:139], v[40:43], a[164:167], v[136:139]\n\t"
    "v_cvt_pk_bf16_f32 v251, v206, v207\n\t"
    "v_accvgpr_write_b32 a58, v250\n\t"
    "v_accvgpr_write_b32 a59, v251\n\t"
    "v_exp_f32 v77, v77\n\t"
    "v_mfma_f32_16x16x32_bf16 v[140:143], v[36:39], a[168:171], 0\n\t"
    "v_lshlrev_b32 v200, 16, v252\n\t"
    "v_and_b32 v201, s69, v252\n\t"
    "v_lshlrev_b32 v202, 16, v253\n\t"
    "v_and_b32 v203, s69, v253\n\t"
    "v_exp_f32 v78, v78\n\t"
    "v_mfma_f32_16x16x32_bf16 v[140:143], v[40:43], a[172:175], v[140:143]\n\t"
    "v_pk_mul_f32 v[200:201], v[200:201], s[66:67]\n\t"
    "v_pk_mul_f32 v[202:203], v[202:203], s[66:67]\n\t"
    "v_cvt_pk_bf16_f32 v252, v200, v201\n\t"
    "v_exp_f32 v79, v79\n\t"
    "v_mfma_f32_16x16x32_bf16 v[144:147], v[36:39], a[176:179], 0\n\t"
    "v_cvt_pk_bf16_f32 v253, v202, v203\n\t"
    "v_accvgpr_write_b32 a60, v252\n\t"
    "v_accvgpr_write_b32 a61, v253\n\t"
    "v_mfma_f32_16x16x32_bf16 v[144:147], v[40:43], a[180:183], v[144:147]\n\t"
    "v_lshlrev_b32 v204, 16, v254\n\t"
    "v_and_b32 v205, s69, v254\n\t"
    "v_lshlrev_b32 v206, 16, v255\n\t"
    "v_and_b32 v207, s69, v255\n\t"
    "v_mfma_f32_16x16x32_bf16 v[148:151], v[36:39], a[184:187], 0\n\t"
    "v_pk_mul_f32 v[204:205], v[204:205], s[66:67]\n\t"
    "v_pk_mul_f32 v[206:207], v[206:207], s[66:67]\n\t"
    "v_cvt_pk_bf16_f32 v254, v204, v205\n\t"
    "v_mfma_f32_16x16x32_bf16 v[148:151], v[40:43], a[188:191], v[148:151]\n\t"
    "v_cvt_pk_bf16_f32 v255, v206, v207\n\t"
    "v_accvgpr_write_b32 a62, v254\n\t"
    "v_accvgpr_write_b32 a63, v255\n\t"
    "v_mfma_f32_16x16x32_bf16 v[152:155], v[36:39], a[192:195], 0\n\t"
    "v_mfma_f32_16x16x32_bf16 v[56:59], v[224:227], a[48:51], -4.0\n\t"
    "v_mfma_f32_16x16x32_bf16 v[152:155], v[40:43], a[196:199], v[152:155]\n\t"
    "v_mfma_f32_16x16x32_bf16 v[56:59], v[228:231], a[52:55], v[56:59]\n\t"
    "v_mfma_f32_16x16x32_bf16 v[156:159], v[36:39], a[200:203], 0\n\t"
    "v_mfma_f32_16x16x32_bf16 v[56:59], v[232:235], a[56:59], v[56:59]\n\t"
    "v_mfma_f32_16x16x32_bf16 v[156:159], v[40:43], a[204:207], v[156:159]\n\t"
    "v_mfma_f32_16x16x32_bf16 v[56:59], v[236:239], a[60:63], v[56:59]\n\t"
    "v_smfmac_f32_16x16x64_bf16 a[216:219], a[208:211], v[36:43], v24\n\t"
    "v_mfma_f32_16x16x32_bf16 v[80:83], a[80:83], a[48:51], v[56:59]\n\t"
    "s_waitcnt vmcnt(8)\n\t"
    "ds_read_b128 v[240:243], v192 offset:0\n\t"
    "ds_read_b128 v[244:247], v193 offset:0\n\t"
    "ds_read_b128 v[248:251], v194 offset:0\n\t"
    "ds_read_b128 v[252:255], v195 offset:0\n\t"
    "v_cvt_pk_bf16_f32 v36, v64, v65\n\t"
    "v_mfma_f32_16x16x32_bf16 v[80:83], a[84:87], a[52:55], v[80:83]\n\t"
    "s_add_u32 m0, s59, 32768\n\t"
    "v_cvt_pk_bf16_f32 v37, v66, v67\n\t"
    "v_mfma_f32_16x16x32_bf16 v[80:83], a[88:91], a[56:59], v[80:83]\n\t"
    "buffer_load_dwordx4 v20, s[40:43], s74 offen offset:0 nt lds\n\t"
    "v_cvt_pk_bf16_f32 v38, v68, v69\n\t"
    "v_mfma_f32_16x16x32_bf16 v[80:83], a[92:95], a[60:63], v[80:83]\n\t"
    "buffer_load_dwordx4 v21, s[40:43], s74 offen offset:1024 nt lds\n\t"
    "v_cvt_pk_bf16_f32 v39, v70, v71\n\t"
    "v_mfma_f32_16x16x32_bf16 v[84:87], a[96:99], a[48:51], v[56:59]\n\t"
    "buffer_load_dwordx4 v22, s[40:43], s74 offen offset:2048 nt lds\n\t"
    "v_cvt_pk_bf16_f32 v40, v72, v73\n\t"
    "s_waitcnt lgkmcnt(3)\n\t"
    "v_lshlrev_b32 v200, 16, v240\n\t"
    "v_and_b32 v201, s69, v240\n\t"
    "v_lshlrev_b32 v202, 16, v241\n\t"
    "v_and_b32 v203, s69, v241\n\t"
    "v_mfma_f32_16x16x32_bf16 v[84:87], a[100:103], a[52:55], v[84:87]\n\t"
    "buffer_load_dwordx4 v23, s[40:43], s74 offen offset:3072 nt lds\n\t"
    "v_cvt_pk_bf16_f32 v41, v74, v75\n\t"
    "v_pk_mul_f32 v[200:201], v[200:201], s[66:67]\n\t"
    "v_pk_mul_f32 v[202:203], v[202:203], s[66:67]\n\t"
    "v_cvt_pk_bf16_f32 v240, v200, v201\n\t"
    "v_mfma_f32_16x16x32_bf16 v[84:87], a[104:107], a[56:59], v[84:87]\n\t"
    "v_cvt_pk_bf16_f32 v42, v76, v77\n\t"
    "v_cvt_pk_bf16_f32 v241, v202, v203\n\t"
    "v_accvgpr_write_b32 a64, v240\n\t"
    "v_accvgpr_write_b32 a65, v241\n\t"
    "v_mfma_f32_16x16x32_bf16 v[84:87], a[108:111], a[60:63], v[84:87]\n\t"
    "v_cvt_pk_bf16_f32 v43, v78, v79\n\t"
    "v_lshlrev_b32 v204, 16, v242\n\t"
    "v_and_b32 v205, s69, v242\n\t"
    "v_lshlrev_b32 v206, 16, v243\n\t"
    "v_and_b32 v207, s69, v243\n\t"
    "v_mfma_f32_16x16x32_bf16 v[88:91], a[112:115], a[48:51], v[56:59]\n\t"
    "v_pk_mul_f32 v[204:205], v[204:205], s[66:67]\n\t"
    "v_pk_mul_f32 v[206:207], v[206:207], s[66:67]\n\t"
    "v_cvt_pk_bf16_f32 v242, v204, v205\n\t"
    "v_exp_f32 v80, v80\n\t"
    "v_mfma_f32_16x16x32_bf16 v[88:91], a[116:119], a[52:55], v[88:91]\n\t"
    "v_cvt_pk_bf16_f32 v243, v206, v207\n\t"
    "v_accvgpr_write_b32 a66, v242\n\t"
    "v_accvgpr_write_b32 a67, v243\n\t"
    "v_exp_f32 v81, v81\n\t"
    "v_mfma_f32_16x16x32_bf16 v[88:91], a[120:123], a[56:59], v[88:91]\n\t"
    "s_waitcnt lgkmcnt(0)\n\t"
    "v_lshlrev_b32 v200, 16, v244\n\t"
    "v_and_b32 v201, s69, v244\n\t"
    "v_lshlrev_b32 v202, 16, v245\n\t"
    "v_and_b32 v203, s69, v245\n\t"
    "v_exp_f32 v82, v82\n\t"
    "v_mfma_f32_16x16x32_bf16 v[88:91], a[124:127], a[60:63], v[88:91]\n\t"
    "v_pk_mul_f32 v[200:201], v[200:201], s[66:67]\n\t"
    "v_pk_mul_f32 v[202:203], v[202:203], s[66:67]\n\t"
    "v_cvt_pk_bf16_f32 v244, v200, v201\n\t"
    "v_exp_f32 v83, v83\n\t"
    "v_mfma_f32_16x16x32_bf16 v[92:95], a[128:131], a[48:51], v[56:59]\n\t"
    "v_cvt_pk_bf16_f32 v245, v202, v203\n\t"
    "v_accvgpr_write_b32 a68, v244\n\t"
    "v_accvgpr_write_b32 a69, v245\n\t"
    "v_exp_f32 v84, v84\n\t"
    "v_mfma_f32_16x16x32_bf16 v[92:95], a[132:135], a[52:55], v[92:95]\n\t"
    "v_lshlrev_b32 v204, 16, v246\n\t"
    "v_and_b32 v205, s69, v246\n\t"
    "v_lshlrev_b32 v206, 16, v247\n\t"
    "v_and_b32 v207, s69, v247\n\t"
    "v_exp_f32 v85, v85\n\t"
    "v_mfma_f32_16x16x32_bf16 v[92:95], a[136:139], a[56:59], v[92:95]\n\t"
    "v_pk_mul_f32 v[204:205], v[204:205], s[66:67]\n\t"
    "v_pk_mul_f32 v[206:207], v[206:207], s[66:67]\n\t"
    "v_cvt_pk_bf16_f32 v246, v204, v205\n\t"
    "v_exp_f32 v86, v86\n\t"
    "v_mfma_f32_16x16x32_bf16 v[92:95], a[140:143], a[60:63], v[92:95]\n\t"
    "v_cvt_pk_bf16_f32 v247, v206, v207\n\t"
    "v_accvgpr_write_b32 a70, v246\n\t"
    "v_accvgpr_write_b32 a71, v247\n\t"
    "v_exp_f32 v87, v87\n\t"
    "v_mfma_f32_16x16x32_bf16 v[160:163], v[36:39], a[144:147], 0\n\t"
    "s_add_u32 m0, s60, 32768\n\t"
    "v_lshlrev_b32 v200, 16, v248\n\t"
    "v_and_b32 v201, s69, v248\n\t"
    "v_lshlrev_b32 v202, 16, v249\n\t"
    "v_and_b32 v203, s69, v249\n\t"
    "v_exp_f32 v88, v88\n\t"
    "v_mfma_f32_16x16x32_bf16 v[160:163], v[40:43], a[148:151], v[160:163]\n\t"
    "buffer_load_dwordx4 v20, s[44:47], s74 offen offset:0 nt lds\n\t"
    "v_pk_mul_f32 v[200:201], v[200:201], s[66:67]\n\t"
    "v_pk_mul_f32 v[202:203], v[202:203], s[66:67]\n\t"
    "v_cvt_pk_bf16_f32 v248, v200, v201\n\t"
    "v_exp_f32 v89, v89\n\t"
    "v_mfma_f32_16x16x32_bf16 v[164:167], v[36:39], a[152:155], 0\n\t"
    "buffer_load_dwordx4 v22, s[44:47], s74 offen offset:1024 nt lds\n\t"
    "v_cvt_pk_bf16_f32 v249, v202, v203\n\t"
    "v_accvgpr_write_b32 a72, v248\n\t"
    "v_accvgpr_write_b32 a73, v249\n\t"
    "v_exp_f32 v90, v90\n\t"
    "v_mfma_f32_16x16x32_bf16 v[164:167], v[40:43], a[156:159], v[164:167]\n\t"
    "buffer_load_dwordx4 v20, s[44:47], s74 offen offset:2048 nt lds\n\t"
    "v_lshlrev_b32 v204, 16, v250\n\t"
    "v_and_b32 v205, s69, v250\n\t"
    "v_lshlrev_b32 v206, 16, v251\n\t"
    "v_and_b32 v207, s69, v251\n\t"
    "v_exp_f32 v91, v91\n\t"
    "v_mfma_f32_16x16x32_bf16 v[168:171], v[36:39], a[160:163], 0\n\t"
    "buffer_load_dwordx4 v22, s[44:47], s74 offen offset:3072 nt lds\n\t"
    "v_pk_mul_f32 v[204:205], v[204:205], s[66:67]\n\t"
    "v_pk_mul_f32 v[206:207], v[206:207], s[66:67]\n\t"
    "v_cvt_pk_bf16_f32 v250, v204, v205\n\t"
    "v_exp_f32 v92, v92\n\t"
    "v_mfma_f32_16x16x32_bf16 v[168:171], v[40:43], a[164:167], v[168:171]\n\t"
    "v_cvt_pk_bf16_f32 v251, v206, v207\n\t"
    "v_accvgpr_write_b32 a74, v250\n\t"
    "v_accvgpr_write_b32 a75, v251\n\t"
    "v_exp_f32 v93, v93\n\t"
    "v_mfma_f32_16x16x32_bf16 v[172:175], v[36:39], a[168:171], 0\n\t"
    "v_lshlrev_b32 v200, 16, v252\n\t"
    "v_and_b32 v201, s69, v252\n\t"
    "v_lshlrev_b32 v202, 16, v253\n\t"
    "v_and_b32 v203, s69, v253\n\t"
    "v_exp_f32 v94, v94\n\t"
    "v_mfma_f32_16x16x32_bf16 v[172:175], v[40:43], a[172:175], v[172:175]\n\t"
    "v_pk_mul_f32 v[200:201], v[200:201], s[66:67]\n\t"
    "v_pk_mul_f32 v[202:203], v[202:203], s[66:67]\n\t"
    "v_cvt_pk_bf16_f32 v252, v200, v201\n\t"
    "v_exp_f32 v95, v95\n\t"
    "v_mfma_f32_16x16x32_bf16 v[176:179], v[36:39], a[176:179], 0\n\t"
    "v_cvt_pk_bf16_f32 v253, v202, v203\n\t"
    "v_accvgpr_write_b32 a76, v252\n\t"
    "v_accvgpr_write_b32 a77, v253\n\t"
    "v_mfma_f32_16x16x32_bf16 v[176:179], v[40:43], a[180:183], v[176:179]\n\t"
    "v_lshlrev_b32 v204, 16, v254\n\t"
    "v_and_b32 v205, s69, v254\n\t"
    "v_lshlrev_b32 v206, 16, v255\n\t"
    "v_and_b32 v207, s69, v255\n\t"
    "v_mfma_f32_16x16x32_bf16 v[180:183], v[36:39], a[184:187], 0\n\t"
    "v_pk_mul_f32 v[204:205], v[204:205], s[66:67]\n\t"
    "v_pk_mul_f32 v[206:207], v[206:207], s[66:67]\n\t"
    "v_cvt_pk_bf16_f32 v254, v204, v205\n\t"
    "v_mfma_f32_16x16x32_bf16 v[180:183], v[40:43], a[188:191], v[180:183]\n\t"
    "v_cvt_pk_bf16_f32 v255, v206, v207\n\t"
    "v_accvgpr_write_b32 a78, v254\n\t"
    "v_accvgpr_write_b32 a79, v255\n\t"
    "v_mfma_f32_16x16x32_bf16 v[184:187], v[36:39], a[192:195], 0\n\t"
    "v_mfma_f32_16x16x32_bf16 v[60:63], v[224:227], a[64:67], -4.0\n\t"
    "v_mfma_f32_16x16x32_bf16 v[184:187], v[40:43], a[196:199], v[184:187]\n\t"
    "v_mfma_f32_16x16x32_bf16 v[60:63], v[228:231], a[68:71], v[60:63]\n\t"
    "v_mfma_f32_16x16x32_bf16 v[188:191], v[36:39], a[200:203], 0\n\t"
    "v_mfma_f32_16x16x32_bf16 v[60:63], v[232:235], a[72:75], v[60:63]\n\t"
    "v_mfma_f32_16x16x32_bf16 v[188:191], v[40:43], a[204:207], v[188:191]\n\t"
    "v_mfma_f32_16x16x32_bf16 v[60:63], v[236:239], a[76:79], v[60:63]\n\t"
    "v_smfmac_f32_16x16x64_bf16 a[220:223], a[208:211], v[36:43], v24\n\t"
    "s_waitcnt vmcnt(8)\n\t"
    "s_barrier\n\t"
    "v_mfma_f32_16x16x32_bf16 v[64:67], a[80:83], a[64:67], v[60:63]\n\t"
    "ds_read_b128 a[80:83], v8 offset:16384\n\t"
    "v_cvt_pk_bf16_f32 v36, v80, v81\n\t"
    "v_mfma_f32_16x16x32_bf16 v[64:67], a[84:87], a[68:71], v[64:67]\n\t"
    "ds_read_b128 a[84:87], v9 offset:16384\n\t"
    "v_cvt_pk_bf16_f32 v37, v82, v83\n\t"
    "v_mfma_f32_16x16x32_bf16 v[64:67], a[88:91], a[72:75], v[64:67]\n\t"
    "ds_read_b128 a[88:91], v10 offset:16384\n\t"
    "v_cvt_pk_bf16_f32 v38, v84, v85\n\t"
    "v_mfma_f32_16x16x32_bf16 v[64:67], a[92:95], a[76:79], v[64:67]\n\t"
    "ds_read_b128 a[92:95], v11 offset:16384\n\t"
    "v_cvt_pk_bf16_f32 v39, v86, v87\n\t"
    "v_mfma_f32_16x16x32_bf16 v[68:71], a[96:99], a[64:67], v[60:63]\n\t"
    "ds_read_b128 a[96:99], v8 offset:20480\n\t"
    "v_cvt_pk_bf16_f32 v40, v88, v89\n\t"
    "v_mov_b32 v224, 0\n\t"
    "v_mov_b32 v225, 0\n\t"
    "v_mfma_f32_16x16x32_bf16 v[68:71], a[100:103], a[68:71], v[68:71]\n\t"
    "ds_read_b128 a[100:103], v9 offset:20480\n\t"
    "v_cvt_pk_bf16_f32 v41, v90, v91\n\t"
    "v_mov_b32 v226, 0\n\t"
    "v_mfma_f32_16x16x32_bf16 v[68:71], a[104:107], a[72:75], v[68:71]\n\t"
    "ds_read_b128 a[104:107], v10 offset:20480\n\t"
    "v_cvt_pk_bf16_f32 v42, v92, v93\n\t"
    "v_mov_b32 v227, 0\n\t"
    "v_mfma_f32_16x16x32_bf16 v[68:71], a[108:111], a[76:79], v[68:71]\n\t"
    "ds_read_b128 a[108:111], v11 offset:20480\n\t"
    "v_cvt_pk_bf16_f32 v43, v94, v95\n\t"
    "v_mov_b32 v228, 0\n\t"
    "v_mov_b32 v229, 0\n\t"
    "v_mfma_f32_16x16x32_bf16 v[72:75], a[112:115], a[64:67], v[60:63]\n\t"
    "ds_read_b64_tr_b16 a[240:241], v18 offset:16384\n\t"
    "v_mov_b32 v230, 0\n\t"
    "v_exp_f32 v64, v64\n\t"
    "v_mfma_f32_16x16x32_bf16 v[72:75], a[116:119], a[68:71], v[72:75]\n\t"
    "ds_read_b64_tr_b16 a[242:243], v18 offset:20480\n\t"
    "v_mov_b32 v231, 0\n\t"
    "v_exp_f32 v65, v65\n\t"
    "v_mfma_f32_16x16x32_bf16 v[72:75], a[120:123], a[72:75], v[72:75]\n\t"
    "ds_read_b64_tr_b16 a[244:245], v18 offset:24576\n\t"
    "v_mov_b32 v232, 0\n\t"
    "v_mov_b32 v233, 0\n\t"
    "v_exp_f32 v66, v66\n\t"
    "v_mfma_f32_16x16x32_bf16 v[72:75], a[124:127], a[76:79], v[72:75]\n\t"
    "ds_read_b64_tr_b16 a[246:247], v18 offset:28672\n\t"
    "v_mov_b32 v234, 0\n\t"
    "v_exp_f32 v67, v67\n\t"
    "v_mfma_f32_16x16x32_bf16 v[76:79], a[128:131], a[64:67], v[60:63]\n\t"
    "ds_read_b64_tr_b16 a[248:249], v19 offset:16384\n\t"
    "v_mov_b32 v235, 0\n\t"
    "v_exp_f32 v68, v68\n\t"
    "v_mfma_f32_16x16x32_bf16 v[76:79], a[132:135], a[68:71], v[76:79]\n\t"
    "ds_read_b64_tr_b16 a[250:251], v19 offset:20480\n\t"
    "v_mov_b32 v236, 0\n\t"
    "v_mov_b32 v237, 0\n\t"
    "v_exp_f32 v69, v69\n\t"
    "v_mfma_f32_16x16x32_bf16 v[76:79], a[136:139], a[72:75], v[76:79]\n\t"
    "ds_read_b64_tr_b16 a[252:253], v19 offset:24576\n\t"
    "v_mov_b32 v238, 0\n\t"
    "v_exp_f32 v70, v70\n\t"
    "v_mfma_f32_16x16x32_bf16 v[76:79], a[140:143], a[76:79], v[76:79]\n\t"
    "ds_read_b64_tr_b16 a[254:255], v19 offset:28672\n\t"
    "v_mov_b32 v239, 0\n\t"
    "v_exp_f32 v71, v71\n\t"
    "v_mfma_f32_16x16x32_bf16 v[192:195], v[36:39], a[144:147], 0\n\t"
    "v_mov_b32 v240, 0\n\t"
    "v_mov_b32 v241, 0\n\t"
    "v_exp_f32 v72, v72\n\t"
    "v_mfma_f32_16x16x32_bf16 v[192:195], v[40:43], a[148:151], v[192:195]\n\t"
    "v_mov_b32 v242, 0\n\t"
    "v_exp_f32 v73, v73\n\t"
    "v_mfma_f32_16x16x32_bf16 v[196:199], v[36:39], a[152:155], 0\n\t"
    "v_mov_b32 v243, 0\n\t"
    "v_exp_f32 v74, v74\n\t"
    "v_mfma_f32_16x16x32_bf16 v[196:199], v[40:43], a[156:159], v[196:199]\n\t"
    "v_mov_b32 v244, 0\n\t"
    "v_mov_b32 v245, 0\n\t"
    "v_exp_f32 v75, v75\n\t"
    "v_mfma_f32_16x16x32_bf16 v[200:203], v[36:39], a[160:163], 0\n\t"
    "v_mov_b32 v246, 0\n\t"
    "v_exp_f32 v76, v76\n\t"
    "v_mfma_f32_16x16x32_bf16 v[200:203], v[40:43], a[164:167], v[200:203]\n\t"
    "v_mov_b32 v247, 0\n\t"
    "v_exp_f32 v77, v77\n\t"
    "v_mfma_f32_16x16x32_bf16 v[204:207], v[36:39], a[168:171], 0\n\t"
    "v_mov_b32 v248, 0\n\t"
    "v_mov_b32 v249, 0\n\t"
    "v_exp_f32 v78, v78\n\t"
    "v_mfma_f32_16x16x32_bf16 v[204:207], v[40:43], a[172:175], v[204:207]\n\t"
    "v_mov_b32 v250, 0\n\t"
    "v_exp_f32 v79, v79\n\t"
    "v_mfma_f32_16x16x32_bf16 v[208:211], v[36:39], a[176:179], 0\n\t"
    "ds_read_b128 a[112:115], v8 offset:24576\n\t"
    "v_mov_b32 v251, 0\n\t"
    "v_mfma_f32_16x16x32_bf16 v[208:211], v[40:43], a[180:183], v[208:211]\n\t"
    "ds_read_b128 a[116:119], v9 offset:24576\n\t"
    "v_mov_b32 v252, 0\n\t"
    "v_mov_b32 v253, 0\n\t"
    "v_mfma_f32_16x16x32_bf16 v[212:215], v[36:39], a[184:187], 0\n\t"
    "ds_read_b128 a[120:123], v10 offset:24576\n\t"
    "v_mov_b32 v254, 0\n\t"
    "v_mfma_f32_16x16x32_bf16 v[212:215], v[40:43], a[188:191], v[212:215]\n\t"
    "ds_read_b128 a[124:127], v11 offset:24576\n\t"
    "v_mov_b32 v255, 0\n\t"
    "v_mfma_f32_16x16x32_bf16 v[216:219], v[36:39], a[192:195], 0\n\t"
    "ds_read_b128 a[128:131], v8 offset:28672\n\t"
    "v_mfma_f32_16x16x32_bf16 v[216:219], v[40:43], a[196:199], v[216:219]\n\t"
    "ds_read_b128 a[132:135], v9 offset:28672\n\t"
    "v_mfma_f32_16x16x32_bf16 v[220:223], v[36:39], a[200:203], 0\n\t"
    "ds_read_b128 a[136:139], v10 offset:28672\n\t"
    "v_mfma_f32_16x16x32_bf16 v[220:223], v[40:43], a[204:207], v[220:223]\n\t"
    "ds_read_b128 a[140:143], v11 offset:28672\n\t"
    "v_smfmac_f32_16x16x64_bf16 a[224:227], a[208:211], v[36:43], v24\n\t"
    "s_waitcnt lgkmcnt(7)\n\t"
    "v_mfma_f32_16x16x32_bf16 v[80:83], a[80:83], a[0:3], v[44:47]\n\t"
    "v_cvt_pk_bf16_f32 v36, v64, v65\n\t"
    "v_mfma_f32_16x16x32_bf16 v[80:83], a[84:87], a[4:7], v[80:83]\n\t"
    "ds_write_b128 v34, a[64:67] offset:0\n\t"
    "v_cvt_pk_bf16_f32 v37, v66, v67\n\t"
    "v_mfma_f32_16x16x32_bf16 v[80:83], a[88:91], a[8:11], v[80:83]\n\t"
    "ds_write_b128 v34, a[68:71] offset:1024\n\t"
    "v_cvt_pk_bf16_f32 v38, v68, v69\n\t"
    "v_mfma_f32_16x16x32_bf16 v[80:83], a[92:95], a[12:15], v[80:83]\n\t"
    "ds_write_b128 v34, a[72:75] offset:2048\n\t"
    "v_cvt_pk_bf16_f32 v39, v70, v71\n\t"
    "v_mfma_f32_16x16x32_bf16 v[84:87], a[96:99], a[0:3], v[44:47]\n\t"
    "ds_write_b128 v34, a[76:79] offset:3072\n\t"
    "v_cvt_pk_bf16_f32 v40, v72, v73\n\t"
    "v_mfma_f32_16x16x32_bf16 v[84:87], a[100:103], a[4:7], v[84:87]\n\t"
    "v_cvt_pk_bf16_f32 v41, v74, v75\n\t"
    "v_mfma_f32_16x16x32_bf16 v[84:87], a[104:107], a[8:11], v[84:87]\n\t"
    "v_cvt_pk_bf16_f32 v42, v76, v77\n\t"
    "v_mfma_f32_16x16x32_bf16 v[84:87], a[108:111], a[12:15], v[84:87]\n\t"
    "ds_write_b128 v34, v[60:63] offset:4096\n\t"
    "v_cvt_pk_bf16_f32 v43, v78, v79\n\t"
    "; TILE 1\n\t"
    "v_mfma_f32_16x16x32_bf16 v[88:91], a[112:115], a[0:3], v[44:47]\n\t"
    "s_waitcnt lgkmcnt(5)\n\t"
    "v_exp_f32 v80, v80\n\t"
    "v_mfma_f32_16x16x32_bf16 v[88:91], a[116:119], a[4:7], v[88:91]\n\t"
    "v_exp_f32 v81, v81\n\t"
    "v_mfma_f32_16x16x32_bf16 v[88:91], a[120:123], a[8:11], v[88:91]\n\t"
    "v_exp_f32 v82, v82\n\t"
    "v_mfma_f32_16x16x32_bf16 v[88:91], a[124:127], a[12:15], v[88:91]\n\t"
    "v_exp_f32 v83, v83\n\t"
    "v_mfma_f32_16x16x32_bf16 v[92:95], a[128:131], a[0:3], v[44:47]\n\t"
    "v_exp_f32 v84, v84\n\t"
    "v_mfma_f32_16x16x32_bf16 v[92:95], a[132:135], a[4:7], v[92:95]\n\t"
    "v_exp_f32 v85, v85\n\t"
    "v_mfma_f32_16x16x32_bf16 v[92:95], a[136:139], a[8:11], v[92:95]\n\t"
    "v_exp_f32 v86, v86\n\t"
    "v_mfma_f32_16x16x32_bf16 v[92:95], a[140:143], a[12:15], v[92:95]\n\t"
    "v_exp_f32 v87, v87\n\t"
    "v_mfma_f32_16x16x32_bf16 v[224:227], v[36:39], a[144:147], v[224:227]\n\t"
    "v_exp_f32 v88, v88\n\t"
    "v_mfma_f32_16x16x32_bf16 v[224:227], v[40:43], a[148:151], v[224:227]\n\t"
    "v_exp_f32 v89, v89\n\t"
    "v_mfma_f32_16x16x32_bf16 v[228:231], v[36:39], a[152:155], v[228:231]\n\t"
    "v_exp_f32 v90, v90\n\t"
    "v_mfma_f32_16x16x32_bf16 v[228:231], v[40:43], a[156:159], v[228:231]\n\t"
    "v_exp_f32 v91, v91\n\t"
    "v_mfma_f32_16x16x32_bf16 v[232:235], v[36:39], a[160:163], v[232:235]\n\t"
    "v_exp_f32 v92, v92\n\t"
    "v_mfma_f32_16x16x32_bf16 v[232:235], v[40:43], a[164:167], v[232:235]\n\t"
    "v_exp_f32 v93, v93\n\t"
    "v_mfma_f32_16x16x32_bf16 v[236:239], v[36:39], a[168:171], v[236:239]\n\t"
    "v_exp_f32 v94, v94\n\t"
    "v_mfma_f32_16x16x32_bf16 v[236:239], v[40:43], a[172:175], v[236:239]\n\t"
    "v_exp_f32 v95, v95\n\t"
    "v_mfma_f32_16x16x32_bf16 v[240:243], v[36:39], a[176:179], v[240:243]\n\t"
    "ds_read_b64_tr_b16 a[144:145], v12 offset:16384\n\t"
    "ds_read_b64_tr_b16 a[146:147], v12 offset:20480\n\t"
    "v_add_u32 v35, 1, v35\n\t"
    "v_mfma_f32_16x16x32_bf16 v[240:243], v[40:43], a[180:183], v[240:243]\n\t"
    "ds_write_b32 v27, v35 offset:6144\n\t"
    "ds_read_b64_tr_b16 a[148:149], v12 offset:24576\n\t"
    "ds_read_b64_tr_b16 a[150:151], v12 offset:28672\n\t"
    "v_mfma_f32_16x16x32_bf16 v[244:247], v[36:39], a[184:187], v[244:247]\n\t"
    "ds_read_b64_tr_b16 a[152:153], v13 offset:16384\n\t"
    "ds_read_b64_tr_b16 a[154:155], v13 offset:20480\n\t"
    "v_mfma_f32_16x16x32_bf16 v[244:247], v[40:43], a[188:191], v[244:247]\n\t"
    "ds_read_b64_tr_b16 a[156:157], v13 offset:24576\n\t"
    "ds_read_b64_tr_b16 a[158:159], v13 offset:28672\n\t"
    "v_mfma_f32_16x16x32_bf16 v[248:251], v[36:39], a[192:195], v[248:251]\n\t"
    "ds_read_b64_tr_b16 a[160:161], v14 offset:16384\n\t"
    "ds_read_b64_tr_b16 a[162:163], v14 offset:20480\n\t"
    "v_mfma_f32_16x16x32_bf16 v[248:251], v[40:43], a[196:199], v[248:251]\n\t"
    "ds_read_b64_tr_b16 a[164:165], v14 offset:24576\n\t"
    "ds_read_b64_tr_b16 a[166:167], v14 offset:28672\n\t"
    "v_mfma_f32_16x16x32_bf16 v[252:255], v[36:39], a[200:203], v[252:255]\n\t"
    "ds_read_b64_tr_b16 a[168:169], v15 offset:16384\n\t"
    "ds_read_b64_tr_b16 a[170:171], v15 offset:20480\n\t"
    "v_mfma_f32_16x16x32_bf16 v[252:255], v[40:43], a[204:207], v[252:255]\n\t"
    "ds_read_b64_tr_b16 a[172:173], v15 offset:24576\n\t"
    "ds_read_b64_tr_b16 a[174:175], v15 offset:28672\n\t"
    "v_smfmac_f32_16x16x64_bf16 a[228:231], a[208:211], v[36:43], v24\n\t"
    "v_mfma_f32_16x16x32_bf16 v[64:67], a[80:83], a[16:19], v[48:51]\n\t"
    "ds_read_b64_tr_b16 a[176:177], v16 offset:16384\n\t"
    "v_cvt_pk_bf16_f32 v36, v80, v81\n\t"
    "v_mfma_f32_16x16x32_bf16 v[64:67], a[84:87], a[20:23], v[64:67]\n\t"
    "ds_read_b64_tr_b16 a[178:179], v16 offset:20480\n\t"
    "v_cvt_pk_bf16_f32 v37, v82, v83\n\t"
    "v_mfma_f32_16x16x32_bf16 v[64:67], a[88:91], a[24:27], v[64:67]\n\t"
    "ds_read_b64_tr_b16 a[180:181], v16 offset:24576\n\t"
    "v_cvt_pk_bf16_f32 v38, v84, v85\n\t"
    "v_mfma_f32_16x16x32_bf16 v[64:67], a[92:95], a[28:31], v[64:67]\n\t"
    "ds_read_b64_tr_b16 a[182:183], v16 offset:28672\n\t"
    "v_cvt_pk_bf16_f32 v39, v86, v87\n\t"
    "v_mfma_f32_16x16x32_bf16 v[68:71], a[96:99], a[16:19], v[48:51]\n\t"
    "ds_read_b64_tr_b16 a[184:185], v17 offset:16384\n\t"
    "v_cvt_pk_bf16_f32 v40, v88, v89\n\t"
    "v_mfma_f32_16x16x32_bf16 v[68:71], a[100:103], a[20:23], v[68:71]\n\t"
    "ds_read_b64_tr_b16 a[186:187], v17 offset:20480\n\t"
    "v_cvt_pk_bf16_f32 v41, v90, v91\n\t"
    "v_mfma_f32_16x16x32_bf16 v[68:71], a[104:107], a[24:27], v[68:71]\n\t"
    "ds_read_b64_tr_b16 a[188:189], v17 offset:24576\n\t"
    "v_cvt_pk_bf16_f32 v42, v92, v93\n\t"
    "v_mfma_f32_16x16x32_bf16 v[68:71], a[108:111], a[28:31], v[68:71]\n\t"
    "ds_read_b64_tr_b16 a[190:191], v17 offset:28672\n\t"
    "v_cvt_pk_bf16_f32 v43, v94, v95\n\t"
    "v_mfma_f32_16x16x32_bf16 v[72:75], a[112:115], a[16:19], v[48:51]\n\t"
    "v_exp_f32 v64, v64\n\t"
    "v_mfma_f32_16x16x32_bf16 v[72:75], a[116:119], a[20:23], v[72:75]\n\t"
    "v_exp_f32 v65, v65\n\t"
    "v_mfma_f32_16x16x32_bf16 v[72:75], a[120:123], a[24:27], v[72:75]\n\t"
    "v_exp_f32 v66, v66\n\t"
    "v_mfma_f32_16x16x32_bf16 v[72:75], a[124:127], a[28:31], v[72:75]\n\t"
    "v_exp_f32 v67, v67\n\t"
    "v_mfma_f32_16x16x32_bf16 v[76:79], a[128:131], a[16:19], v[48:51]\n\t"
    "v_exp_f32 v68, v68\n\t"
    "v_mfma_f32_16x16x32_bf16 v[76:79], a[132:135], a[20:23], v[76:79]\n\t"
    "v_exp_f32 v69, v69\n\t"
    "v_mfma_f32_16x16x32_bf16 v[76:79], a[136:139], a[24:27], v[76:79]\n\t"
    "v_exp_f32 v70, v70\n\t"
    "v_mfma_f32_16x16x32_bf16 v[76:79], a[140:143], a[28:31], v[76:79]\n\t"
    "s_waitcnt lgkmcnt(0)\n\t"
    "v_exp_f32 v71, v71\n\t"
    "v_mfma_f32_16x16x32_bf16 v[96:99], v[36:39], a[144:147], v[96:99]\n\t"
    "v_exp_f32 v72, v72\n\t"
    "v_mfma_f32_16x16x32_bf16 v[96:99], v[40:43], a[148:151], v[96:99]\n\t"
    "v_exp_f32 v73, v73\n\t"
    "v_mfma_f32_16x16x32_bf16 v[100:103], v[36:39], a[152:155], v[100:103]\n\t"
    "v_exp_f32 v74, v74\n\t"
    "v_mfma_f32_16x16x32_bf16 v[100:103], v[40:43], a[156:159], v[100:103]\n\t"
    "v_exp_f32 v75, v75\n\t"
    "v_mfma_f32_16x16x32_bf16 v[104:107], v[36:39], a[160:163], v[104:107]\n\t"
    "v_exp_f32 v76, v76\n\t"
    "v_mfma_f32_16x16x32_bf16 v[104:107], v[40:43], a[164:167], v[104:107]\n\t"
    "v_exp_f32 v77, v77\n\t"
    "v_mfma_f32_16x16x32_bf16 v[108:111], v[36:39], a[168:171], v[108:111]\n\t"
    "v_exp_f32 v78, v78\n\t"
    "v_mfma_f32_16x16x32_bf16 v[108:111], v[40:43], a[172:175], v[108:111]\n\t"
    "v_exp_f32 v79, v79\n\t"
    "v_mfma_f32_16x16x32_bf16 v[112:115], v[36:39], a[176:179], v[112:115]\n\t"
    "v_mfma_f32_16x16x32_bf16 v[112:115], v[40:43], a[180:183], v[112:115]\n\t"
    "v_mfma_f32_16x16x32_bf16 v[116:119], v[36:39], a[184:187], v[116:119]\n\t"
    "v_mfma_f32_16x16x32_bf16 v[116:119], v[40:43], a[188:191], v[116:119]\n\t"
    "v_mfma_f32_16x16x32_bf16 v[120:123], v[36:39], a[240:243], v[120:123]\n\t"
    "v_mfma_f32_16x16x32_bf16 v[120:123], v[40:43], a[244:247], v[120:123]\n\t"
    "v_mfma_f32_16x16x32_bf16 v[124:127], v[36:39], a[248:251], v[124:127]\n\t"
    "v_mfma_f32_16x16x32_bf16 v[124:127], v[40:43], a[252:255], v[124:127]\n\t"
    "ds_read_b32 v33, v27 offset:3072\n\t"
    "v_smfmac_f32_16x16x64_bf16 a[212:215], a[208:211], v[36:43], v24\n\t"
    "s_waitcnt vmcnt(0)\n\t"
    "s_barrier\n\t"
    "v_mfma_f32_16x16x32_bf16 v[80:83], a[80:83], a[32:35], v[52:55]\n\t"
    "v_cvt_pk_bf16_f32 v36, v64, v65\n\t"
    "v_mfma_f32_16x16x32_bf16 v[80:83], a[84:87], a[36:39], v[80:83]\n\t"
    "v_cvt_pk_bf16_f32 v37, v66, v67\n\t"
    "v_mfma_f32_16x16x32_bf16 v[80:83], a[88:91], a[40:43], v[80:83]\n\t"
    "v_cvt_pk_bf16_f32 v38, v68, v69\n\t"
    "v_mfma_f32_16x16x32_bf16 v[80:83], a[92:95], a[44:47], v[80:83]\n\t"
    "v_cvt_pk_bf16_f32 v39, v70, v71\n\t"
    "v_mfma_f32_16x16x32_bf16 v[84:87], a[96:99], a[32:35], v[52:55]\n\t"
    "v_cvt_pk_bf16_f32 v40, v72, v73\n\t"
    "v_mfma_f32_16x16x32_bf16 v[84:87], a[100:103], a[36:39], v[84:87]\n\t"
    "v_cvt_pk_bf16_f32 v41, v74, v75\n\t"
    "v_mfma_f32_16x16x32_bf16 v[84:87], a[104:107], a[40:43], v[84:87]\n\t"
    "v_cvt_pk_bf16_f32 v42, v76, v77\n\t"
    "v_mfma_f32_16x16x32_bf16 v[84:87], a[108:111], a[44:47], v[84:87]\n\t"
    "v_cvt_pk_bf16_f32 v43, v78, v79\n\t"
    "v_mfma_f32_16x16x32_bf16 v[88:91], a[112:115], a[32:35], v[52:55]\n\t"
    "v_exp_f32 v80, v80\n\t"
    "v_mfma_f32_16x16x32_bf16 v[88:91], a[116:119], a[36:39], v[88:91]\n\t"
    "v_exp_f32 v81, v81\n\t"
    "v_mfma_f32_16x16x32_bf16 v[88:91], a[120:123], a[40:43], v[88:91]\n\t"
    "v_exp_f32 v82, v82\n\t"
    "v_mfma_f32_16x16x32_bf16 v[88:91], a[124:127], a[44:47], v[88:91]\n\t"
    "v_exp_f32 v83, v83\n\t"
    "v_mfma_f32_16x16x32_bf16 v[92:95], a[128:131], a[32:35], v[52:55]\n\t"
    "v_exp_f32 v84, v84\n\t"
    "v_mfma_f32_16x16x32_bf16 v[92:95], a[132:135], a[36:39], v[92:95]\n\t"
    "v_exp_f32 v85, v85\n\t"
    "v_mfma_f32_16x16x32_bf16 v[92:95], a[136:139], a[40:43], v[92:95]\n\t"
    "v_exp_f32 v86, v86\n\t"
    "v_mfma_f32_16x16x32_bf16 v[92:95], a[140:143], a[44:47], v[92:95]\n\t"
    "v_exp_f32 v87, v87\n\t"
    "v_mfma_f32_16x16x32_bf16 v[128:131], v[36:39], a[144:147], v[128:131]\n\t"
    "v_exp_f32 v88, v88\n\t"
    "v_mfma_f32_16x16x32_bf16 v[128:131], v[40:43], a[148:151], v[128:131]\n\t"
    "v_exp_f32 v89, v89\n\t"
    "v_mfma_f32_16x16x32_bf16 v[132:135], v[36:39], a[152:155], v[132:135]\n\t"
    "v_exp_f32 v90, v90\n\t"
    "v_mfma_f32_16x16x32_bf16 v[132:135], v[40:43], a[156:159], v[132:135]\n\t"
    "v_exp_f32 v91, v91\n\t"
    "v_mfma_f32_16x16x32_bf16 v[136:139], v[36:39], a[160:163], v[136:139]\n\t"
    "v_exp_f32 v92, v92\n\t"
    "v_mfma_f32_16x16x32_bf16 v[136:139], v[40:43], a[164:167], v[136:139]\n\t"
    "v_exp_f32 v93, v93\n\t"
    "v_mfma_f32_16x16x32_bf16 v[140:143], v[36:39], a[168:171], v[140:143]\n\t"
    "v_exp_f32 v94, v94\n\t"
    "v_mfma_f32_16x16x32_bf16 v[140:143], v[40:43], a[172:175], v[140:143]\n\t"
    "v_exp_f32 v95, v95\n\t"
    "v_mfma_f32_16x16x32_bf16 v[144:147], v[36:39], a[176:179], v[144:147]\n\t"
    "v_mfma_f32_16x16x32_bf16 v[144:147], v[40:43], a[180:183], v[144:147]\n\t"
    "v_mfma_f32_16x16x32_bf16 v[148:151], v[36:39], a[184:187], v[148:151]\n\t"
    "s_waitcnt lgkmcnt(0)\n\t"
    "v_cmp_gt_u32 vcc, v35, v33\n\t"
    "v_mfma_f32_16x16x32_bf16 v[148:151], v[40:43], a[188:191], v[148:151]\n\t"
    "v_mfma_f32_16x16x32_bf16 v[152:155], v[36:39], a[240:243], v[152:155]\n\t"
    "v_mfma_f32_16x16x32_bf16 v[152:155], v[40:43], a[244:247], v[152:155]\n\t"
    "v_mfma_f32_16x16x32_bf16 v[156:159], v[36:39], a[248:251], v[156:159]\n\t"
    "s_cbranch_vccnz SPIN5P1%=\n\t"
    "RET5P1%=:\n\t"
    "v_mfma_f32_16x16x32_bf16 v[156:159], v[40:43], a[252:255], v[156:159]\n\t"
    "v_smfmac_f32_16x16x64_bf16 a[216:219], a[208:211], v[36:43], v24\n\t"
    "v_mfma_f32_16x16x32_bf16 v[64:67], a[80:83], a[48:51], v[56:59]\n\t"
    "ds_read_b64_tr_b16 a[192:193], v18 offset:32768\n\t"
    "v_cvt_pk_bf16_f32 v36, v80, v81\n\t"
    "v_mfma_f32_16x16x32_bf16 v[64:67], a[84:87], a[52:55], v[64:67]\n\t"
    "ds_read_b64_tr_b16 a[194:195], v18 offset:36864\n\t"
    "v_cvt_pk_bf16_f32 v37, v82, v83\n\t"
    "v_mfma_f32_16x16x32_bf16 v[64:67], a[88:91], a[56:59], v[64:67]\n\t"
    "ds_read_b64_tr_b16 a[196:197], v18 offset:40960\n\t"
    "v_cvt_pk_bf16_f32 v38, v84, v85\n\t"
    "v_mfma_f32_16x16x32_bf16 v[64:67], a[92:95], a[60:63], v[64:67]\n\t"
    "ds_read_b64_tr_b16 a[198:199], v18 offset:45056\n\t"
    "v_cvt_pk_bf16_f32 v39, v86, v87\n\t"
    "v_mfma_f32_16x16x32_bf16 v[68:71], a[96:99], a[48:51], v[56:59]\n\t"
    "ds_read_b64_tr_b16 a[200:201], v19 offset:32768\n\t"
    "v_cvt_pk_bf16_f32 v40, v88, v89\n\t"
    "v_mfma_f32_16x16x32_bf16 v[68:71], a[100:103], a[52:55], v[68:71]\n\t"
    "ds_read_b64_tr_b16 a[202:203], v19 offset:36864\n\t"
    "v_cvt_pk_bf16_f32 v41, v90, v91\n\t"
    "v_mfma_f32_16x16x32_bf16 v[68:71], a[104:107], a[56:59], v[68:71]\n\t"
    "ds_read_b64_tr_b16 a[204:205], v19 offset:40960\n\t"
    "v_cvt_pk_bf16_f32 v42, v92, v93\n\t"
    "v_mfma_f32_16x16x32_bf16 v[68:71], a[108:111], a[60:63], v[68:71]\n\t"
    "ds_read_b64_tr_b16 a[206:207], v19 offset:45056\n\t"
    "v_cvt_pk_bf16_f32 v43, v94, v95\n\t"
    "v_mfma_f32_16x16x32_bf16 v[72:75], a[112:115], a[48:51], v[56:59]\n\t"
    "v_exp_f32 v64, v64\n\t"
    "v_mfma_f32_16x16x32_bf16 v[72:75], a[116:119], a[52:55], v[72:75]\n\t"
    "v_exp_f32 v65, v65\n\t"
    "v_mfma_f32_16x16x32_bf16 v[72:75], a[120:123], a[56:59], v[72:75]\n\t"
    "v_exp_f32 v66, v66\n\t"
    "v_mfma_f32_16x16x32_bf16 v[72:75], a[124:127], a[60:63], v[72:75]\n\t"
    "v_exp_f32 v67, v67\n\t"
    "v_mfma_f32_16x16x32_bf16 v[76:79], a[128:131], a[48:51], v[56:59]\n\t"
    "v_exp_f32 v68, v68\n\t"
    "v_mfma_f32_16x16x32_bf16 v[76:79], a[132:135], a[52:55], v[76:79]\n\t"
    "v_exp_f32 v69, v69\n\t"
    "v_mfma_f32_16x16x32_bf16 v[76:79], a[136:139], a[56:59], v[76:79]\n\t"
    "v_exp_f32 v70, v70\n\t"
    "v_mfma_f32_16x16x32_bf16 v[76:79], a[140:143], a[60:63], v[76:79]\n\t"
    "v_exp_f32 v71, v71\n\t"
    "v_mfma_f32_16x16x32_bf16 v[160:163], v[36:39], a[144:147], v[160:163]\n\t"
    "v_exp_f32 v72, v72\n\t"
    "v_mfma_f32_16x16x32_bf16 v[160:163], v[40:43], a[148:151], v[160:163]\n\t"
    "v_exp_f32 v73, v73\n\t"
    "v_mfma_f32_16x16x32_bf16 v[164:167], v[36:39], a[152:155], v[164:167]\n\t"
    "v_exp_f32 v74, v74\n\t"
    "v_mfma_f32_16x16x32_bf16 v[164:167], v[40:43], a[156:159], v[164:167]\n\t"
    "v_exp_f32 v75, v75\n\t"
    "v_mfma_f32_16x16x32_bf16 v[168:171], v[36:39], a[160:163], v[168:171]\n\t"
    "v_exp_f32 v76, v76\n\t"
    "v_mfma_f32_16x16x32_bf16 v[168:171], v[40:43], a[164:167], v[168:171]\n\t"
    "v_exp_f32 v77, v77\n\t"
    "v_mfma_f32_16x16x32_bf16 v[172:175], v[36:39], a[168:171], v[172:175]\n\t"
    "v_exp_f32 v78, v78\n\t"
    "v_mfma_f32_16x16x32_bf16 v[172:175], v[40:43], a[172:175], v[172:175]\n\t"
    "v_exp_f32 v79, v79\n\t"
    "v_mfma_f32_16x16x32_bf16 v[176:179], v[36:39], a[176:179], v[176:179]\n\t"
    "v_mfma_f32_16x16x32_bf16 v[176:179], v[40:43], a[180:183], v[176:179]\n\t"
    "v_mfma_f32_16x16x32_bf16 v[180:183], v[36:39], a[184:187], v[180:183]\n\t"
    "v_mfma_f32_16x16x32_bf16 v[180:183], v[40:43], a[188:191], v[180:183]\n\t"
    "v_mfma_f32_16x16x32_bf16 v[184:187], v[36:39], a[240:243], v[184:187]\n\t"
    "v_mfma_f32_16x16x32_bf16 v[184:187], v[40:43], a[244:247], v[184:187]\n\t"
    "v_mfma_f32_16x16x32_bf16 v[188:191], v[36:39], a[248:251], v[188:191]\n\t"
    "v_mfma_f32_16x16x32_bf16 v[188:191], v[40:43], a[252:255], v[188:191]\n\t"
    "v_smfmac_f32_16x16x64_bf16 a[220:223], a[208:211], v[36:43], v24\n\t"
    "v_mfma_f32_16x16x32_bf16 v[80:83], a[80:83], a[64:67], v[60:63]\n\t"
    "ds_read_b128 a[80:83], v8 offset:32768\n\t"
    "v_cvt_pk_bf16_f32 v36, v64, v65\n\t"
    "v_mfma_f32_16x16x32_bf16 v[80:83], a[84:87], a[68:71], v[80:83]\n\t"
    "ds_read_b128 a[84:87], v9 offset:32768\n\t"
    "v_cvt_pk_bf16_f32 v37, v66, v67\n\t"
    "v_mfma_f32_16x16x32_bf16 v[80:83], a[88:91], a[72:75], v[80:83]\n\t"
    "ds_read_b128 a[88:91], v10 offset:32768\n\t"
    "v_cvt_pk_bf16_f32 v38, v68, v69\n\t"
    "v_mfma_f32_16x16x32_bf16 v[80:83], a[92:95], a[76:79], v[80:83]\n\t"
    "ds_read_b128 a[92:95], v11 offset:32768\n\t"
    "v_cvt_pk_bf16_f32 v39, v70, v71\n\t"
    "v_mfma_f32_16x16x32_bf16 v[84:87], a[96:99], a[64:67], v[60:63]\n\t"
    "ds_read_b128 a[96:99], v8 offset:36864\n\t"
    "v_cvt_pk_bf16_f32 v40, v72, v73\n\t"
    "v_mfma_f32_16x16x32_bf16 v[84:87], a[100:103], a[68:71], v[84:87]\n\t"
    "ds_read_b128 a[100:103], v9 offset:36864\n\t"
    "v_cvt_pk_bf16_f32 v41, v74, v75\n\t"
    "v_mfma_f32_16x16x32_bf16 v[84:87], a[104:107], a[72:75], v[84:87]\n\t"
    "ds_read_b128 a[104:107], v10 offset:36864\n\t"
    "v_cvt_pk_bf16_f32 v42, v76, v77\n\t"
    "v_mfma_f32_16x16x32_bf16 v[84:87], a[108:111], a[76:79], v[84:87]\n\t"
    "ds_read_b128 a[108:111], v11 offset:36864\n\t"
    "v_cvt_pk_bf16_f32 v43, v78, v79\n\t"
    "v_mfma_f32_16x16x32_bf16 v[88:91], a[112:115], a[64:67], v[60:63]\n\t"
    "v_exp_f32 v80, v80\n\t"
    "v_mfma_f32_16x16x32_bf16 v[88:91], a[116:119], a[68:71], v[88:91]\n\t"
    "v_exp_f32 v81, v81\n\t"
    "v_mfma_f32_16x16x32_bf16 v[88:91], a[120:123], a[72:75], v[88:91]\n\t"
    "v_exp_f32 v82, v82\n\t"
    "v_mfma_f32_16x16x32_bf16 v[88:91], a[124:127], a[76:79], v[88:91]\n\t"
    "v_exp_f32 v83, v83\n\t"
    "v_mfma_f32_16x16x32_bf16 v[92:95], a[128:131], a[64:67], v[60:63]\n\t"
    "v_exp_f32 v84, v84\n\t"
    "v_mfma_f32_16x16x32_bf16 v[92:95], a[132:135], a[68:71], v[92:95]\n\t"
    "v_exp_f32 v85, v85\n\t"
    "v_mfma_f32_16x16x32_bf16 v[92:95], a[136:139], a[72:75], v[92:95]\n\t"
    "v_exp_f32 v86, v86\n\t"
    "v_mfma_f32_16x16x32_bf16 v[92:95], a[140:143], a[76:79], v[92:95]\n\t"
    "v_exp_f32 v87, v87\n\t"
    "v_mfma_f32_16x16x32_bf16 v[192:195], v[36:39], a[144:147], v[192:195]\n\t"
    "v_exp_f32 v88, v88\n\t"
    "v_mfma_f32_16x16x32_bf16 v[192:195], v[40:43], a[148:151], v[192:195]\n\t"
    "v_exp_f32 v89, v89\n\t"
    "v_mfma_f32_16x16x32_bf16 v[196:199], v[36:39], a[152:155], v[196:199]\n\t"
    "v_exp_f32 v90, v90\n\t"
    "v_mfma_f32_16x16x32_bf16 v[196:199], v[40:43], a[156:159], v[196:199]\n\t"
    "v_exp_f32 v91, v91\n\t"
    "v_mfma_f32_16x16x32_bf16 v[200:203], v[36:39], a[160:163], v[200:203]\n\t"
    "v_exp_f32 v92, v92\n\t"
    "v_mfma_f32_16x16x32_bf16 v[200:203], v[40:43], a[164:167], v[200:203]\n\t"
    "v_exp_f32 v93, v93\n\t"
    "v_mfma_f32_16x16x32_bf16 v[204:207], v[36:39], a[168:171], v[204:207]\n\t"
    "v_exp_f32 v94, v94\n\t"
    "v_mfma_f32_16x16x32_bf16 v[204:207], v[40:43], a[172:175], v[204:207]\n\t"
    "v_exp_f32 v95, v95\n\t"
    "v_mfma_f32_16x16x32_bf16 v[208:211], v[36:39], a[176:179], v[208:211]\n\t"
    "ds_read_b128 a[112:115], v8 offset:40960\n\t"
    "v_mfma_f32_16x16x32_bf16 v[208:211], v[40:43], a[180:183], v[208:211]\n\t"
    "ds_read_b128 a[116:119], v9 offset:40960\n\t"
    "v_mfma_f32_16x16x32_bf16 v[212:215], v[36:39], a[184:187], v[212:215]\n\t"
    "ds_read_b128 a[120:123], v10 offset:40960\n\t"
    "v_mfma_f32_16x16x32_bf16 v[212:215], v[40:43], a[188:191], v[212:215]\n\t"
    "ds_read_b128 a[124:127], v11 offset:40960\n\t"
    "v_mfma_f32_16x16x32_bf16 v[216:219], v[36:39], a[240:243], v[216:219]\n\t"
    "ds_read_b128 a[128:131], v8 offset:45056\n\t"
    "v_mfma_f32_16x16x32_bf16 v[216:219], v[40:43], a[244:247], v[216:219]\n\t"
    "ds_read_b128 a[132:135], v9 offset:45056\n\t"
    "v_mfma_f32_16x16x32_bf16 v[220:223], v[36:39], a[248:251], v[220:223]\n\t"
    "ds_read_b128 a[136:139], v10 offset:45056\n\t"
    "v_mfma_f32_16x16x32_bf16 v[220:223], v[40:43], a[252:255], v[220:223]\n\t"
    "ds_read_b128 a[140:143], v11 offset:45056\n\t"
    "v_smfmac_f32_16x16x64_bf16 a[224:227], a[208:211], v[36:43], v24\n\t"
    "s_waitcnt lgkmcnt(7)\n\t"
    "v_mfma_f32_16x16x32_bf16 v[64:67], a[80:83], a[0:3], v[44:47]\n\t"
    "v_cvt_pk_bf16_f32 v36, v80, v81\n\t"
    "v_mfma_f32_16x16x32_bf16 v[64:67], a[84:87], a[4:7], v[64:67]\n\t"
    "v_cvt_pk_bf16_f32 v37, v82, v83\n\t"
    "v_mfma_f32_16x16x32_bf16 v[64:67], a[88:91], a[8:11], v[64:67]\n\t"
    "v_cvt_pk_bf16_f32 v38, v84, v85\n\t"
    "v_mfma_f32_16x16x32_bf16 v[64:67], a[92:95], a[12:15], v[64:67]\n\t"
    "v_cvt_pk_bf16_f32 v39, v86, v87\n\t"
    "v_mfma_f32_16x16x32_bf16 v[68:71], a[96:99], a[0:3], v[44:47]\n\t"
    "v_cvt_pk_bf16_f32 v40, v88, v89\n\t"
    "v_mfma_f32_16x16x32_bf16 v[68:71], a[100:103], a[4:7], v[68:71]\n\t"
    "v_cvt_pk_bf16_f32 v41, v90, v91\n\t"
    "v_mfma_f32_16x16x32_bf16 v[68:71], a[104:107], a[8:11], v[68:71]\n\t"
    "v_cvt_pk_bf16_f32 v42, v92, v93\n\t"
    "v_mfma_f32_16x16x32_bf16 v[68:71], a[108:111], a[12:15], v[68:71]\n\t"
    "v_cvt_pk_bf16_f32 v43, v94, v95\n\t"
    "s_mov_b32 s57, 7\n\t"
    "LOOPA%=:\n\t"
    "; TILE 2\n\t"
    "v_mfma_f32_16x16x32_bf16 v[72:75], a[112:115], a[0:3], v[44:47]\n\t"
    "s_waitcnt lgkmcnt(0)\n\t"
    "v_exp_f32 v64, v64\n\t"
    "v_mfma_f32_16x16x32_bf16 v[72:75], a[116:119], a[4:7], v[72:75]\n\t"
    "v_exp_f32 v65, v65\n\t"
    "v_mfma_f32_16x16x32_bf16 v[72:75], a[120:123], a[8:11], v[72:75]\n\t"
    "v_exp_f32 v66, v66\n\t"
    "v_mfma_f32_16x16x32_bf16 v[72:75], a[124:127], a[12:15], v[72:75]\n\t"
    "v_exp_f32 v67, v67\n\t"
    "v_mfma_f32_16x16x32_bf16 v[76:79], a[128:131], a[0:3], v[44:47]\n\t"
    "v_exp_f32 v68, v68\n\t"
    "v_mfma_f32_16x16x32_bf16 v[76:79], a[132:135], a[4:7], v[76:79]\n\t"
    "v_exp_f32 v69, v69\n\t"
    "v_mfma_f32_16x16x32_bf16 v[76:79], a[136:139], a[8:11], v[76:79]\n\t"
    "v_exp_f32 v70, v70\n\t"
    "v_mfma_f32_16x16x32_bf16 v[76:79], a[140:143], a[12:15], v[76:79]\n\t"
    "v_exp_f32 v71, v71\n\t"
    "v_mfma_f32_16x16x32_bf16 v[224:227], v[36:39], a[144:147], v[224:227]\n\t"
    "v_exp_f32 v72, v72\n\t"
    "v_mfma_f32_16x16x32_bf16 v[224:227], v[40:43], a[148:151], v[224:227]\n\t"
    "v_exp_f32 v73, v73\n\t"
    "v_mfma_f32_16x16x32_bf16 v[228:231], v[36:39], a[152:155], v[228:231]\n\t"
    "v_exp_f32 v74, v74\n\t"
    "v_mfma_f32_16x16x32_bf16 v[228:231], v[40:43], a[156:159], v[228:231]\n\t"
    "v_exp_f32 v75, v75\n\t"
    "v_mfma_f32_16x16x32_bf16 v[232:235], v[36:39], a[160:163], v[232:235]\n\t"
    "v_exp_f32 v76, v76\n\t"
    "v_mfma_f32_16x16x32_bf16 v[232:235], v[40:43], a[164:167], v[232:235]\n\t"
    "v_exp_f32 v77, v77\n\t"
    "v_mfma_f32_16x16x32_bf16 v[236:239], v[36:39], a[168:171], v[236:239]\n\t"
    "v_exp_f32 v78, v78\n\t"
    "v_mfma_f32_16x16x32_bf16 v[236:239], v[40:43], a[172:175], v[236:239]\n\t"
    "v_exp_f32 v79, v79\n\t"
    "v_mfma_f32_16x16x32_bf16 v[240:243], v[36:39], a[176:179], v[240:243]\n\t"
    "ds_read_b64_tr_b16 a[144:145], v12 offset:32768\n\t"
    "ds_read_b64_tr_b16 a[146:147], v12 offset:36864\n\t"
    "v_mfma_f32_16x16x32_bf16 v[240:243], v[40:43], a[180:183], v[240:243]\n\t"
    "ds_read_b64_tr_b16 a[148:149], v12 offset:40960\n\t"
    "ds_read_b64_tr_b16 a[150:151], v12 offset:45056\n\t"
    "v_mfma_f32_16x16x32_bf16 v[244:247], v[36:39], a[184:187], v[244:247]\n\t"
    "ds_read_b64_tr_b16 a[152:153], v13 offset:32768\n\t"
    "ds_read_b64_tr_b16 a[154:155], v13 offset:36864\n\t"
    "v_mfma_f32_16x16x32_bf16 v[244:247], v[40:43], a[188:191], v[244:247]\n\t"
    "ds_read_b64_tr_b16 a[156:157], v13 offset:40960\n\t"
    "ds_read_b64_tr_b16 a[158:159], v13 offset:45056\n\t"
    "v_mfma_f32_16x16x32_bf16 v[248:251], v[36:39], a[240:243], v[248:251]\n\t"
    "ds_read_b64_tr_b16 a[160:161], v14 offset:32768\n\t"
    "ds_read_b64_tr_b16 a[162:163], v14 offset:36864\n\t"
    "v_mfma_f32_16x16x32_bf16 v[248:251], v[40:43], a[244:247], v[248:251]\n\t"
    "ds_read_b64_tr_b16 a[164:165], v14 offset:40960\n\t"
    "ds_read_b64_tr_b16 a[166:167], v14 offset:45056\n\t"
    "v_mfma_f32_16x16x32_bf16 v[252:255], v[36:39], a[248:251], v[252:255]\n\t"
    "ds_read_b64_tr_b16 a[168:169], v15 offset:32768\n\t"
    "ds_read_b64_tr_b16 a[170:171], v15 offset:36864\n\t"
    "v_mfma_f32_16x16x32_bf16 v[252:255], v[40:43], a[252:255], v[252:255]\n\t"
    "ds_read_b64_tr_b16 a[172:173], v15 offset:40960\n\t"
    "ds_read_b64_tr_b16 a[174:175], v15 offset:45056\n\t"
    "v_smfmac_f32_16x16x64_bf16 a[228:231], a[208:211], v[36:43], v24\n\t"
    "v_mfma_f32_16x16x32_bf16 v[80:83], a[80:83], a[16:19], v[48:51]\n\t"
    "ds_read_b64_tr_b16 a[176:177], v16 offset:32768\n\t"
    "v_cvt_pk_bf16_f32 v36, v64, v65\n\t"
    "v_mfma_f32_16x16x32_bf16 v[80:83], a[84:87], a[20:23], v[80:83]\n\t"
    "ds_read_b64_tr_b16 a[178:179], v16 offset:36864\n\t"
    "v_cvt_pk_bf16_f32 v37, v66, v67\n\t"
    "v_mfma_f32_16x16x32_bf16 v[80:83], a[88:91], a[24:27], v[80:83]\n\t"
    "ds_read_b64_tr_b16 a[180:181], v16 offset:40960\n\t"
    "v_cvt_pk_bf16_f32 v38, v68, v69\n\t"
    "v_mfma_f32_16x16x32_bf16 v[80:83], a[92:95], a[28:31], v[80:83]\n\t"
    "ds_read_b64_tr_b16 a[182:183], v16 offset:45056\n\t"
    "v_cvt_pk_bf16_f32 v39, v70, v71\n\t"
    "v_mfma_f32_16x16x32_bf16 v[84:87], a[96:99], a[16:19], v[48:51]\n\t"
    "ds_read_b64_tr_b16 a[184:185], v17 offset:32768\n\t"
    "v_cvt_pk_bf16_f32 v40, v72, v73\n\t"
    "v_mfma_f32_16x16x32_bf16 v[84:87], a[100:103], a[20:23], v[84:87]\n\t"
    "ds_read_b64_tr_b16 a[186:187], v17 offset:36864\n\t"
    "v_cvt_pk_bf16_f32 v41, v74, v75\n\t"
    "v_mfma_f32_16x16x32_bf16 v[84:87], a[104:107], a[24:27], v[84:87]\n\t"
    "ds_read_b64_tr_b16 a[188:189], v17 offset:40960\n\t"
    "v_cvt_pk_bf16_f32 v42, v76, v77\n\t"
    "v_mfma_f32_16x16x32_bf16 v[84:87], a[108:111], a[28:31], v[84:87]\n\t"
    "ds_read_b64_tr_b16 a[190:191], v17 offset:45056\n\t"
    "v_cvt_pk_bf16_f32 v43, v78, v79\n\t"
    "v_mfma_f32_16x16x32_bf16 v[88:91], a[112:115], a[16:19], v[48:51]\n\t"
    "v_exp_f32 v80, v80\n\t"
    "v_mfma_f32_16x16x32_bf16 v[88:91], a[116:119], a[20:23], v[88:91]\n\t"
    "v_exp_f32 v81, v81\n\t"
    "v_mfma_f32_16x16x32_bf16 v[88:91], a[120:123], a[24:27], v[88:91]\n\t"
    "v_exp_f32 v82, v82\n\t"
    "v_mfma_f32_16x16x32_bf16 v[88:91], a[124:127], a[28:31], v[88:91]\n\t"
    "v_exp_f32 v83, v83\n\t"
    "v_mfma_f32_16x16x32_bf16 v[92:95], a[128:131], a[16:19], v[48:51]\n\t"
    "v_exp_f32 v84, v84\n\t"
    "v_mfma_f32_16x16x32_bf16 v[92:95], a[132:135], a[20:23], v[92:95]\n\t"
    "v_exp_f32 v85, v85\n\t"
    "v_mfma_f32_16x16x32_bf16 v[92:95], a[136:139], a[24:27], v[92:95]\n\t"
    "v_exp_f32 v86, v86\n\t"
    "v_mfma_f32_16x16x32_bf16 v[92:95], a[140:143], a[28:31], v[92:95]\n\t"
    "s_waitcnt lgkmcnt(0)\n\t"
    "v_exp_f32 v87, v87\n\t"
    "v_mfma_f32_16x16x32_bf16 v[96:99], v[36:39], a[144:147], v[96:99]\n\t"
    "v_exp_f32 v88, v88\n\t"
    "v_mfma_f32_16x16x32_bf16 v[96:99], v[40:43], a[148:151], v[96:99]\n\t"
    "v_exp_f32 v89, v89\n\t"
    "v_mfma_f32_16x16x32_bf16 v[100:103], v[36:39], a[152:155], v[100:103]\n\t"
    "v_exp_f32 v90, v90\n\t"
    "v_mfma_f32_16x16x32_bf16 v[100:103], v[40:43], a[156:159], v[100:103]\n\t"
    "v_exp_f32 v91, v91\n\t"
    "v_mfma_f32_16x16x32_bf16 v[104:107], v[36:39], a[160:163], v[104:107]\n\t"
    "v_exp_f32 v92, v92\n\t"
    "v_mfma_f32_16x16x32_bf16 v[104:107], v[40:43], a[164:167], v[104:107]\n\t"
    "v_exp_f32 v93, v93\n\t"
    "v_mfma_f32_16x16x32_bf16 v[108:111], v[36:39], a[168:171], v[108:111]\n\t"
    "v_exp_f32 v94, v94\n\t"
    "v_mfma_f32_16x16x32_bf16 v[108:111], v[40:43], a[172:175], v[108:111]\n\t"
    "v_exp_f32 v95, v95\n\t"
    "v_mfma_f32_16x16x32_bf16 v[112:115], v[36:39], a[176:179], v[112:115]\n\t"
    "v_add_u32 v35, 1, v35\n\t"
    "v_mfma_f32_16x16x32_bf16 v[112:115], v[40:43], a[180:183], v[112:115]\n\t"
    "ds_write_b32 v27, v35 offset:6144\n\t"
    "v_mfma_f32_16x16x32_bf16 v[116:119], v[36:39], a[184:187], v[116:119]\n\t"
    "v_mfma_f32_16x16x32_bf16 v[116:119], v[40:43], a[188:191], v[116:119]\n\t"
    "v_mfma_f32_16x16x32_bf16 v[120:123], v[36:39], a[192:195], v[120:123]\n\t"
    "v_mfma_f32_16x16x32_bf16 v[120:123], v[40:43], a[196:199], v[120:123]\n\t"
    "v_mfma_f32_16x16x32_bf16 v[124:127], v[36:39], a[200:203], v[124:127]\n\t"
    "v_mfma_f32_16x16x32_bf16 v[124:127], v[40:43], a[204:207], v[124:127]\n\t"
    "ds_read_b32 v33, v27 offset:3072\n\t"
    "v_smfmac_f32_16x16x64_bf16 a[212:215], a[208:211], v[36:43], v24\n\t"
    "v_mfma_f32_16x16x32_bf16 v[64:67], a[80:83], a[32:35], v[52:55]\n\t"
    "v_cvt_pk_bf16_f32 v36, v80, v81\n\t"
    "v_mfma_f32_16x16x32_bf16 v[64:67], a[84:87], a[36:39], v[64:67]\n\t"
    "v_cvt_pk_bf16_f32 v37, v82, v83\n\t"
    "v_mfma_f32_16x16x32_bf16 v[64:67], a[88:91], a[40:43], v[64:67]\n\t"
    "v_cvt_pk_bf16_f32 v38, v84, v85\n\t"
    "v_mfma_f32_16x16x32_bf16 v[64:67], a[92:95], a[44:47], v[64:67]\n\t"
    "v_cvt_pk_bf16_f32 v39, v86, v87\n\t"
    "v_mfma_f32_16x16x32_bf16 v[68:71], a[96:99], a[32:35], v[52:55]\n\t"
    "v_cvt_pk_bf16_f32 v40, v88, v89\n\t"
    "v_mfma_f32_16x16x32_bf16 v[68:71], a[100:103], a[36:39], v[68:71]\n\t"
    "v_cvt_pk_bf16_f32 v41, v90, v91\n\t"
    "v_mfma_f32_16x16x32_bf16 v[68:71], a[104:107], a[40:43], v[68:71]\n\t"
    "v_cvt_pk_bf16_f32 v42, v92, v93\n\t"
    "v_mfma_f32_16x16x32_bf16 v[68:71], a[108:111], a[44:47], v[68:71]\n\t"
    "v_cvt_pk_bf16_f32 v43, v94, v95\n\t"
    "v_mfma_f32_16x16x32_bf16 v[72:75], a[112:115], a[32:35], v[52:55]\n\t"
    "v_exp_f32 v64, v64\n\t"
    "v_mfma_f32_16x16x32_bf16 v[72:75], a[116:119], a[36:39], v[72:75]\n\t"
    "v_exp_f32 v65, v65\n\t"
    "v_mfma_f32_16x16x32_bf16 v[72:75], a[120:123], a[40:43], v[72:75]\n\t"
    "v_exp_f32 v66, v66\n\t"
    "v_mfma_f32_16x16x32_bf16 v[72:75], a[124:127], a[44:47], v[72:75]\n\t"
    "v_exp_f32 v67, v67\n\t"
    "v_mfma_f32_16x16x32_bf16 v[76:79], a[128:131], a[32:35], v[52:55]\n\t"
    "v_exp_f32 v68, v68\n\t"
    "v_mfma_f32_16x16x32_bf16 v[76:79], a[132:135], a[36:39], v[76:79]\n\t"
    "v_exp_f32 v69, v69\n\t"
    "v_mfma_f32_16x16x32_bf16 v[76:79], a[136:139], a[40:43], v[76:79]\n\t"
    "v_exp_f32 v70, v70\n\t"
    "v_mfma_f32_16x16x32_bf16 v[76:79], a[140:143], a[44:47], v[76:79]\n\t"
    "v_exp_f32 v71, v71\n\t"
    "v_mfma_f32_16x16x32_bf16 v[128:131], v[36:39], a[144:147], v[128:131]\n\t"
    "v_exp_f32 v72, v72\n\t"
    "v_mfma_f32_16x16x32_bf16 v[128:131], v[40:43], a[148:151], v[128:131]\n\t"
    "v_exp_f32 v73, v73\n\t"
    "v_mfma_f32_16x16x32_bf16 v[132:135], v[36:39], a[152:155], v[132:135]\n\t"
    "v_exp_f32 v74, v74\n\t"
    "v_mfma_f32_16x16x32_bf16 v[132:135], v[40:43], a[156:159], v[132:135]\n\t"
    "v_exp_f32 v75, v75\n\t"
    "v_mfma_f32_16x16x32_bf16 v[136:139], v[36:39], a[160:163], v[136:139]\n\t"
    "v_exp_f32 v76, v76\n\t"
    "v_mfma_f32_16x16x32_bf16 v[136:139], v[40:43], a[164:167], v[136:139]\n\t"
    "v_exp_f32 v77, v77\n\t"
    "v_mfma_f32_16x16x32_bf16 v[140:143], v[36:39], a[168:171], v[140:143]\n\t"
    "v_exp_f32 v78, v78\n\t"
    "v_mfma_f32_16x16x32_bf16 v[140:143], v[40:43], a[172:175], v[140:143]\n\t"
    "v_exp_f32 v79, v79\n\t"
    "v_mfma_f32_16x16x32_bf16 v[144:147], v[36:39], a[176:179], v[144:147]\n\t"
    "v_mfma_f32_16x16x32_bf16 v[144:147], v[40:43], a[180:183], v[144:147]\n\t"
    "v_mfma_f32_16x16x32_bf16 v[148:151], v[36:39], a[184:187], v[148:151]\n\t"
    "s_waitcnt lgkmcnt(0)\n\t"
    "v_cmp_gt_u32 vcc, v35, v33\n\t"
    "v_mfma_f32_16x16x32_bf16 v[148:151], v[40:43], a[188:191], v[148:151]\n\t"
    "v_mfma_f32_16x16x32_bf16 v[152:155], v[36:39], a[192:195], v[152:155]\n\t"
    "v_mfma_f32_16x16x32_bf16 v[152:155], v[40:43], a[196:199], v[152:155]\n\t"
    "v_mfma_f32_16x16x32_bf16 v[156:159], v[36:39], a[200:203], v[156:159]\n\t"
    "s_cbranch_vccnz SPIN5L2%=\n\t"
    "RET5L2%=:\n\t"
    "v_mfma_f32_16x16x32_bf16 v[156:159], v[40:43], a[204:207], v[156:159]\n\t"
    "v_smfmac_f32_16x16x64_bf16 a[216:219], a[208:211], v[36:43], v24\n\t"
    "v_mfma_f32_16x16x32_bf16 v[80:83], a[80:83], a[48:51], v[56:59]\n\t"
    "ds_read_b64_tr_b16 a[240:241], v18 offset:49152\n\t"
    "v_cvt_pk_bf16_f32 v36, v64, v65\n\t"
    "v_mfma_f32_16x16x32_bf16 v[80:83], a[84:87], a[52:55], v[80:83]\n\t"
    "ds_read_b64_tr_b16 a[242:243], v18 offset:53248\n\t"
    "v_cvt_pk_bf16_f32 v37, v66, v67\n\t"
    "v_mfma_f32_16x16x32_bf16 v[80:83], a[88:91], a[56:59], v[80:83]\n\t"
    "ds_read_b64_tr_b16 a[244:245], v18 offset:57344\n\t"
    "v_cvt_pk_bf16_f32 v38, v68, v69\n\t"
    "v_mfma_f32_16x16x32_bf16 v[80:83], a[92:95], a[60:63], v[80:83]\n\t"
    "ds_read_b64_tr_b16 a[246:247], v18 offset:61440\n\t"
    "v_cvt_pk_bf16_f32 v39, v70, v71\n\t"
    "v_mfma_f32_16x16x32_bf16 v[84:87], a[96:99], a[48:51], v[56:59]\n\t"
    "ds_read_b64_tr_b16 a[248:249], v19 offset:49152\n\t"
    "v_cvt_pk_bf16_f32 v40, v72, v73\n\t"
    "v_mfma_f32_16x16x32_bf16 v[84:87], a[100:103], a[52:55], v[84:87]\n\t"
    "ds_read_b64_tr_b16 a[250:251], v19 offset:53248\n\t"
    "v_cvt_pk_bf16_f32 v41, v74, v75\n\t"
    "v_mfma_f32_16x16x32_bf16 v[84:87], a[104:107], a[56:59], v[84:87]\n\t"
    "ds_read_b64_tr_b16 a[252:253], v19 offset:57344\n\t"
    "v_cvt_pk_bf16_f32 v42, v76, v77\n\t"
    "v_mfma_f32_16x16x32_bf16 v[84:87], a[108:111], a[60:63], v[84:87]\n\t"
    "ds_read_b64_tr_b16 a[254:255], v19 offset:61440\n\t"
    "v_cvt_pk_bf16_f32 v43, v78, v79\n\t"
    "v_mfma_f32_16x16x32_bf16 v[88:91], a[112:115], a[48:51], v[56:59]\n\t"
    "v_exp_f32 v80, v80\n\t"
    "v_mfma_f32_16x16x32_bf16 v[88:91], a[116:119], a[52:55], v[88:91]\n\t"
    "v_exp_f32 v81, v81\n\t"
    "v_mfma_f32_16x16x32_bf16 v[88:91], a[120:123], a[56:59], v[88:91]\n\t"
    "v_exp_f32 v82, v82\n\t"
    "v_mfma_f32_16x16x32_bf16 v[88:91], a[124:127], a[60:63], v[88:91]\n\t"
    "v_exp_f32 v83, v83\n\t"
    "v_mfma_f32_16x16x32_bf16 v[92:95], a[128:131], a[48:51], v[56:59]\n\t"
    "v_exp_f32 v84, v84\n\t"
    "v_mfma_f32_16x16x32_bf16 v[92:95], a[132:135], a[52:55], v[92:95]\n\t"
    "v_exp_f32 v85, v85\n\t"
    "v_mfma_f32_16x16x32_bf16 v[92:95], a[136:139], a[56:59], v[92:95]\n\t"
    "v_exp_f32 v86, v86\n\t"
    "v_mfma_f32_16x16x32_bf16 v[92:95], a[140:143], a[60:63], v[92:95]\n\t"
    "v_exp_f32 v87, v87\n\t"
    "v_mfma_f32_16x16x32_bf16 v[160:163], v[36:39], a[144:147], v[160:163]\n\t"
    "v_exp_f32 v88, v88\n\t"
    "v_mfma_f32_16x16x32_bf16 v[160:163], v[40:43], a[148:151], v[160:163]\n\t"
    "v_exp_f32 v89, v89\n\t"
    "v_mfma_f32_16x16x32_bf16 v[164:167], v[36:39], a[152:155], v[164:167]\n\t"
    "v_exp_f32 v90, v90\n\t"
    "v_mfma_f32_16x16x32_bf16 v[164:167], v[40:43], a[156:159], v[164:167]\n\t"
    "v_exp_f32 v91, v91\n\t"
    "v_mfma_f32_16x16x32_bf16 v[168:171], v[36:39], a[160:163], v[168:171]\n\t"
    "v_exp_f32 v92, v92\n\t"
    "v_mfma_f32_16x16x32_bf16 v[168:171], v[40:43], a[164:167], v[168:171]\n\t"
    "v_exp_f32 v93, v93\n\t"
    "v_mfma_f32_16x16x32_bf16 v[172:175], v[36:39], a[168:171], v[172:175]\n\t"
    "v_exp_f32 v94, v94\n\t"
    "v_mfma_f32_16x16x32_bf16 v[172:175], v[40:43], a[172:175], v[172:175]\n\t"
    "v_exp_f32 v95, v95\n\t"
    "v_mfma_f32_16x16x32_bf16 v[176:179], v[36:39], a[176:179], v[176:179]\n\t"
    "v_mfma_f32_16x16x32_bf16 v[176:179], v[40:43], a[180:183], v[176:179]\n\t"
    "v_mfma_f32_16x16x32_bf16 v[180:183], v[36:39], a[184:187], v[180:183]\n\t"
    "v_mfma_f32_16x16x32_bf16 v[180:183], v[40:43], a[188:191], v[180:183]\n\t"
    "v_mfma_f32_16x16x32_bf16 v[184:187], v[36:39], a[192:195], v[184:187]\n\t"
    "v_mfma_f32_16x16x32_bf16 v[184:187], v[40:43], a[196:199], v[184:187]\n\t"
    "v_mfma_f32_16x16x32_bf16 v[188:191], v[36:39], a[200:203], v[188:191]\n\t"
    "v_mfma_f32_16x16x32_bf16 v[188:191], v[40:43], a[204:207], v[188:191]\n\t"
    "v_smfmac_f32_16x16x64_bf16 a[220:223], a[208:211], v[36:43], v24\n\t"
    "v_mfma_f32_16x16x32_bf16 v[68:71], a[96:99], a[64:67], v[60:63]\n\t"
    "ds_read_b128 a[80:83], v8 offset:49152\n\t"
    "v_cvt_pk_bf16_f32 v36, v80, v81\n\t"
    "v_mfma_f32_16x16x32_bf16 v[68:71], a[100:103], a[68:71], v[68:71]\n\t"
    "ds_read_b128 a[84:87], v9 offset:49152\n\t"
    "v_cvt_pk_bf16_f32 v37, v82, v83\n\t"
    "v_mfma_f32_16x16x32_bf16 v[68:71], a[104:107], a[72:75], v[68:71]\n\t"
    "ds_read_b128 a[88:91], v10 offset:49152\n\t"
    "v_cvt_pk_bf16_f32 v38, v84, v85\n\t"
    "v_mfma_f32_16x16x32_bf16 v[68:71], a[108:111], a[76:79], v[68:71]\n\t"
    "ds_read_b128 a[92:95], v11 offset:49152\n\t"
    "v_cvt_pk_bf16_f32 v39, v86, v87\n\t"
    "v_mfma_f32_16x16x32_bf16 v[72:75], a[112:115], a[64:67], v[60:63]\n\t"
    "ds_read_b128 a[96:99], v8 offset:53248\n\t"
    "v_cvt_pk_bf16_f32 v40, v88, v89\n\t"
    "v_mfma_f32_16x16x32_bf16 v[72:75], a[116:119], a[68:71], v[72:75]\n\t"
    "ds_read_b128 a[100:103], v9 offset:53248\n\t"
    "v_cvt_pk_bf16_f32 v41, v90, v91\n\t"
    "v_mfma_f32_16x16x32_bf16 v[72:75], a[120:123], a[72:75], v[72:75]\n\t"
    "ds_read_b128 a[104:107], v10 offset:53248\n\t"
    "v_cvt_pk_bf16_f32 v42, v92, v93\n\t"
    "v_mfma_f32_16x16x32_bf16 v[72:75], a[124:127], a[76:79], v[72:75]\n\t"
    "ds_read_b128 a[108:111], v11 offset:53248\n\t"
    "v_cvt_pk_bf16_f32 v43, v94, v95\n\t"
    "v_mfma_f32_16x16x32_bf16 v[76:79], a[128:131], a[64:67], v[60:63]\n\t"
    "v_exp_f32 v68, v68\n\t"
    "v_mfma_f32_16x16x32_bf16 v[76:79], a[132:135], a[68:71], v[76:79]\n\t"
    "v_exp_f32 v69, v69\n\t"
    "v_mfma_f32_16x16x32_bf16 v[76:79], a[136:139], a[72:75], v[76:79]\n\t"
    "v_exp_f32 v70, v70\n\t"
    "v_mfma_f32_16x16x32_bf16 v[76:79], a[140:143], a[76:79], v[76:79]\n\t"
    "v_exp_f32 v71, v71\n\t"
    "v_mfma_f32_16x16x32_bf16 v[192:195], v[36:39], a[144:147], v[192:195]\n\t"
    "v_exp_f32 v72, v72\n\t"
    "v_mfma_f32_16x16x32_bf16 v[192:195], v[40:43], a[148:151], v[192:195]\n\t"
    "v_exp_f32 v73, v73\n\t"
    "v_mfma_f32_16x16x32_bf16 v[196:199], v[36:39], a[152:155], v[196:199]\n\t"
    "v_exp_f32 v74, v74\n\t"
    "v_mfma_f32_16x16x32_bf16 v[196:199], v[40:43], a[156:159], v[196:199]\n\t"
    "v_exp_f32 v75, v75\n\t"
    "v_mfma_f32_16x16x32_bf16 v[200:203], v[36:39], a[160:163], v[200:203]\n\t"
    "v_exp_f32 v76, v76\n\t"
    "v_mfma_f32_16x16x32_bf16 v[200:203], v[40:43], a[164:167], v[200:203]\n\t"
    "v_exp_f32 v77, v77\n\t"
    "v_mfma_f32_16x16x32_bf16 v[204:207], v[36:39], a[168:171], v[204:207]\n\t"
    "v_exp_f32 v78, v78\n\t"
    "v_mfma_f32_16x16x32_bf16 v[204:207], v[40:43], a[172:175], v[204:207]\n\t"
    "v_exp_f32 v79, v79\n\t"
    "v_mfma_f32_16x16x32_bf16 v[208:211], v[36:39], a[176:179], v[208:211]\n\t"
    "ds_read_b128 a[112:115], v8 offset:57344\n\t"
    "v_mfma_f32_16x16x32_bf16 v[208:211], v[40:43], a[180:183], v[208:211]\n\t"
    "ds_read_b128 a[116:119], v9 offset:57344\n\t"
    "v_mfma_f32_16x16x32_bf16 v[212:215], v[36:39], a[184:187], v[212:215]\n\t"
    "ds_read_b128 a[120:123], v10 offset:57344\n\t"
    "v_mfma_f32_16x16x32_bf16 v[212:215], v[40:43], a[188:191], v[212:215]\n\t"
    "ds_read_b128 a[124:127], v11 offset:57344\n\t"
    "v_mfma_f32_16x16x32_bf16 v[216:219], v[36:39], a[192:195], v[216:219]\n\t"
    "ds_read_b128 a[128:131], v8 offset:61440\n\t"
    "v_mfma_f32_16x16x32_bf16 v[216:219], v[40:43], a[196:199], v[216:219]\n\t"
    "ds_read_b128 a[132:135], v9 offset:61440\n\t"
    "v_mfma_f32_16x16x32_bf16 v[220:223], v[36:39], a[200:203], v[220:223]\n\t"
    "ds_read_b128 a[136:139], v10 offset:61440\n\t"
    "v_mfma_f32_16x16x32_bf16 v[220:223], v[40:43], a[204:207], v[220:223]\n\t"
    "ds_read_b128 a[140:143], v11 offset:61440\n\t"
    "v_smfmac_f32_16x16x64_bf16 a[224:227], a[208:211], v[36:43], v24\n\t"
    "s_waitcnt lgkmcnt(7)\n\t"
    "v_mfma_f32_16x16x32_bf16 v[80:83], a[80:83], a[0:3], v[44:47]\n\t"
    "ds_read_b64 v[36:37], v27 offset:0\n\t"
    "v_mfma_f32_16x16x32_bf16 v[80:83], a[84:87], a[4:7], v[80:83]\n\t"
    "v_mfma_f32_16x16x32_bf16 v[80:83], a[88:91], a[8:11], v[80:83]\n\t"
    "v_cvt_pk_bf16_f32 v38, v68, v69\n\t"
    "v_mfma_f32_16x16x32_bf16 v[80:83], a[92:95], a[12:15], v[80:83]\n\t"
    "v_cvt_pk_bf16_f32 v39, v70, v71\n\t"
    "v_mfma_f32_16x16x32_bf16 v[84:87], a[96:99], a[0:3], v[44:47]\n\t"
    "v_cvt_pk_bf16_f32 v40, v72, v73\n\t"
    "v_mfma_f32_16x16x32_bf16 v[84:87], a[100:103], a[4:7], v[84:87]\n\t"
    "v_cvt_pk_bf16_f32 v41, v74, v75\n\t"
    "v_mfma_f32_16x16x32_bf16 v[84:87], a[104:107], a[8:11], v[84:87]\n\t"
    "v_cvt_pk_bf16_f32 v42, v76, v77\n\t"
    "v_mfma_f32_16x16x32_bf16 v[84:87], a[108:111], a[12:15], v[84:87]\n\t"
    "v_cvt_pk_bf16_f32 v43, v78, v79\n\t"
    "; TILE 3\n\t"
    "v_mfma_f32_16x16x32_bf16 v[88:91], a[112:115], a[0:3], v[44:47]\n\t"
    "s_waitcnt lgkmcnt(0)\n\t"
    "v_exp_f32 v80, v80\n\t"
    "v_mfma_f32_16x16x32_bf16 v[88:91], a[116:119], a[4:7], v[88:91]\n\t"
    "v_exp_f32 v81, v81\n\t"
    "v_mfma_f32_16x16x32_bf16 v[88:91], a[120:123], a[8:11], v[88:91]\n\t"
    "v_exp_f32 v82, v82\n\t"
    "v_mfma_f32_16x16x32_bf16 v[88:91], a[124:127], a[12:15], v[88:91]\n\t"
    "v_exp_f32 v83, v83\n\t"
    "v_mfma_f32_16x16x32_bf16 v[92:95], a[128:131], a[0:3], v[44:47]\n\t"
    "v_exp_f32 v84, v84\n\t"
    "v_mfma_f32_16x16x32_bf16 v[92:95], a[132:135], a[4:7], v[92:95]\n\t"
    "v_exp_f32 v85, v85\n\t"
    "v_mfma_f32_16x16x32_bf16 v[92:95], a[136:139], a[8:11], v[92:95]\n\t"
    "v_exp_f32 v86, v86\n\t"
    "v_mfma_f32_16x16x32_bf16 v[92:95], a[140:143], a[12:15], v[92:95]\n\t"
    "v_exp_f32 v87, v87\n\t"
    "v_mfma_f32_16x16x32_bf16 v[224:227], v[36:39], a[144:147], v[224:227]\n\t"
    "v_exp_f32 v88, v88\n\t"
    "v_mfma_f32_16x16x32_bf16 v[224:227], v[40:43], a[148:151], v[224:227]\n\t"
    "v_exp_f32 v89, v89\n\t"
    "v_mfma_f32_16x16x32_bf16 v[228:231], v[36:39], a[152:155], v[228:231]\n\t"
    "v_exp_f32 v90, v90\n\t"
    "v_mfma_f32_16x16x32_bf16 v[228:231], v[40:43], a[156:159], v[228:231]\n\t"
    "v_exp_f32 v91, v91\n\t"
    "v_mfma_f32_16x16x32_bf16 v[232:235], v[36:39], a[160:163], v[232:235]\n\t"
    "v_exp_f32 v92, v92\n\t"
    "v_mfma_f32_16x16x32_bf16 v[232:235], v[40:43], a[164:167], v[232:235]\n\t"
    "v_exp_f32 v93, v93\n\t"
    "v_mfma_f32_16x16x32_bf16 v[236:239], v[36:39], a[168:171], v[236:239]\n\t"
    "v_exp_f32 v94, v94\n\t"
    "v_mfma_f32_16x16x32_bf16 v[236:239], v[40:43], a[172:175], v[236:239]\n\t"
    "v_exp_f32 v95, v95\n\t"
    "v_mfma_f32_16x16x32_bf16 v[240:243], v[36:39], a[176:179], v[240:243]\n\t"
    "ds_read_b64_tr_b16 a[144:145], v12 offset:49152\n\t"
    "ds_read_b64_tr_b16 a[146:147], v12 offset:53248\n\t"
    "v_mfma_f32_16x16x32_bf16 v[240:243], v[40:43], a[180:183], v[240:243]\n\t"
    "ds_read_b64_tr_b16 a[148:149], v12 offset:57344\n\t"
    "ds_read_b64_tr_b16 a[150:151], v12 offset:61440\n\t"
    "v_mfma_f32_16x16x32_bf16 v[244:247], v[36:39], a[184:187], v[244:247]\n\t"
    "ds_read_b64_tr_b16 a[152:153], v13 offset:49152\n\t"
    "ds_read_b64_tr_b16 a[154:155], v13 offset:53248\n\t"
    "v_mfma_f32_16x16x32_bf16 v[244:247], v[40:43], a[188:191], v[244:247]\n\t"
    "ds_read_b64_tr_b16 a[156:157], v13 offset:57344\n\t"
    "ds_read_b64_tr_b16 a[158:159], v13 offset:61440\n\t"
    "v_mfma_f32_16x16x32_bf16 v[248:251], v[36:39], a[192:195], v[248:251]\n\t"
    "ds_read_b64_tr_b16 a[160:161], v14 offset:49152\n\t"
    "ds_read_b64_tr_b16 a[162:163], v14 offset:53248\n\t"
    "v_mfma_f32_16x16x32_bf16 v[248:251], v[40:43], a[196:199], v[248:251]\n\t"
    "ds_read_b64_tr_b16 a[164:165], v14 offset:57344\n\t"
    "ds_read_b64_tr_b16 a[166:167], v14 offset:61440\n\t"
    "v_mfma_f32_16x16x32_bf16 v[252:255], v[36:39], a[200:203], v[252:255]\n\t"
    "ds_read_b64_tr_b16 a[168:169], v15 offset:49152\n\t"
    "ds_read_b64_tr_b16 a[170:171], v15 offset:53248\n\t"
    "v_mfma_f32_16x16x32_bf16 v[252:255], v[40:43], a[204:207], v[252:255]\n\t"
    "ds_read_b64_tr_b16 a[172:173], v15 offset:57344\n\t"
    "ds_read_b64_tr_b16 a[174:175], v15 offset:61440\n\t"
    "v_smfmac_f32_16x16x64_bf16 a[228:231], a[208:211], v[36:43], v24\n\t"
    "v_mfma_f32_16x16x32_bf16 v[64:67], a[80:83], a[16:19], v[48:51]\n\t"
    "ds_read_b64_tr_b16 a[176:177], v16 offset:49152\n\t"
    "v_cvt_pk_bf16_f32 v36, v80, v81\n\t"
    "v_mfma_f32_16x16x32_bf16 v[64:67], a[84:87], a[20:23], v[64:67]\n\t"
    "ds_read_b64_tr_b16 a[178:179], v16 offset:53248\n\t"
    "v_cvt_pk_bf16_f32 v37, v82, v83\n\t"
    "v_mfma_f32_16x16x32_bf16 v[64:67], a[88:91], a[24:27], v[64:67]\n\t"
    "ds_read_b64_tr_b16 a[180:181], v16 offset:57344\n\t"
    "v_cvt_pk_bf16_f32 v38, v84, v85\n\t"
    "v_mfma_f32_16x16x32_bf16 v[64:67], a[92:95], a[28:31], v[64:67]\n\t"
    "ds_read_b64_tr_b16 a[182:183], v16 offset:61440\n\t"
    "v_cvt_pk_bf16_f32 v39, v86, v87\n\t"
    "v_mfma_f32_16x16x32_bf16 v[68:71], a[96:99], a[16:19], v[48:51]\n\t"
    "ds_read_b64_tr_b16 a[184:185], v17 offset:49152\n\t"
    "v_cvt_pk_bf16_f32 v40, v88, v89\n\t"
    "v_mfma_f32_16x16x32_bf16 v[68:71], a[100:103], a[20:23], v[68:71]\n\t"
    "ds_read_b64_tr_b16 a[186:187], v17 offset:53248\n\t"
    "v_cvt_pk_bf16_f32 v41, v90, v91\n\t"
    "v_mfma_f32_16x16x32_bf16 v[68:71], a[104:107], a[24:27], v[68:71]\n\t"
    "ds_read_b64_tr_b16 a[188:189], v17 offset:57344\n\t"
    "v_cvt_pk_bf16_f32 v42, v92, v93\n\t"
    "v_mfma_f32_16x16x32_bf16 v[68:71], a[108:111], a[28:31], v[68:71]\n\t"
    "ds_read_b64_tr_b16 a[190:191], v17 offset:61440\n\t"
    "v_cvt_pk_bf16_f32 v43, v94, v95\n\t"
    "v_mfma_f32_16x16x32_bf16 v[72:75], a[112:115], a[16:19], v[48:51]\n\t"
    "v_exp_f32 v64, v64\n\t"
    "v_mfma_f32_16x16x32_bf16 v[72:75], a[116:119], a[20:23], v[72:75]\n\t"
    "v_exp_f32 v65, v65\n\t"
    "v_mfma_f32_16x16x32_bf16 v[72:75], a[120:123], a[24:27], v[72:75]\n\t"
    "v_exp_f32 v66, v66\n\t"
    "v_mfma_f32_16x16x32_bf16 v[72:75], a[124:127], a[28:31], v[72:75]\n\t"
    "v_exp_f32 v67, v67\n\t"
    "v_mfma_f32_16x16x32_bf16 v[76:79], a[128:131], a[16:19], v[48:51]\n\t"
    "v_exp_f32 v68, v68\n\t"
    "v_mfma_f32_16x16x32_bf16 v[76:79], a[132:135], a[20:23], v[76:79]\n\t"
    "v_exp_f32 v69, v69\n\t"
    "v_mfma_f32_16x16x32_bf16 v[76:79], a[136:139], a[24:27], v[76:79]\n\t"
    "v_exp_f32 v70, v70\n\t"
    "v_mfma_f32_16x16x32_bf16 v[76:79], a[140:143], a[28:31], v[76:79]\n\t"
    "s_waitcnt lgkmcnt(0)\n\t"
    "v_exp_f32 v71, v71\n\t"
    "v_mfma_f32_16x16x32_bf16 v[96:99], v[36:39], a[144:147], v[96:99]\n\t"
    "v_exp_f32 v72, v72\n\t"
    "v_mfma_f32_16x16x32_bf16 v[96:99], v[40:43], a[148:151], v[96:99]\n\t"
    "v_exp_f32 v73, v73\n\t"
    "v_mfma_f32_16x16x32_bf16 v[100:103], v[36:39], a[152:155], v[100:103]\n\t"
    "v_exp_f32 v74, v74\n\t"
    "v_mfma_f32_16x16x32_bf16 v[100:103], v[40:43], a[156:159], v[100:103]\n\t"
    "v_exp_f32 v75, v75\n\t"
    "v_mfma_f32_16x16x32_bf16 v[104:107], v[36:39], a[160:163], v[104:107]\n\t"
    "v_exp_f32 v76, v76\n\t"
    "v_mfma_f32_16x16x32_bf16 v[104:107], v[40:43], a[164:167], v[104:107]\n\t"
    "v_exp_f32 v77, v77\n\t"
    "v_mfma_f32_16x16x32_bf16 v[108:111], v[36:39], a[168:171], v[108:111]\n\t"
    "v_exp_f32 v78, v78\n\t"
    "v_mfma_f32_16x16x32_bf16 v[108:111], v[40:43], a[172:175], v[108:111]\n\t"
    "v_exp_f32 v79, v79\n\t"
    "v_mfma_f32_16x16x32_bf16 v[112:115], v[36:39], a[176:179], v[112:115]\n\t"
    "v_add_u32 v35, 1, v35\n\t"
    "v_mfma_f32_16x16x32_bf16 v[112:115], v[40:43], a[180:183], v[112:115]\n\t"
    "ds_write_b32 v27, v35 offset:6144\n\t"
    "v_mfma_f32_16x16x32_bf16 v[116:119], v[36:39], a[184:187], v[116:119]\n\t"
    "v_mfma_f32_16x16x32_bf16 v[116:119], v[40:43], a[188:191], v[116:119]\n\t"
    "v_mfma_f32_16x16x32_bf16 v[120:123], v[36:39], a[240:243], v[120:123]\n\t"
    "v_mfma_f32_16x16x32_bf16 v[120:123], v[40:43], a[244:247], v[120:123]\n\t"
    "v_mfma_f32_16x16x32_bf16 v[124:127], v[36:39], a[248:251], v[124:127]\n\t"
    "v_mfma_f32_16x16x32_bf16 v[124:127], v[40:43], a[252:255], v[124:127]\n\t"
    "ds_read_b32 v33, v27 offset:3072\n\t"
    "v_smfmac_f32_16x16x64_bf16 a[212:215], a[208:211], v[36:43], v24\n\t"
    "v_mfma_f32_16x16x32_bf16 v[80:83], a[80:83], a[32:35], v[52:55]\n\t"
    "v_cvt_pk_bf16_f32 v36, v64, v65\n\t"
    "v_mfma_f32_16x16x32_bf16 v[80:83], a[84:87], a[36:39], v[80:83]\n\t"
    "v_cvt_pk_bf16_f32 v37, v66, v67\n\t"
    "v_mfma_f32_16x16x32_bf16 v[80:83], a[88:91], a[40:43], v[80:83]\n\t"
    "v_cvt_pk_bf16_f32 v38, v68, v69\n\t"
    "v_mfma_f32_16x16x32_bf16 v[80:83], a[92:95], a[44:47], v[80:83]\n\t"
    "v_cvt_pk_bf16_f32 v39, v70, v71\n\t"
    "v_mfma_f32_16x16x32_bf16 v[84:87], a[96:99], a[32:35], v[52:55]\n\t"
    "v_cvt_pk_bf16_f32 v40, v72, v73\n\t"
    "v_mfma_f32_16x16x32_bf16 v[84:87], a[100:103], a[36:39], v[84:87]\n\t"
    "v_cvt_pk_bf16_f32 v41, v74, v75\n\t"
    "v_mfma_f32_16x16x32_bf16 v[84:87], a[104:107], a[40:43], v[84:87]\n\t"
    "v_cvt_pk_bf16_f32 v42, v76, v77\n\t"
    "v_mfma_f32_16x16x32_bf16 v[84:87], a[108:111], a[44:47], v[84:87]\n\t"
    "v_cvt_pk_bf16_f32 v43, v78, v79\n\t"
    "v_mfma_f32_16x16x32_bf16 v[88:91], a[112:115], a[32:35], v[52:55]\n\t"
    "v_exp_f32 v80, v80\n\t"
    "v_mfma_f32_16x16x32_bf16 v[88:91], a[116:119], a[36:39], v[88:91]\n\t"
    "v_exp_f32 v81, v81\n\t"
    "v_mfma_f32_16x16x32_bf16 v[88:91], a[120:123], a[40:43], v[88:91]\n\t"
    "v_exp_f32 v82, v82\n\t"
    "v_mfma_f32_16x16x32_bf16 v[88:91], a[124:127], a[44:47], v[88:91]\n\t"
    "v_exp_f32 v83, v83\n\t"
    "v_mfma_f32_16x16x32_bf16 v[92:95], a[128:131], a[32:35], v[52:55]\n\t"
    "v_exp_f32 v84, v84\n\t"
    "v_mfma_f32_16x16x32_bf16 v[92:95], a[132:135], a[36:39], v[92:95]\n\t"
    "v_exp_f32 v85, v85\n\t"
    "v_mfma_f32_16x16x32_bf16 v[92:95], a[136:139], a[40:43], v[92:95]\n\t"
    "v_exp_f32 v86, v86\n\t"
    "v_mfma_f32_16x16x32_bf16 v[92:95], a[140:143], a[44:47], v[92:95]\n\t"
    "v_exp_f32 v87, v87\n\t"
    "v_mfma_f32_16x16x32_bf16 v[128:131], v[36:39], a[144:147], v[128:131]\n\t"
    "v_exp_f32 v88, v88\n\t"
    "v_mfma_f32_16x16x32_bf16 v[128:131], v[40:43], a[148:151], v[128:131]\n\t"
    "v_exp_f32 v89, v89\n\t"
    "v_mfma_f32_16x16x32_bf16 v[132:135], v[36:39], a[152:155], v[132:135]\n\t"
    "v_exp_f32 v90, v90\n\t"
    "v_mfma_f32_16x16x32_bf16 v[132:135], v[40:43], a[156:159], v[132:135]\n\t"
    "v_exp_f32 v91, v91\n\t"
    "v_mfma_f32_16x16x32_bf16 v[136:139], v[36:39], a[160:163], v[136:139]\n\t"
    "v_exp_f32 v92, v92\n\t"
    "v_mfma_f32_16x16x32_bf16 v[136:139], v[40:43], a[164:167], v[136:139]\n\t"
    "v_exp_f32 v93, v93\n\t"
    "v_mfma_f32_16x16x32_bf16 v[140:143], v[36:39], a[168:171], v[140:143]\n\t"
    "v_exp_f32 v94, v94\n\t"
    "v_mfma_f32_16x16x32_bf16 v[140:143], v[40:43], a[172:175], v[140:143]\n\t"
    "v_exp_f32 v95, v95\n\t"
    "v_mfma_f32_16x16x32_bf16 v[144:147], v[36:39], a[176:179], v[144:147]\n\t"
    "v_mfma_f32_16x16x32_bf16 v[144:147], v[40:43], a[180:183], v[144:147]\n\t"
    "v_mfma_f32_16x16x32_bf16 v[148:151], v[36:39], a[184:187], v[148:151]\n\t"
    "s_waitcnt lgkmcnt(0)\n\t"
    "v_cmp_gt_u32 vcc, v35, v33\n\t"
    "v_mfma_f32_16x16x32_bf16 v[148:151], v[40:43], a[188:191], v[148:151]\n\t"
    "v_mfma_f32_16x16x32_bf16 v[152:155], v[36:39], a[240:243], v[152:155]\n\t"
    "v_mfma_f32_16x16x32_bf16 v[152:155], v[40:43], a[244:247], v[152:155]\n\t"
    "v_mfma_f32_16x16x32_bf16 v[156:159], v[36:39], a[248:251], v[156:159]\n\t"
    "s_cbranch_vccnz SPIN5L3%=\n\t"
    "RET5L3%=:\n\t"
    "v_mfma_f32_16x16x32_bf16 v[156:159], v[40:43], a[252:255], v[156:159]\n\t"
    "v_smfmac_f32_16x16x64_bf16 a[216:219], a[208:211], v[36:43], v24\n\t"
    "v_mfma_f32_16x16x32_bf16 v[64:67], a[80:83], a[48:51], v[56:59]\n\t"
    "ds_read_b64_tr_b16 a[192:193], v18 offset:0\n\t"
    "v_cvt_pk_bf16_f32 v36, v80, v81\n\t"
    "v_mfma_f32_16x16x32_bf16 v[64:67], a[84:87], a[52:55], v[64:67]\n\t"
    "ds_read_b64_tr_b16 a[194:195], v18 offset:4096\n\t"
    "v_cvt_pk_bf16_f32 v37, v82, v83\n\t"
    "v_mfma_f32_16x16x32_bf16 v[64:67], a[88:91], a[56:59], v[64:67]\n\t"
    "ds_read_b64_tr_b16 a[196:197], v18 offset:8192\n\t"
    "v_cvt_pk_bf16_f32 v38, v84, v85\n\t"
    "v_mfma_f32_16x16x32_bf16 v[64:67], a[92:95], a[60:63], v[64:67]\n\t"
    "ds_read_b64_tr_b16 a[198:199], v18 offset:12288\n\t"
    "v_cvt_pk_bf16_f32 v39, v86, v87\n\t"
    "v_mfma_f32_16x16x32_bf16 v[68:71], a[96:99], a[48:51], v[56:59]\n\t"
    "ds_read_b64_tr_b16 a[200:201], v19 offset:0\n\t"
    "v_cvt_pk_bf16_f32 v40, v88, v89\n\t"
    "v_mfma_f32_16x16x32_bf16 v[68:71], a[100:103], a[52:55], v[68:71]\n\t"
    "ds_read_b64_tr_b16 a[202:203], v19 offset:4096\n\t"
    "v_cvt_pk_bf16_f32 v41, v90, v91\n\t"
    "v_mfma_f32_16x16x32_bf16 v[68:71], a[104:107], a[56:59], v[68:71]\n\t"
    "ds_read_b64_tr_b16 a[204:205], v19 offset:8192\n\t"
    "v_cvt_pk_bf16_f32 v42, v92, v93\n\t"
    "v_mfma_f32_16x16x32_bf16 v[68:71], a[108:111], a[60:63], v[68:71]\n\t"
    "ds_read_b64_tr_b16 a[206:207], v19 offset:12288\n\t"
    "v_cvt_pk_bf16_f32 v43, v94, v95\n\t"
    "v_mfma_f32_16x16x32_bf16 v[72:75], a[112:115], a[48:51], v[56:59]\n\t"
    "v_exp_f32 v64, v64\n\t"
    "v_mfma_f32_16x16x32_bf16 v[72:75], a[116:119], a[52:55], v[72:75]\n\t"
    "v_exp_f32 v65, v65\n\t"
    "v_mfma_f32_16x16x32_bf16 v[72:75], a[120:123], a[56:59], v[72:75]\n\t"
    "v_exp_f32 v66, v66\n\t"
    "v_mfma_f32_16x16x32_bf16 v[72:75], a[124:127], a[60:63], v[72:75]\n\t"
    "v_exp_f32 v67, v67\n\t"
    "v_mfma_f32_16x16x32_bf16 v[76:79], a[128:131], a[48:51], v[56:59]\n\t"
    "v_exp_f32 v68, v68\n\t"
    "v_mfma_f32_16x16x32_bf16 v[76:79], a[132:135], a[52:55], v[76:79]\n\t"
    "v_exp_f32 v69, v69\n\t"
    "v_mfma_f32_16x16x32_bf16 v[76:79], a[136:139], a[56:59], v[76:79]\n\t"
    "v_exp_f32 v70, v70\n\t"
    "v_mfma_f32_16x16x32_bf16 v[76:79], a[140:143], a[60:63], v[76:79]\n\t"
    "v_exp_f32 v71, v71\n\t"
    "v_mfma_f32_16x16x32_bf16 v[160:163], v[36:39], a[144:147], v[160:163]\n\t"
    "v_exp_f32 v72, v72\n\t"
    "v_mfma_f32_16x16x32_bf16 v[160:163], v[40:43], a[148:151], v[160:163]\n\t"
    "v_exp_f32 v73, v73\n\t"
    "v_mfma_f32_16x16x32_bf16 v[164:167], v[36:39], a[152:155], v[164:167]\n\t"
    "v_exp_f32 v74, v74\n\t"
    "v_mfma_f32_16x16x32_bf16 v[164:167], v[40:43], a[156:159], v[164:167]\n\t"
    "v_exp_f32 v75, v75\n\t"
    "v_mfma_f32_16x16x32_bf16 v[168:171], v[36:39], a[160:163], v[168:171]\n\t"
    "v_exp_f32 v76, v76\n\t"
    "v_mfma_f32_16x16x32_bf16 v[168:171], v[40:43], a[164:167], v[168:171]\n\t"
    "v_exp_f32 v77, v77\n\t"
    "v_mfma_f32_16x16x32_bf16 v[172:175], v[36:39], a[168:171], v[172:175]\n\t"
    "v_exp_f32 v78, v78\n\t"
    "v_mfma_f32_16x16x32_bf16 v[172:175], v[40:43], a[172:175], v[172:175]\n\t"
    "v_exp_f32 v79, v79\n\t"
    "v_mfma_f32_16x16x32_bf16 v[176:179], v[36:39], a[176:179], v[176:179]\n\t"
    "v_mfma_f32_16x16x32_bf16 v[176:179], v[40:43], a[180:183], v[176:179]\n\t"
    "v_mfma_f32_16x16x32_bf16 v[180:183], v[36:39], a[184:187], v[180:183]\n\t"
    "v_mfma_f32_16x16x32_bf16 v[180:183], v[40:43], a[188:191], v[180:183]\n\t"
    "v_mfma_f32_16x16x32_bf16 v[184:187], v[36:39], a[240:243], v[184:187]\n\t"
    "v_mfma_f32_16x16x32_bf16 v[184:187], v[40:43], a[244:247], v[184:187]\n\t"
    "v_mfma_f32_16x16x32_bf16 v[188:191], v[36:39], a[248:251], v[188:191]\n\t"
    "v_mfma_f32_16x16x32_bf16 v[188:191], v[40:43], a[252:255], v[188:191]\n\t"
    "v_smfmac_f32_16x16x64_bf16 a[220:223], a[208:211], v[36:43], v24\n\t"
    "v_mfma_f32_16x16x32_bf16 v[84:87], a[96:99], a[64:67], v[60:63]\n\t"
    "ds_read_b128 a[80:83], v8 offset:0\n\t"
    "v_cvt_pk_bf16_f32 v36, v64, v65\n\t"
    "v_mfma_f32_16x16x32_bf16 v[84:87], a[100:103], a[68:71], v[84:87]\n\t"
    "ds_read_b128 a[84:87], v9 offset:0\n\t"
    "v_cvt_pk_bf16_f32 v37, v66, v67\n\t"
    "v_mfma_f32_16x16x32_bf16 v[84:87], a[104:107], a[72:75], v[84:87]\n\t"
    "ds_read_b128 a[88:91], v10 offset:0\n\t"
    "v_cvt_pk_bf16_f32 v38, v68, v69\n\t"
    "v_mfma_f32_16x16x32_bf16 v[84:87], a[108:111], a[76:79], v[84:87]\n\t"
    "ds_read_b128 a[92:95], v11 offset:0\n\t"
    "v_cvt_pk_bf16_f32 v39, v70, v71\n\t"
    "v_mfma_f32_16x16x32_bf16 v[88:91], a[112:115], a[64:67], v[60:63]\n\t"
    "ds_read_b128 a[96:99], v8 offset:4096\n\t"
    "v_cvt_pk_bf16_f32 v40, v72, v73\n\t"
    "v_mfma_f32_16x16x32_bf16 v[88:91], a[116:119], a[68:71], v[88:91]\n\t"
    "ds_read_b128 a[100:103], v9 offset:4096\n\t"
    "v_cvt_pk_bf16_f32 v41, v74, v75\n\t"
    "v_mfma_f32_16x16x32_bf16 v[88:91], a[120:123], a[72:75], v[88:91]\n\t"
    "ds_read_b128 a[104:107], v10 offset:4096\n\t"
    "v_cvt_pk_bf16_f32 v42, v76, v77\n\t"
    "v_mfma_f32_16x16x32_bf16 v[88:91], a[124:127], a[76:79], v[88:91]\n\t"
    "ds_read_b128 a[108:111], v11 offset:4096\n\t"
    "v_cvt_pk_bf16_f32 v43, v78, v79\n\t"
    "v_mfma_f32_16x16x32_bf16 v[92:95], a[128:131], a[64:67], v[60:63]\n\t"
    "v_exp_f32 v84, v84\n\t"
    "v_mfma_f32_16x16x32_bf16 v[92:95], a[132:135], a[68:71], v[92:95]\n\t"
    "v_exp_f32 v85, v85\n\t"
    "v_mfma_f32_16x16x32_bf16 v[92:95], a[136:139], a[72:75], v[92:95]\n\t"
    "v_exp_f32 v86, v86\n\t"
    "v_mfma_f32_16x16x32_bf16 v[92:95], a[140:143], a[76:79], v[92:95]\n\t"
    "v_exp_f32 v87, v87\n\t"
    "v_mfma_f32_16x16x32_bf16 v[192:195], v[36:39], a[144:147], v[192:195]\n\t"
    "v_exp_f32 v88, v88\n\t"
    "v_mfma_f32_16x16x32_bf16 v[192:195], v[40:43], a[148:151], v[192:195]\n\t"
    "v_exp_f32 v89, v89\n\t"
    "v_mfma_f32_16x16x32_bf16 v[196:199], v[36:39], a[152:155], v[196:199]\n\t"
    "v_exp_f32 v90, v90\n\t"
    "v_mfma_f32_16x16x32_bf16 v[196:199], v[40:43], a[156:159], v[196:199]\n\t"
    "v_exp_f32 v91, v91\n\t"
    "v_mfma_f32_16x16x32_bf16 v[200:203], v[36:39], a[160:163], v[200:203]\n\t"
    "v_exp_f32 v92, v92\n\t"
    "v_mfma_f32_16x16x32_bf16 v[200:203], v[40:43], a[164:167], v[200:203]\n\t"
    "v_exp_f32 v93, v93\n\t"
    "v_mfma_f32_16x16x32_bf16 v[204:207], v[36:39], a[168:171], v[204:207]\n\t"
    "v_exp_f32 v94, v94\n\t"
    "v_mfma_f32_16x16x32_bf16 v[204:207], v[40:43], a[172:175], v[204:207]\n\t"
    "v_exp_f32 v95, v95\n\t"
    "v_mfma_f32_16x16x32_bf16 v[208:211], v[36:39], a[176:179], v[208:211]\n\t"
    "ds_read_b128 a[112:115], v8 offset:8192\n\t"
    "v_mfma_f32_16x16x32_bf16 v[208:211], v[40:43], a[180:183], v[208:211]\n\t"
    "ds_read_b128 a[116:119], v9 offset:8192\n\t"
    "v_mfma_f32_16x16x32_bf16 v[212:215], v[36:39], a[184:187], v[212:215]\n\t"
    "ds_read_b128 a[120:123], v10 offset:8192\n\t"
    "v_mfma_f32_16x16x32_bf16 v[212:215], v[40:43], a[188:191], v[212:215]\n\t"
    "ds_read_b128 a[124:127], v11 offset:8192\n\t"
    "v_mfma_f32_16x16x32_bf16 v[216:219], v[36:39], a[240:243], v[216:219]\n\t"
    "ds_read_b128 a[128:131], v8 offset:12288\n\t"
    "v_mfma_f32_16x16x32_bf16 v[216:219], v[40:43], a[244:247], v[216:219]\n\t"
    "ds_read_b128 a[132:135], v9 offset:12288\n\t"
    "v_mfma_f32_16x16x32_bf16 v[220:223], v[36:39], a[248:251], v[220:223]\n\t"
    "ds_read_b128 a[136:139], v10 offset:12288\n\t"
    "v_mfma_f32_16x16x32_bf16 v[220:223], v[40:43], a[252:255], v[220:223]\n\t"
    "ds_read_b128 a[140:143], v11 offset:12288\n\t"
    "v_smfmac_f32_16x16x64_bf16 a[224:227], a[208:211], v[36:43], v24\n\t"
    "s_waitcnt lgkmcnt(7)\n\t"
    "v_mfma_f32_16x16x32_bf16 v[64:67], a[80:83], a[0:3], v[44:47]\n\t"
    "ds_read_b64 v[36:37], v27 offset:512\n\t"
    "v_mfma_f32_16x16x32_bf16 v[64:67], a[84:87], a[4:7], v[64:67]\n\t"
    "v_mfma_f32_16x16x32_bf16 v[64:67], a[88:91], a[8:11], v[64:67]\n\t"
    "v_cvt_pk_bf16_f32 v38, v84, v85\n\t"
    "v_mfma_f32_16x16x32_bf16 v[64:67], a[92:95], a[12:15], v[64:67]\n\t"
    "v_cvt_pk_bf16_f32 v39, v86, v87\n\t"
    "v_mfma_f32_16x16x32_bf16 v[68:71], a[96:99], a[0:3], v[44:47]\n\t"
    "v_cvt_pk_bf16_f32 v40, v88, v89\n\t"
    "v_mfma_f32_16x16x32_bf16 v[68:71], a[100:103], a[4:7], v[68:71]\n\t"
    "v_cvt_pk_bf16_f32 v41, v90, v91\n\t"
    "v_mfma_f32_16x16x32_bf16 v[68:71], a[104:107], a[8:11], v[68:71]\n\t"
    "v_cvt_pk_bf16_f32 v42, v92, v93\n\t"
    "v_mfma_f32_16x16x32_bf16 v[68:71], a[108:111], a[12:15], v[68:71]\n\t"
    "v_cvt_pk_bf16_f32 v43, v94, v95\n\t"
    "; TILE 4\n\t"
    "v_mfma_f32_16x16x32_bf16 v[72:75], a[112:115], a[0:3], v[44:47]\n\t"
    "s_waitcnt lgkmcnt(0)\n\t"
    "v_exp_f32 v64, v64\n\t"
    "v_mfma_f32_16x16x32_bf16 v[72:75], a[116:119], a[4:7], v[72:75]\n\t"
    "v_exp_f32 v65, v65\n\t"
    "v_mfma_f32_16x16x32_bf16 v[72:75], a[120:123], a[8:11], v[72:75]\n\t"
    "v_exp_f32 v66, v66\n\t"
    "v_mfma_f32_16x16x32_bf16 v[72:75], a[124:127], a[12:15], v[72:75]\n\t"
    "v_exp_f32 v67, v67\n\t"
    "v_mfma_f32_16x16x32_bf16 v[76:79], a[128:131], a[0:3], v[44:47]\n\t"
    "v_exp_f32 v68, v68\n\t"
    "v_mfma_f32_16x16x32_bf16 v[76:79], a[132:135], a[4:7], v[76:79]\n\t"
    "v_exp_f32 v69, v69\n\t"
    "v_mfma_f32_16x16x32_bf16 v[76:79], a[136:139], a[8:11], v[76:79]\n\t"
    "v_exp_f32 v70, v70\n\t"
    "v_mfma_f32_16x16x32_bf16 v[76:79], a[140:143], a[12:15], v[76:79]\n\t"
    "v_exp_f32 v71, v71\n\t"
    "v_mfma_f32_16x16x32_bf16 v[224:227], v[36:39], a[144:147], v[224:227]\n\t"
    "v_exp_f32 v72, v72\n\t"
    "v_mfma_f32_16x16x32_bf16 v[224:227], v[40:43], a[148:151], v[224:227]\n\t"
    "v_exp_f32 v73, v73\n\t"
    "v_mfma_f32_16x16x32_bf16 v[228:231], v[36:39], a[152:155], v[228:231]\n\t"
    "v_exp_f32 v74, v74\n\t"
    "v_mfma_f32_16x16x32_bf16 v[228:231], v[40:43], a[156:159], v[228:231]\n\t"
    "v_exp_f32 v75, v75\n\t"
    "v_mfma_f32_16x16x32_bf16 v[232:235], v[36:39], a[160:163], v[232:235]\n\t"
    "v_exp_f32 v76, v76\n\t"
    "v_mfma_f32_16x16x32_bf16 v[232:235], v[40:43], a[164:167], v[232:235]\n\t"
    "v_exp_f32 v77, v77\n\t"
    "v_mfma_f32_16x16x32_bf16 v[236:239], v[36:39], a[168:171], v[236:239]\n\t"
    "v_exp_f32 v78, v78\n\t"
    "v_mfma_f32_16x16x32_bf16 v[236:239], v[40:43], a[172:175], v[236:239]\n\t"
    "v_exp_f32 v79, v79\n\t"
    "v_mfma_f32_16x16x32_bf16 v[240:243], v[36:39], a[176:179], v[240:243]\n\t"
    "ds_read_b64_tr_b16 a[144:145], v12 offset:0\n\t"
    "ds_read_b64_tr_b16 a[146:147], v12 offset:4096\n\t"
    "v_mfma_f32_16x16x32_bf16 v[240:243], v[40:43], a[180:183], v[240:243]\n\t"
    "ds_read_b64_tr_b16 a[148:149], v12 offset:8192\n\t"
    "ds_read_b64_tr_b16 a[150:151], v12 offset:12288\n\t"
    "v_mfma_f32_16x16x32_bf16 v[244:247], v[36:39], a[184:187], v[244:247]\n\t"
    "ds_read_b64_tr_b16 a[152:153], v13 offset:0\n\t"
    "ds_read_b64_tr_b16 a[154:155], v13 offset:4096\n\t"
    "v_mfma_f32_16x16x32_bf16 v[244:247], v[40:43], a[188:191], v[244:247]\n\t"
    "ds_read_b64_tr_b16 a[156:157], v13 offset:8192\n\t"
    "ds_read_b64_tr_b16 a[158:159], v13 offset:12288\n\t"
    "v_mfma_f32_16x16x32_bf16 v[248:251], v[36:39], a[240:243], v[248:251]\n\t"
    "ds_read_b64_tr_b16 a[160:161], v14 offset:0\n\t"
    "ds_read_b64_tr_b16 a[162:163], v14 offset:4096\n\t"
    "v_mfma_f32_16x16x32_bf16 v[248:251], v[40:43], a[244:247], v[248:251]\n\t"
    "ds_read_b64_tr_b16 a[164:165], v14 offset:8192\n\t"
    "ds_read_b64_tr_b16 a[166:167], v14 offset:12288\n\t"
    "v_mfma_f32_16x16x32_bf16 v[252:255], v[36:39], a[248:251], v[252:255]\n\t"
    "ds_read_b64_tr_b16 a[168:169], v15 offset:0\n\t"
    "ds_read_b64_tr_b16 a[170:171], v15 offset:4096\n\t"
    "v_mfma_f32_16x16x32_bf16 v[252:255], v[40:43], a[252:255], v[252:255]\n\t"
    "ds_read_b64_tr_b16 a[172:173], v15 offset:8192\n\t"
    "ds_read_b64_tr_b16 a[174:175], v15 offset:12288\n\t"
    "v_smfmac_f32_16x16x64_bf16 a[228:231], a[208:211], v[36:43], v24\n\t"
    "v_mfma_f32_16x16x32_bf16 v[80:83], a[80:83], a[16:19], v[48:51]\n\t"
    "ds_read_b64_tr_b16 a[176:177], v16 offset:0\n\t"
    "v_cvt_pk_bf16_f32 v36, v64, v65\n\t"
    "v_mfma_f32_16x16x32_bf16 v[80:83], a[84:87], a[20:23], v[80:83]\n\t"
    "ds_read_b64_tr_b16 a[178:179], v16 offset:4096\n\t"
    "v_cvt_pk_bf16_f32 v37, v66, v67\n\t"
    "v_mfma_f32_16x16x32_bf16 v[80:83], a[88:91], a[24:27], v[80:83]\n\t"
    "ds_read_b64_tr_b16 a[180:181], v16 offset:8192\n\t"
    "v_cvt_pk_bf16_f32 v38, v68, v69\n\t"
    "v_mfma_f32_16x16x32_bf16 v[80:83], a[92:95], a[28:31], v[80:83]\n\t"
    "ds_read_b64_tr_b16 a[182:183], v16 offset:12288\n\t"
    "v_cvt_pk_bf16_f32 v39, v70, v71\n\t"
    "v_mfma_f32_16x16x32_bf16 v[84:87], a[96:99], a[16:19], v[48:51]\n\t"
    "ds_read_b64_tr_b16 a[184:185], v17 offset:0\n\t"
    "v_cvt_pk_bf16_f32 v40, v72, v73\n\t"
    "v_mfma_f32_16x16x32_bf16 v[84:87], a[100:103], a[20:23], v[84:87]\n\t"
    "ds_read_b64_tr_b16 a[186:187], v17 offset:4096\n\t"
    "v_cvt_pk_bf16_f32 v41, v74, v75\n\t"
    "v_mfma_f32_16x16x32_bf16 v[84:87], a[104:107], a[24:27], v[84:87]\n\t"
    "ds_read_b64_tr_b16 a[188:189], v17 offset:8192\n\t"
    "v_cvt_pk_bf16_f32 v42, v76, v77\n\t"
    "v_mfma_f32_16x16x32_bf16 v[84:87], a[108:111], a[28:31], v[84:87]\n\t"
    "ds_read_b64_tr_b16 a[190:191], v17 offset:12288\n\t"
    "v_cvt_pk_bf16_f32 v43, v78, v79\n\t"
    "v_mfma_f32_16x16x32_bf16 v[88:91], a[112:115], a[16:19], v[48:51]\n\t"
    "v_exp_f32 v80, v80\n\t"
    "v_mfma_f32_16x16x32_bf16 v[88:91], a[116:119], a[20:23], v[88:91]\n\t"
    "v_exp_f32 v81, v81\n\t"
    "v_mfma_f32_16x16x32_bf16 v[88:91], a[120:123], a[24:27], v[88:91]\n\t"
    "v_exp_f32 v82, v82\n\t"
    "v_mfma_f32_16x16x32_bf16 v[88:91], a[124:127], a[28:31], v[88:91]\n\t"
    "v_exp_f32 v83, v83\n\t"
    "v_mfma_f32_16x16x32_bf16 v[92:95], a[128:131], a[16:19], v[48:51]\n\t"
    "v_exp_f32 v84, v84\n\t"
    "v_mfma_f32_16x16x32_bf16 v[92:95], a[132:135], a[20:23], v[92:95]\n\t"
    "v_exp_f32 v85, v85\n\t"
    "v_mfma_f32_16x16x32_bf16 v[92:95], a[136:139], a[24:27], v[92:95]\n\t"
    "v_exp_f32 v86, v86\n\t"
    "v_mfma_f32_16x16x32_bf16 v[92:95], a[140:143], a[28:31], v[92:95]\n\t"
    "s_waitcnt lgkmcnt(0)\n\t"
    "v_exp_f32 v87, v87\n\t"
    "v_mfma_f32_16x16x32_bf16 v[96:99], v[36:39], a[144:147], v[96:99]\n\t"
    "v_exp_f32 v88, v88\n\t"
    "v_mfma_f32_16x16x32_bf16 v[96:99], v[40:43], a[148:151], v[96:99]\n\t"
    "v_exp_f32 v89, v89\n\t"
    "v_mfma_f32_16x16x32_bf16 v[100:103], v[36:39], a[152:155], v[100:103]\n\t"
    "v_exp_f32 v90, v90\n\t"
    "v_mfma_f32_16x16x32_bf16 v[100:103], v[40:43], a[156:159], v[100:103]\n\t"
    "v_exp_f32 v91, v91\n\t"
    "v_mfma_f32_16x16x32_bf16 v[104:107], v[36:39], a[160:163], v[104:107]\n\t"
    "v_exp_f32 v92, v92\n\t"
    "v_mfma_f32_16x16x32_bf16 v[104:107], v[40:43], a[164:167], v[104:107]\n\t"
    "v_exp_f32 v93, v93\n\t"
    "v_mfma_f32_16x16x32_bf16 v[108:111], v[36:39], a[168:171], v[108:111]\n\t"
    "v_exp_f32 v94, v94\n\t"
    "v_mfma_f32_16x16x32_bf16 v[108:111], v[40:43], a[172:175], v[108:111]\n\t"
    "v_exp_f32 v95, v95\n\t"
    "v_mfma_f32_16x16x32_bf16 v[112:115], v[36:39], a[176:179], v[112:115]\n\t"
    "v_add_u32 v35, 1, v35\n\t"
    "v_mfma_f32_16x16x32_bf16 v[112:115], v[40:43], a[180:183], v[112:115]\n\t"
    "ds_write_b32 v27, v35 offset:6144\n\t"
    "v_mfma_f32_16x16x32_bf16 v[116:119], v[36:39], a[184:187], v[116:119]\n\t"
    "v_mfma_f32_16x16x32_bf16 v[116:119], v[40:43], a[188:191], v[116:119]\n\t"
    "v_mfma_f32_16x16x32_bf16 v[120:123], v[36:39], a[192:195], v[120:123]\n\t"
    "v_mfma_f32_16x16x32_bf16 v[120:123], v[40:43], a[196:199], v[120:123]\n\t"
    "v_mfma_f32_16x16x32_bf16 v[124:127], v[36:39], a[200:203], v[124:127]\n\t"
    "v_mfma_f32_16x16x32_bf16 v[124:127], v[40:43], a[204:207], v[124:127]\n\t"
    "ds_read_b32 v33, v27 offset:3072\n\t"
    "v_smfmac_f32_16x16x64_bf16 a[212:215], a[208:211], v[36:43], v24\n\t"
    "v_mfma_f32_16x16x32_bf16 v[64:67], a[80:83], a[32:35], v[52:55]\n\t"
    "v_cvt_pk_bf16_f32 v36, v80, v81\n\t"
    "v_mfma_f32_16x16x32_bf16 v[64:67], a[84:87], a[36:39], v[64:67]\n\t"
    "v_cvt_pk_bf16_f32 v37, v82, v83\n\t"
    "v_mfma_f32_16x16x32_bf16 v[64:67], a[88:91], a[40:43], v[64:67]\n\t"
    "v_cvt_pk_bf16_f32 v38, v84, v85\n\t"
    "v_mfma_f32_16x16x32_bf16 v[64:67], a[92:95], a[44:47], v[64:67]\n\t"
    "v_cvt_pk_bf16_f32 v39, v86, v87\n\t"
    "v_mfma_f32_16x16x32_bf16 v[68:71], a[96:99], a[32:35], v[52:55]\n\t"
    "v_cvt_pk_bf16_f32 v40, v88, v89\n\t"
    "v_mfma_f32_16x16x32_bf16 v[68:71], a[100:103], a[36:39], v[68:71]\n\t"
    "v_cvt_pk_bf16_f32 v41, v90, v91\n\t"
    "v_mfma_f32_16x16x32_bf16 v[68:71], a[104:107], a[40:43], v[68:71]\n\t"
    "v_cvt_pk_bf16_f32 v42, v92, v93\n\t"
    "v_mfma_f32_16x16x32_bf16 v[68:71], a[108:111], a[44:47], v[68:71]\n\t"
    "v_cvt_pk_bf16_f32 v43, v94, v95\n\t"
    "v_mfma_f32_16x16x32_bf16 v[72:75], a[112:115], a[32:35], v[52:55]\n\t"
    "v_exp_f32 v64, v64\n\t"
    "v_mfma_f32_16x16x32_bf16 v[72:75], a[116:119], a[36:39], v[72:75]\n\t"
    "v_exp_f32 v65, v65\n\t"
    "v_mfma_f32_16x16x32_bf16 v[72:75], a[120:123], a[40:43], v[72:75]\n\t"
    "v_exp_f32 v66, v66\n\t"
    "v_mfma_f32_16x16x32_bf16 v[72:75], a[124:127], a[44:47], v[72:75]\n\t"
    "v_exp_f32 v67, v67\n\t"
    "v_mfma_f32_16x16x32_bf16 v[76:79], a[128:131], a[32:35], v[52:55]\n\t"
    "v_exp_f32 v68, v68\n\t"
    "v_mfma_f32_16x16x32_bf16 v[76:79], a[132:135], a[36:39], v[76:79]\n\t"
    "v_exp_f32 v69, v69\n\t"
    "v_mfma_f32_16x16x32_bf16 v[76:79], a[136:139], a[40:43], v[76:79]\n\t"
    "v_exp_f32 v70, v70\n\t"
    "v_mfma_f32_16x16x32_bf16 v[76:79], a[140:143], a[44:47], v[76:79]\n\t"
    "v_exp_f32 v71, v71\n\t"
    "v_mfma_f32_16x16x32_bf16 v[128:131], v[36:39], a[144:147], v[128:131]\n\t"
    "v_exp_f32 v72, v72\n\t"
    "v_mfma_f32_16x16x32_bf16 v[128:131], v[40:43], a[148:151], v[128:131]\n\t"
    "v_exp_f32 v73, v73\n\t"
    "v_mfma_f32_16x16x32_bf16 v[132:135], v[36:39], a[152:155], v[132:135]\n\t"
    "v_exp_f32 v74, v74\n\t"
    "v_mfma_f32_16x16x32_bf16 v[132:135], v[40:43], a[156:159], v[132:135]\n\t"
    "v_exp_f32 v75, v75\n\t"
    "v_mfma_f32_16x16x32_bf16 v[136:139], v[36:39], a[160:163], v[136:139]\n\t"
    "v_exp_f32 v76, v76\n\t"
    "v_mfma_f32_16x16x32_bf16 v[136:139], v[40:43], a[164:167], v[136:139]\n\t"
    "v_exp_f32 v77, v77\n\t"
    "v_mfma_f32_16x16x32_bf16 v[140:143], v[36:39], a[168:171], v[140:143]\n\t"
    "v_exp_f32 v78, v78\n\t"
    "v_mfma_f32_16x16x32_bf16 v[140:143], v[40:43], a[172:175], v[140:143]\n\t"
    "v_exp_f32 v79, v79\n\t"
    "v_mfma_f32_16x16x32_bf16 v[144:147], v[36:39], a[176:179], v[144:147]\n\t"
    "v_mfma_f32_16x16x32_bf16 v[144:147], v[40:43], a[180:183], v[144:147]\n\t"
    "v_mfma_f32_16x16x32_bf16 v[148:151], v[36:39], a[184:187], v[148:151]\n\t"
    "s_waitcnt lgkmcnt(0)\n\t"
    "v_cmp_gt_u32 vcc, v35, v33\n\t"
    "v_mfma_f32_16x16x32_bf16 v[148:151], v[40:43], a[188:191], v[148:151]\n\t"
    "v_mfma_f32_16x16x32_bf16 v[152:155], v[36:39], a[192:195], v[152:155]\n\t"
    "v_mfma_f32_16x16x32_bf16 v[152:155], v[40:43], a[196:199], v[152:155]\n\t"
    "v_mfma_f32_16x16x32_bf16 v[156:159], v[36:39], a[200:203], v[156:159]\n\t"
    "s_cbranch_vccnz SPIN5L0%=\n\t"
    "RET5L0%=:\n\t"
    "v_mfma_f32_16x16x32_bf16 v[156:159], v[40:43], a[204:207], v[156:159]\n\t"
    "v_smfmac_f32_16x16x64_bf16 a[216:219], a[208:211], v[36:43], v24\n\t"
    "v_mfma_f32_16x16x32_bf16 v[80:83], a[80:83], a[48:51], v[56:59]\n\t"
    "ds_read_b64_tr_b16 a[240:241], v18 offset:16384\n\t"
    "v_cvt_pk_bf16_f32 v36, v64, v65\n\t"
    "v_mfma_f32_16x16x32_bf16 v[80:83], a[84:87], a[52:55], v[80:83]\n\t"
    "ds_read_b64_tr_b16 a[242:243], v18 offset:20480\n\t"
    "v_cvt_pk_bf16_f32 v37, v66, v67\n\t"
    "v_mfma_f32_16x16x32_bf16 v[80:83], a[88:91], a[56:59], v[80:83]\n\t"
    "ds_read_b64_tr_b16 a[244:245], v18 offset:24576\n\t"
    "v_cvt_pk_bf16_f32 v38, v68, v69\n\t"
    "v_mfma_f32_16x16x32_bf16 v[80:83], a[92:95], a[60:63], v[80:83]\n\t"
    "ds_read_b64_tr_b16 a[246:247], v18 offset:28672\n\t"
    "v_cvt_pk_bf16_f32 v39, v70, v71\n\t"
    "v_mfma_f32_16x16x32_bf16 v[84:87], a[96:99], a[48:51], v[56:59]\n\t"
    "ds_read_b64_tr_b16 a[248:249], v19 offset:16384\n\t"
    "v_cvt_pk_bf16_f32 v40, v72, v73\n\t"
    "v_mfma_f32_16x16x32_bf16 v[84:87], a[100:103], a[52:55], v[84:87]\n\t"
    "ds_read_b64_tr_b16 a[250:251], v19 offset:20480\n\t"
    "v_cvt_pk_bf16_f32 v41, v74, v75\n\t"
    "v_mfma_f32_16x16x32_bf16 v[84:87], a[104:107], a[56:59], v[84:87]\n\t"
    "ds_read_b64_tr_b16 a[252:253], v19 offset:24576\n\t"
    "v_cvt_pk_bf16_f32 v42, v76, v77\n\t"
    "v_mfma_f32_16x16x32_bf16 v[84:87], a[108:111], a[60:63], v[84:87]\n\t"
    "ds_read_b64_tr_b16 a[254:255], v19 offset:28672\n\t"
    "v_cvt_pk_bf16_f32 v43, v78, v79\n\t"
    "v_mfma_f32_16x16x32_bf16 v[88:91], a[112:115], a[48:51], v[56:59]\n\t"
    "v_exp_f32 v80, v80\n\t"
    "v_mfma_f32_16x16x32_bf16 v[88:91], a[116:119], a[52:55], v[88:91]\n\t"
    "v_exp_f32 v81, v81\n\t"
    "v_mfma_f32_16x16x32_bf16 v[88:91], a[120:123], a[56:59], v[88:91]\n\t"
    "v_exp_f32 v82, v82\n\t"
    "v_mfma_f32_16x16x32_bf16 v[88:91], a[124:127], a[60:63], v[88:91]\n\t"
    "v_exp_f32 v83, v83\n\t"
    "v_mfma_f32_16x16x32_bf16 v[92:95], a[128:131], a[48:51], v[56:59]\n\t"
    "v_exp_f32 v84, v84\n\t"
    "v_mfma_f32_16x16x32_bf16 v[92:95], a[132:135], a[52:55], v[92:95]\n\t"
    "v_exp_f32 v85, v85\n\t"
    "v_mfma_f32_16x16x32_bf16 v[92:95], a[136:139], a[56:59], v[92:95]\n\t"
    "v_exp_f32 v86, v86\n\t"
    "v_mfma_f32_16x16x32_bf16 v[92:95], a[140:143], a[60:63], v[92:95]\n\t"
    "v_exp_f32 v87, v87\n\t"
    "v_mfma_f32_16x16x32_bf16 v[160:163], v[36:39], a[144:147], v[160:163]\n\t"
    "v_exp_f32 v88, v88\n\t"
    "v_mfma_f32_16x16x32_bf16 v[160:163], v[40:43], a[148:151], v[160:163]\n\t"
    "v_exp_f32 v89, v89\n\t"
    "v_mfma_f32_16x16x32_bf16 v[164:167], v[36:39], a[152:155], v[164:167]\n\t"
    "v_exp_f32 v90, v90\n\t"
    "v_mfma_f32_16x16x32_bf16 v[164:167], v[40:43], a[156:159], v[164:167]\n\t"
    "v_exp_f32 v91, v91\n\t"
    "v_mfma_f32_16x16x32_bf16 v[168:171], v[36:39], a[160:163], v[168:171]\n\t"
    "v_exp_f32 v92, v92\n\t"
    "v_mfma_f32_16x16x32_bf16 v[168:171], v[40:43], a[164:167], v[168:171]\n\t"
    "v_exp_f32 v93, v93\n\t"
    "v_mfma_f32_16x16x32_bf16 v[172:175], v[36:39], a[168:171], v[172:175]\n\t"
    "v_exp_f32 v94, v94\n\t"
    "v_mfma_f32_16x16x32_bf16 v[172:175], v[40:43], a[172:175], v[172:175]\n\t"
    "v_exp_f32 v95, v95\n\t"
    "v_mfma_f32_16x16x32_bf16 v[176:179], v[36:39], a[176:179], v[176:179]\n\t"
    "v_mfma_f32_16x16x32_bf16 v[176:179], v[40:43], a[180:183], v[176:179]\n\t"
    "v_mfma_f32_16x16x32_bf16 v[180:183], v[36:39], a[184:187], v[180:183]\n\t"
    "v_mfma_f32_16x16x32_bf16 v[180:183], v[40:43], a[188:191], v[180:183]\n\t"
    "v_mfma_f32_16x16x32_bf16 v[184:187], v[36:39], a[192:195], v[184:187]\n\t"
    "v_mfma_f32_16x16x32_bf16 v[184:187], v[40:43], a[196:199], v[184:187]\n\t"
    "v_mfma_f32_16x16x32_bf16 v[188:191], v[36:39], a[200:203], v[188:191]\n\t"
    "v_mfma_f32_16x16x32_bf16 v[188:191], v[40:43], a[204:207], v[188:191]\n\t"
    "v_smfmac_f32_16x16x64_bf16 a[220:223], a[208:211], v[36:43], v24\n\t"
    "v_mfma_f32_16x16x32_bf16 v[68:71], a[96:99], a[64:67], v[60:63]\n\t"
    "ds_read_b128 a[80:83], v8 offset:16384\n\t"
    "v_cvt_pk_bf16_f32 v36, v80, v81\n\t"
    "v_mfma_f32_16x16x32_bf16 v[68:71], a[100:103], a[68:71], v[68:71]\n\t"
    "ds_read_b128 a[84:87], v9 offset:16384\n\t"
    "v_cvt_pk_bf16_f32 v37, v82, v83\n\t"
    "v_mfma_f32_16x16x32_bf16 v[68:71], a[104:107], a[72:75], v[68:71]\n\t"
    "ds_read_b128 a[88:91], v10 offset:16384\n\t"
    "v_cvt_pk_bf16_f32 v38, v84, v85\n\t"
    "v_mfma_f32_16x16x32_bf16 v[68:71], a[108:111], a[76:79], v[68:71]\n\t"
    "ds_read_b128 a[92:95], v11 offset:16384\n\t"
    "v_cvt_pk_bf16_f32 v39, v86, v87\n\t"
    "v_mfma_f32_16x16x32_bf16 v[72:75], a[112:115], a[64:67], v[60:63]\n\t"
    "ds_read_b128 a[96:99], v8 offset:20480\n\t"
    "v_cvt_pk_bf16_f32 v40, v88, v89\n\t"
    "v_mfma_f32_16x16x32_bf16 v[72:75], a[116:119], a[68:71], v[72:75]\n\t"
    "ds_read_b128 a[100:103], v9 offset:20480\n\t"
    "v_cvt_pk_bf16_f32 v41, v90, v91\n\t"
    "v_mfma_f32_16x16x32_bf16 v[72:75], a[120:123], a[72:75], v[72:75]\n\t"
    "ds_read_b128 a[104:107], v10 offset:20480\n\t"
    "v_cvt_pk_bf16_f32 v42, v92, v93\n\t"
    "v_mfma_f32_16x16x32_bf16 v[72:75], a[124:127], a[76:79], v[72:75]\n\t"
    "ds_read_b128 a[108:111], v11 offset:20480\n\t"
    "v_cvt_pk_bf16_f32 v43, v94, v95\n\t"
    "v_mfma_f32_16x16x32_bf16 v[76:79], a[128:131], a[64:67], v[60:63]\n\t"
    "v_exp_f32 v68, v68\n\t"
    "v_mfma_f32_16x16x32_bf16 v[76:79], a[132:135], a[68:71], v[76:79]\n\t"
    "v_exp_f32 v69, v69\n\t"
    "v_mfma_f32_16x16x32_bf16 v[76:79], a[136:139], a[72:75], v[76:79]\n\t"
    "v_exp_f32 v70, v70\n\t"
    "v_mfma_f32_16x16x32_bf16 v[76:79], a[140:143], a[76:79], v[76:79]\n\t"
    "v_exp_f32 v71, v71\n\t"
    "v_mfma_f32_16x16x32_bf16 v[192:195], v[36:39], a[144:147], v[192:195]\n\t"
    "v_exp_f32 v72, v72\n\t"
    "v_mfma_f32_16x16x32_bf16 v[192:195], v[40:43], a[148:151], v[192:195]\n\t"
    "v_exp_f32 v73, v73\n\t"
    "v_mfma_f32_16x16x32_bf16 v[196:199], v[36:39], a[152:155], v[196:199]\n\t"
    "v_exp_f32 v74, v74\n\t"
    "v_mfma_f32_16x16x32_bf16 v[196:199], v[40:43], a[156:159], v[196:199]\n\t"
    "v_exp_f32 v75, v75\n\t"
    "v_mfma_f32_16x16x32_bf16 v[200:203], v[36:39], a[160:163], v[200:203]\n\t"
    "v_exp_f32 v76, v76\n\t"
    "v_mfma_f32_16x16x32_bf16 v[200:203], v[40:43], a[164:167], v[200:203]\n\t"
    "v_exp_f32 v77, v77\n\t"
    "v_mfma_f32_16x16x32_bf16 v[204:207], v[36:39], a[168:171], v[204:207]\n\t"
    "v_exp_f32 v78, v78\n\t"
    "v_mfma_f32_16x16x32_bf16 v[204:207], v[40:43], a[172:175], v[204:207]\n\t"
    "v_exp_f32 v79, v79\n\t"
    "v_mfma_f32_16x16x32_bf16 v[208:211], v[36:39], a[176:179], v[208:211]\n\t"
    "ds_read_b128 a[112:115], v8 offset:24576\n\t"
    "v_mfma_f32_16x16x32_bf16 v[208:211], v[40:43], a[180:183], v[208:211]\n\t"
    "ds_read_b128 a[116:119], v9 offset:24576\n\t"
    "v_mfma_f32_16x16x32_bf16 v[212:215], v[36:39], a[184:187], v[212:215]\n\t"
    "ds_read_b128 a[120:123], v10 offset:24576\n\t"
    "v_mfma_f32_16x16x32_bf16 v[212:215], v[40:43], a[188:191], v[212:215]\n\t"
    "ds_read_b128 a[124:127], v11 offset:24576\n\t"
    "v_mfma_f32_16x16x32_bf16 v[216:219], v[36:39], a[192:195], v[216:219]\n\t"
    "ds_read_b128 a[128:131], v8 offset:28672\n\t"
    "v_mfma_f32_16x16x32_bf16 v[216:219], v[40:43], a[196:199], v[216:219]\n\t"
    "ds_read_b128 a[132:135], v9 offset:28672\n\t"
    "v_mfma_f32_16x16x32_bf16 v[220:223], v[36:39], a[200:203], v[220:223]\n\t"
    "ds_read_b128 a[136:139], v10 offset:28672\n\t"
    "v_mfma_f32_16x16x32_bf16 v[220:223], v[40:43], a[204:207], v[220:223]\n\t"
    "ds_read_b128 a[140:143], v11 offset:28672\n\t"
    "v_smfmac_f32_16x16x64_bf16 a[224:227], a[208:211], v[36:43], v24\n\t"
    "s_waitcnt lgkmcnt(7)\n\t"
    "v_mfma_f32_16x16x32_bf16 v[80:83], a[80:83], a[0:3], v[44:47]\n\t"
    "ds_read_b64 v[36:37], v27 offset:0\n\t"
    "v_mfma_f32_16x16x32_bf16 v[80:83], a[84:87], a[4:7], v[80:83]\n\t"
    "v_mfma_f32_16x16x32_bf16 v[80:83], a[88:91], a[8:11], v[80:83]\n\t"
    "v_cvt_pk_bf16_f32 v38, v68, v69\n\t"
    "v_mfma_f32_16x16x32_bf16 v[80:83], a[92:95], a[12:15], v[80:83]\n\t"
    "v_cvt_pk_bf16_f32 v39, v70, v71\n\t"
    "v_mfma_f32_16x16x32_bf16 v[84:87], a[96:99], a[0:3], v[44:47]\n\t"
    "v_cvt_pk_bf16_f32 v40, v72, v73\n\t"
    "v_mfma_f32_16x16x32_bf16 v[84:87], a[100:103], a[4:7], v[84:87]\n\t"
    "v_cvt_pk_bf16_f32 v41, v74, v75\n\t"
    "v_mfma_f32_16x16x32_bf16 v[84:87], a[104:107], a[8:11], v[84:87]\n\t"
    "v_cvt_pk_bf16_f32 v42, v76, v77\n\t"
    "v_mfma_f32_16x16x32_bf16 v[84:87], a[108:111], a[12:15], v[84:87]\n\t"
    "v_cvt_pk_bf16_f32 v43, v78, v79\n\t"
    "; TILE 5\n\t"
    "v_mfma_f32_16x16x32_bf16 v[88:91], a[112:115], a[0:3], v[44:47]\n\t"
    "s_waitcnt lgkmcnt(0)\n\t"
    "v_exp_f32 v80, v80\n\t"
    "v_mfma_f32_16x16x32_bf16 v[88:91], a[116:119], a[4:7], v[88:91]\n\t"
    "v_exp_f32 v81, v81\n\t"
    "v_mfma_f32_16x16x32_bf16 v[88:91], a[120:123], a[8:11], v[88:91]\n\t"
    "v_exp_f32 v82, v82\n\t"
    "v_mfma_f32_16x16x32_bf16 v[88:91], a[124:127], a[12:15], v[88:91]\n\t"
    "v_exp_f32 v83, v83\n\t"
    "v_mfma_f32_16x16x32_bf16 v[92:95], a[128:131], a[0:3], v[44:47]\n\t"
    "v_exp_f32 v84, v84\n\t"
    "v_mfma_f32_16x16x32_bf16 v[92:95], a[132:135], a[4:7], v[92:95]\n\t"
    "v_exp_f32 v85, v85\n\t"
    "v_mfma_f32_16x16x32_bf16 v[92:95], a[136:139], a[8:11], v[92:95]\n\t"
    "v_exp_f32 v86, v86\n\t"
    "v_mfma_f32_16x16x32_bf16 v[92:95], a[140:143], a[12:15], v[92:95]\n\t"
    "v_exp_f32 v87, v87\n\t"
    "v_mfma_f32_16x16x32_bf16 v[224:227], v[36:39], a[144:147], v[224:227]\n\t"
    "v_exp_f32 v88, v88\n\t"
    "v_mfma_f32_16x16x32_bf16 v[224:227], v[40:43], a[148:151], v[224:227]\n\t"
    "v_exp_f32 v89, v89\n\t"
    "v_mfma_f32_16x16x32_bf16 v[228:231], v[36:39], a[152:155], v[228:231]\n\t"
    "v_exp_f32 v90, v90\n\t"
    "v_mfma_f32_16x16x32_bf16 v[228:231], v[40:43], a[156:159], v[228:231]\n\t"
    "v_exp_f32 v91, v91\n\t"
    "v_mfma_f32_16x16x32_bf16 v[232:235], v[36:39], a[160:163], v[232:235]\n\t"
    "v_exp_f32 v92, v92\n\t"
    "v_mfma_f32_16x16x32_bf16 v[232:235], v[40:43], a[164:167], v[232:235]\n\t"
    "v_exp_f32 v93, v93\n\t"
    "v_mfma_f32_16x16x32_bf16 v[236:239], v[36:39], a[168:171], v[236:239]\n\t"
    "v_exp_f32 v94, v94\n\t"
    "v_mfma_f32_16x16x32_bf16 v[236:239], v[40:43], a[172:175], v[236:239]\n\t"
    "v_exp_f32 v95, v95\n\t"
    "v_mfma_f32_16x16x32_bf16 v[240:243], v[36:39], a[176:179], v[240:243]\n\t"
    "ds_read_b64_tr_b16 a[144:145], v12 offset:16384\n\t"
    "ds_read_b64_tr_b16 a[146:147], v12 offset:20480\n\t"
    "v_mfma_f32_16x16x32_bf16 v[240:243], v[40:43], a[180:183], v[240:243]\n\t"
    "ds_read_b64_tr_b16 a[148:149], v12 offset:24576\n\t"
    "ds_read_b64_tr_b16 a[150:151], v12 offset:28672\n\t"
    "v_mfma_f32_16x16x32_bf16 v[244:247], v[36:39], a[184:187], v[244:247]\n\t"
    "ds_read_b64_tr_b16 a[152:153], v13 offset:16384\n\t"
    "ds_read_b64_tr_b16 a[154:155], v13 offset:20480\n\t"
    "v_mfma_f32_16x16x32_bf16 v[244:247], v[40:43], a[188:191], v[244:247]\n\t"
    "ds_read_b64_tr_b16 a[156:157], v13 offset:24576\n\t"
    "ds_read_b64_tr_b16 a[158:159], v13 offset:28672\n\t"
    "v_mfma_f32_16x16x32_bf16 v[248:251], v[36:39], a[192:195], v[248:251]\n\t"
    "ds_read_b64_tr_b16 a[160:161], v14 offset:16384\n\t"
    "ds_read_b64_tr_b16 a[162:163], v14 offset:20480\n\t"
    "v_mfma_f32_16x16x32_bf16 v[248:251], v[40:43], a[196:199], v[248:251]\n\t"
    "ds_read_b64_tr_b16 a[164:165], v14 offset:24576\n\t"
    "ds_read_b64_tr_b16 a[166:167], v14 offset:28672\n\t"
    "v_mfma_f32_16x16x32_bf16 v[252:255], v[36:39], a[200:203], v[252:255]\n\t"
    "ds_read_b64_tr_b16 a[168:169], v15 offset:16384\n\t"
    "ds_read_b64_tr_b16 a[170:171], v15 offset:20480\n\t"
    "v_mfma_f32_16x16x32_bf16 v[252:255], v[40:43], a[204:207], v[252:255]\n\t"
    "ds_read_b64_tr_b16 a[172:173], v15 offset:24576\n\t"
    "ds_read_b64_tr_b16 a[174:175], v15 offset:28672\n\t"
    "v_smfmac_f32_16x16x64_bf16 a[228:231], a[208:211], v[36:43], v24\n\t"
    "v_mfma_f32_16x16x32_bf16 v[64:67], a[80:83], a[16:19], v[48:51]\n\t"
    "ds_read_b64_tr_b16 a[176:177], v16 offset:16384\n\t"
    "v_cvt_pk_bf16_f32 v36, v80, v81\n\t"
    "v_mfma_f32_16x16x32_bf16 v[64:67], a[84:87], a[20:23], v[64:67]\n\t"
    "ds_read_b64_tr_b16 a[178:179], v16 offset:20480\n\t"
    "v_cvt_pk_bf16_f32 v37, v82, v83\n\t"
    "v_mfma_f32_16x16x32_bf16 v[64:67], a[88:91], a[24:27], v[64:67]\n\t"
    "ds_read_b64_tr_b16 a[180:181], v16 offset:24576\n\t"
    "v_cvt_pk_bf16_f32 v38, v84, v85\n\t"
    "v_mfma_f32_16x16x32_bf16 v[64:67], a[92:95], a[28:31], v[64:67]\n\t"
    "ds_read_b64_tr_b16 a[182:183], v16 offset:28672\n\t"
    "v_cvt_pk_bf16_f32 v39, v86, v87\n\t"
    "v_mfma_f32_16x16x32_bf16 v[68:71], a[96:99], a[16:19], v[48:51]\n\t"
    "ds_read_b64_tr_b16 a[184:185], v17 offset:16384\n\t"
    "v_cvt_pk_bf16_f32 v40, v88, v89\n\t"
    "v_mfma_f32_16x16x32_bf16 v[68:71], a[100:103], a[20:23], v[68:71]\n\t"
    "ds_read_b64_tr_b16 a[186:187], v17 offset:20480\n\t"
    "v_cvt_pk_bf16_f32 v41, v90, v91\n\t"
    "v_mfma_f32_16x16x32_bf16 v[68:71], a[104:107], a[24:27], v[68:71]\n\t"
    "ds_read_b64_tr_b16 a[188:189], v17 offset:24576\n\t"
    "v_cvt_pk_bf16_f32 v42, v92, v93\n\t"
    "v_mfma_f32_16x16x32_bf16 v[68:71], a[108:111], a[28:31], v[68:71]\n\t"
    "ds_read_b64_tr_b16 a[190:191], v17 offset:28672\n\t"
    "v_cvt_pk_bf16_f32 v43, v94, v95\n\t"
    "v_mfma_f32_16x16x32_bf16 v[72:75], a[112:115], a[16:19], v[48:51]\n\t"
    "v_exp_f32 v64, v64\n\t"
    "v_mfma_f32_16x16x32_bf16 v[72:75], a[116:119], a[20:23], v[72:75]\n\t"
    "v_exp_f32 v65, v65\n\t"
    "v_mfma_f32_16x16x32_bf16 v[72:75], a[120:123], a[24:27], v[72:75]\n\t"
    "v_exp_f32 v66, v66\n\t"
    "v_mfma_f32_16x16x32_bf16 v[72:75], a[124:127], a[28:31], v[72:75]\n\t"
    "v_exp_f32 v67, v67\n\t"
    "v_mfma_f32_16x16x32_bf16 v[76:79], a[128:131], a[16:19], v[48:51]\n\t"
    "v_exp_f32 v68, v68\n\t"
    "v_mfma_f32_16x16x32_bf16 v[76:79], a[132:135], a[20:23], v[76:79]\n\t"
    "v_exp_f32 v69, v69\n\t"
    "v_mfma_f32_16x16x32_bf16 v[76:79], a[136:139], a[24:27], v[76:79]\n\t"
    "v_exp_f32 v70, v70\n\t"
    "v_mfma_f32_16x16x32_bf16 v[76:79], a[140:143], a[28:31], v[76:79]\n\t"
    "s_waitcnt lgkmcnt(0)\n\t"
    "v_exp_f32 v71, v71\n\t"
    "v_mfma_f32_16x16x32_bf16 v[96:99], v[36:39], a[144:147], v[96:99]\n\t"
    "v_exp_f32 v72, v72\n\t"
    "v_mfma_f32_16x16x32_bf16 v[96:99], v[40:43], a[148:151], v[96:99]\n\t"
    "v_exp_f32 v73, v73\n\t"
    "v_mfma_f32_16x16x32_bf16 v[100:103], v[36:39], a[152:155], v[100:103]\n\t"
    "v_exp_f32 v74, v74\n\t"
    "v_mfma_f32_16x16x32_bf16 v[100:103], v[40:43], a[156:159], v[100:103]\n\t"
    "v_exp_f32 v75, v75\n\t"
    "v_mfma_f32_16x16x32_bf16 v[104:107], v[36:39], a[160:163], v[104:107]\n\t"
    "v_exp_f32 v76, v76\n\t"
    "v_mfma_f32_16x16x32_bf16 v[104:107], v[40:43], a[164:167], v[104:107]\n\t"
    "v_exp_f32 v77, v77\n\t"
    "v_mfma_f32_16x16x32_bf16 v[108:111], v[36:39], a[168:171], v[108:111]\n\t"
    "v_exp_f32 v78, v78\n\t"
    "v_mfma_f32_16x16x32_bf16 v[108:111], v[40:43], a[172:175], v[108:111]\n\t"
    "v_exp_f32 v79, v79\n\t"
    "v_mfma_f32_16x16x32_bf16 v[112:115], v[36:39], a[176:179], v[112:115]\n\t"
    "v_add_u32 v35, 1, v35\n\t"
    "v_mfma_f32_16x16x32_bf16 v[112:115], v[40:43], a[180:183], v[112:115]\n\t"
    "ds_write_b32 v27, v35 offset:6144\n\t"
    "v_mfma_f32_16x16x32_bf16 v[116:119], v[36:39], a[184:187], v[116:119]\n\t"
    "v_mfma_f32_16x16x32_bf16 v[116:119], v[40:43], a[188:191], v[116:119]\n\t"
    "v_mfma_f32_16x16x32_bf16 v[120:123], v[36:39], a[240:243], v[120:123]\n\t"
    "v_mfma_f32_16x16x32_bf16 v[120:123], v[40:43], a[244:247], v[120:123]\n\t"
    "v_mfma_f32_16x16x32_bf16 v[124:127], v[36:39], a[248:251], v[124:127]\n\t"
    "v_mfma_f32_16x16x32_bf16 v[124:127], v[40:43], a[252:255], v[124:127]\n\t"
    "ds_read_b32 v33, v27 offset:3072\n\t"
    "v_smfmac_f32_16x16x64_bf16 a[212:215], a[208:211], v[36:43], v24\n\t"
    "v_mfma_f32_16x16x32_bf16 v[80:83], a[80:83], a[32:35], v[52:55]\n\t"
    "v_cvt_pk_bf16_f32 v36, v64, v65\n\t"
    "v_mfma_f32_16x16x32_bf16 v[80:83], a[84:87], a[36:39], v[80:83]\n\t"
    "v_cvt_pk_bf16_f32 v37, v66, v67\n\t"
    "v_mfma_f32_16x16x32_bf16 v[80:83], a[88:91], a[40:43], v[80:83]\n\t"
    "v_cvt_pk_bf16_f32 v38, v68, v69\n\t"
    "v_mfma_f32_16x16x32_bf16 v[80:83], a[92:95], a[44:47], v[80:83]\n\t"
    "v_cvt_pk_bf16_f32 v39, v70, v71\n\t"
    "v_mfma_f32_16x16x32_bf16 v[84:87], a[96:99], a[32:35], v[52:55]\n\t"
    "v_cvt_pk_bf16_f32 v40, v72, v73\n\t"
    "v_mfma_f32_16x16x32_bf16 v[84:87], a[100:103], a[36:39], v[84:87]\n\t"
    "v_cvt_pk_bf16_f32 v41, v74, v75\n\t"
    "v_mfma_f32_16x16x32_bf16 v[84:87], a[104:107], a[40:43], v[84:87]\n\t"
    "v_cvt_pk_bf16_f32 v42, v76, v77\n\t"
    "v_mfma_f32_16x16x32_bf16 v[84:87], a[108:111], a[44:47], v[84:87]\n\t"
    "v_cvt_pk_bf16_f32 v43, v78, v79\n\t"
    "v_mfma_f32_16x16x32_bf16 v[88:91], a[112:115], a[32:35], v[52:55]\n\t"
    "v_exp_f32 v80, v80\n\t"
    "v_mfma_f32_16x16x32_bf16 v[88:91], a[116:119], a[36:39], v[88:91]\n\t"
    "v_exp_f32 v81, v81\n\t"
    "v_mfma_f32_16x16x32_bf16 v[88:91], a[120:123], a[40:43], v[88:91]\n\t"
    "v_exp_f32 v82, v82\n\t"
    "v_mfma_f32_16x16x32_bf16 v[88:91], a[124:127], a[44:47], v[88:91]\n\t"
    "v_exp_f32 v83, v83\n\t"
    "v_mfma_f32_16x16x32_bf16 v[92:95], a[128:131], a[32:35], v[52:55]\n\t"
    "v_exp_f32 v84, v84\n\t"
    "v_mfma_f32_16x16x32_bf16 v[92:95], a[132:135], a[36:39], v[92:95]\n\t"
    "v_exp_f32 v85, v85\n\t"
    "v_mfma_f32_16x16x32_bf16 v[92:95], a[136:139], a[40:43], v[92:95]\n\t"
    "v_exp_f32 v86, v86\n\t"
    "v_mfma_f32_16x16x32_bf16 v[92:95], a[140:143], a[44:47], v[92:95]\n\t"
    "v_exp_f32 v87, v87\n\t"
    "v_mfma_f32_16x16x32_bf16 v[128:131], v[36:39], a[144:147], v[128:131]\n\t"
    "v_exp_f32 v88, v88\n\t"
    "v_mfma_f32_16x16x32_bf16 v[128:131], v[40:43], a[148:151], v[128:131]\n\t"
    "v_exp_f32 v89, v89\n\t"
    "v_mfma_f32_16x16x32_bf16 v[132:135], v[36:39], a[152:155], v[132:135]\n\t"
    "v_exp_f32 v90, v90\n\t"
    "v_mfma_f32_16x16x32_bf16 v[132:135], v[40:43], a[156:159], v[132:135]\n\t"
    "v_exp_f32 v91, v91\n\t"
    "v_mfma_f32_16x16x32_bf16 v[136:139], v[36:39], a[160:163], v[136:139]\n\t"
    "v_exp_f32 v92, v92\n\t"
    "v_mfma_f32_16x16x32_bf16 v[136:139], v[40:43], a[164:167], v[136:139]\n\t"
    "v_exp_f32 v93, v93\n\t"
    "v_mfma_f32_16x16x32_bf16 v[140:143], v[36:39], a[168:171], v[140:143]\n\t"
    "v_exp_f32 v94, v94\n\t"
    "v_mfma_f32_16x16x32_bf16 v[140:143], v[40:43], a[172:175], v[140:143]\n\t"
    "v_exp_f32 v95, v95\n\t"
    "v_mfma_f32_16x16x32_bf16 v[144:147], v[36:39], a[176:179], v[144:147]\n\t"
    "v_mfma_f32_16x16x32_bf16 v[144:147], v[40:43], a[180:183], v[144:147]\n\t"
    "v_mfma_f32_16x16x32_bf16 v[148:151], v[36:39], a[184:187], v[148:151]\n\t"
    "s_waitcnt lgkmcnt(0)\n\t"
    "v_cmp_gt_u32 vcc, v35, v33\n\t"
    "v_mfma_f32_16x16x32_bf16 v[148:151], v[40:43], a[188:191], v[148:151]\n\t"
    "v_mfma_f32_16x16x32_bf16 v[152:155], v[36:39], a[240:243], v[152:155]\n\t"
    "v_mfma_f32_16x16x32_bf16 v[152:155], v[40:43], a[244:247], v[152:155]\n\t"
    "v_mfma_f32_16x16x32_bf16 v[156:159], v[36:39], a[248:251], v[156:159]\n\t"
    "s_cbranch_vccnz SPIN5L1%=\n\t"
    "RET5L1%=:\n\t"
    "v_mfma_f32_16x16x32_bf16 v[156:159], v[40:43], a[252:255], v[156:159]\n\t"
    "v_smfmac_f32_16x16x64_bf16 a[216:219], a[208:211], v[36:43], v24\n\t"
    "v_mfma_f32_16x16x32_bf16 v[64:67], a[80:83], a[48:51], v[56:59]\n\t"
    "ds_read_b64_tr_b16 a[192:193], v18 offset:32768\n\t"
    "v_cvt_pk_bf16_f32 v36, v80, v81\n\t"
    "v_mfma_f32_16x16x32_bf16 v[64:67], a[84:87], a[52:55], v[64:67]\n\t"
    "ds_read_b64_tr_b16 a[194:195], v18 offset:36864\n\t"
    "v_cvt_pk_bf16_f32 v37, v82, v83\n\t"
    "v_mfma_f32_16x16x32_bf16 v[64:67], a[88:91], a[56:59], v[64:67]\n\t"
    "ds_read_b64_tr_b16 a[196:197], v18 offset:40960\n\t"
    "v_cvt_pk_bf16_f32 v38, v84, v85\n\t"
    "v_mfma_f32_16x16x32_bf16 v[64:67], a[92:95], a[60:63], v[64:67]\n\t"
    "ds_read_b64_tr_b16 a[198:199], v18 offset:45056\n\t"
    "v_cvt_pk_bf16_f32 v39, v86, v87\n\t"
    "v_mfma_f32_16x16x32_bf16 v[68:71], a[96:99], a[48:51], v[56:59]\n\t"
    "ds_read_b64_tr_b16 a[200:201], v19 offset:32768\n\t"
    "v_cvt_pk_bf16_f32 v40, v88, v89\n\t"
    "v_mfma_f32_16x16x32_bf16 v[68:71], a[100:103], a[52:55], v[68:71]\n\t"
    "ds_read_b64_tr_b16 a[202:203], v19 offset:36864\n\t"
    "v_cvt_pk_bf16_f32 v41, v90, v91\n\t"
    "v_mfma_f32_16x16x32_bf16 v[68:71], a[104:107], a[56:59], v[68:71]\n\t"
    "ds_read_b64_tr_b16 a[204:205], v19 offset:40960\n\t"
    "v_cvt_pk_bf16_f32 v42, v92, v93\n\t"
    "v_mfma_f32_16x16x32_bf16 v[68:71], a[108:111], a[60:63], v[68:71]\n\t"
    "ds_read_b64_tr_b16 a[206:207], v19 offset:45056\n\t"
    "v_cvt_pk_bf16_f32 v43, v94, v95\n\t"
    "v_mfma_f32_16x16x32_bf16 v[72:75], a[112:115], a[48:51], v[56:59]\n\t"
    "v_exp_f32 v64, v64\n\t"
    "v_mfma_f32_16x16x32_bf16 v[72:75], a[116:119], a[52:55], v[72:75]\n\t"
    "v_exp_f32 v65, v65\n\t"
    "v_mfma_f32_16x16x32_bf16 v[72:75], a[120:123], a[56:59], v[72:75]\n\t"
    "v_exp_f32 v66, v66\n\t"
    "v_mfma_f32_16x16x32_bf16 v[72:75], a[124:127], a[60:63], v[72:75]\n\t"
    "v_exp_f32 v67, v67\n\t"
    "v_mfma_f32_16x16x32_bf16 v[76:79], a[128:131], a[48:51], v[56:59]\n\t"
    "v_exp_f32 v68, v68\n\t"
    "v_mfma_f32_16x16x32_bf16 v[76:79], a[132:135], a[52:55], v[76:79]\n\t"
    "v_exp_f32 v69, v69\n\t"
    "v_mfma_f32_16x16x32_bf16 v[76:79], a[136:139], a[56:59], v[76:79]\n\t"
    "v_exp_f32 v70, v70\n\t"
    "v_mfma_f32_16x16x32_bf16 v[76:79], a[140:143], a[60:63], v[76:79]\n\t"
    "v_exp_f32 v71, v71\n\t"
    "v_mfma_f32_16x16x32_bf16 v[160:163], v[36:39], a[144:147], v[160:163]\n\t"
    "v_exp_f32 v72, v72\n\t"
    "v_mfma_f32_16x16x32_bf16 v[160:163], v[40:43], a[148:151], v[160:163]\n\t"
    "v_exp_f32 v73, v73\n\t"
    "v_mfma_f32_16x16x32_bf16 v[164:167], v[36:39], a[152:155], v[164:167]\n\t"
    "v_exp_f32 v74, v74\n\t"
    "v_mfma_f32_16x16x32_bf16 v[164:167], v[40:43], a[156:159], v[164:167]\n\t"
    "v_exp_f32 v75, v75\n\t"
    "v_mfma_f32_16x16x32_bf16 v[168:171], v[36:39], a[160:163], v[168:171]\n\t"
    "v_exp_f32 v76, v76\n\t"
    "v_mfma_f32_16x16x32_bf16 v[168:171], v[40:43], a[164:167], v[168:171]\n\t"
    "v_exp_f32 v77, v77\n\t"
    "v_mfma_f32_16x16x32_bf16 v[172:175], v[36:39], a[168:171], v[172:175]\n\t"
    "v_exp_f32 v78, v78\n\t"
    "v_mfma_f32_16x16x32_bf16 v[172:175], v[40:43], a[172:175], v[172:175]\n\t"
    "v_exp_f32 v79, v79\n\t"
    "v_mfma_f32_16x16x32_bf16 v[176:179], v[36:39], a[176:179], v[176:179]\n\t"
    "v_mfma_f32_16x16x32_bf16 v[176:179], v[40:43], a[180:183], v[176:179]\n\t"
    "v_mfma_f32_16x16x32_bf16 v[180:183], v[36:39], a[184:187], v[180:183]\n\t"
    "v_mfma_f32_16x16x32_bf16 v[180:183], v[40:43], a[188:191], v[180:183]\n\t"
    "v_mfma_f32_16x16x32_bf16 v[184:187], v[36:39], a[240:243], v[184:187]\n\t"
    "v_mfma_f32_16x16x32_bf16 v[184:187], v[40:43], a[244:247], v[184:187]\n\t"
    "v_mfma_f32_16x16x32_bf16 v[188:191], v[36:39], a[248:251], v[188:191]\n\t"
    "v_mfma_f32_16x16x32_bf16 v[188:191], v[40:43], a[252:255], v[188:191]\n\t"
    "v_smfmac_f32_16x16x64_bf16 a[220:223], a[208:211], v[36:43], v24\n\t"
    "v_mfma_f32_16x16x32_bf16 v[84:87], a[96:99], a[64:67], v[60:63]\n\t"
    "ds_read_b128 a[80:83], v8 offset:32768\n\t"
    "v_cvt_pk_bf16_f32 v36, v64, v65\n\t"
    "v_mfma_f32_16x16x32_bf16 v[84:87], a[100:103], a[68:71], v[84:87]\n\t"
    "ds_read_b128 a[84:87], v9 offset:32768\n\t"
    "v_cvt_pk_bf16_f32 v37, v66, v67\n\t"
    "v_mfma_f32_16x16x32_bf16 v[84:87], a[104:107], a[72:75], v[84:87]\n\t"
    "ds_read_b128 a[88:91], v10 offset:32768\n\t"
    "v_cvt_pk_bf16_f32 v38, v68, v69\n\t"
    "v_mfma_f32_16x16x32_bf16 v[84:87], a[108:111], a[76:79], v[84:87]\n\t"
    "ds_read_b128 a[92:95], v11 offset:32768\n\t"
    "v_cvt_pk_bf16_f32 v39, v70, v71\n\t"
    "v_mfma_f32_16x16x32_bf16 v[88:91], a[112:115], a[64:67], v[60:63]\n\t"
    "ds_read_b128 a[96:99], v8 offset:36864\n\t"
    "v_cvt_pk_bf16_f32 v40, v72, v73\n\t"
    "v_mfma_f32_16x16x32_bf16 v[88:91], a[116:119], a[68:71], v[88:91]\n\t"
    "ds_read_b128 a[100:103], v9 offset:36864\n\t"
    "v_cvt_pk_bf16_f32 v41, v74, v75\n\t"
    "v_mfma_f32_16x16x32_bf16 v[88:91], a[120:123], a[72:75], v[88:91]\n\t"
    "ds_read_b128 a[104:107], v10 offset:36864\n\t"
    "v_cvt_pk_bf16_f32 v42, v76, v77\n\t"
    "v_mfma_f32_16x16x32_bf16 v[88:91], a[124:127], a[76:79], v[88:91]\n\t"
    "ds_read_b128 a[108:111], v11 offset:36864\n\t"
    "v_cvt_pk_bf16_f32 v43, v78, v79\n\t"
    "v_mfma_f32_16x16x32_bf16 v[92:95], a[128:131], a[64:67], v[60:63]\n\t"
    "v_exp_f32 v84, v84\n\t"
    "v_mfma_f32_16x16x32_bf16 v[92:95], a[132:135], a[68:71], v[92:95]\n\t"
    "v_exp_f32 v85, v85\n\t"
    "v_mfma_f32_16x16x32_bf16 v[92:95], a[136:139], a[72:75], v[92:95]\n\t"
    "v_exp_f32 v86, v86\n\t"
    "v_mfma_f32_16x16x32_bf16 v[92:95], a[140:143], a[76:79], v[92:95]\n\t"
    "v_exp_f32 v87, v87\n\t"
    "v_mfma_f32_16x16x32_bf16 v[192:195], v[36:39], a[144:147], v[192:195]\n\t"
    "v_exp_f32 v88, v88\n\t"
    "v_mfma_f32_16x16x32_bf16 v[192:195], v[40:43], a[148:151], v[192:195]\n\t"
    "v_exp_f32 v89, v89\n\t"
    "v_mfma_f32_16x16x32_bf16 v[196:199], v[36:39], a[152:155], v[196:199]\n\t"
    "v_exp_f32 v90, v90\n\t"
    "v_mfma_f32_16x16x32_bf16 v[196:199], v[40:43], a[156:159], v[196:199]\n\t"
    "v_exp_f32 v91, v91\n\t"
    "v_mfma_f32_16x16x32_bf16 v[200:203], v[36:39], a[160:163], v[200:203]\n\t"
    "v_exp_f32 v92, v92\n\t"
    "v_mfma_f32_16x16x32_bf16 v[200:203], v[40:43], a[164:167], v[200:203]\n\t"
    "v_exp_f32 v93, v93\n\t"
    "v_mfma_f32_16x16x32_bf16 v[204:207], v[36:39], a[168:171], v[204:207]\n\t"
    "v_exp_f32 v94, v94\n\t"
    "v_mfma_f32_16x16x32_bf16 v[204:207], v[40:43], a[172:175], v[204:207]\n\t"
    "v_exp_f32 v95, v95\n\t"
    "v_mfma_f32_16x16x32_bf16 v[208:211], v[36:39], a[176:179], v[208:211]\n\t"
    "ds_read_b128 a[112:115], v8 offset:40960\n\t"
    "v_mfma_f32_16x16x32_bf16 v[208:211], v[40:43], a[180:183], v[208:211]\n\t"
    "ds_read_b128 a[116:119], v9 offset:40960\n\t"
    "v_mfma_f32_16x16x32_bf16 v[212:215], v[36:39], a[184:187], v[212:215]\n\t"
    "ds_read_b128 a[120:123], v10 offset:40960\n\t"
    "v_mfma_f32_16x16x32_bf16 v[212:215], v[40:43], a[188:191], v[212:215]\n\t"
    "ds_read_b128 a[124:127], v11 offset:40960\n\t"
    "v_mfma_f32_16x16x32_bf16 v[216:219], v[36:39], a[240:243], v[216:219]\n\t"
    "ds_read_b128 a[128:131], v8 offset:45056\n\t"
    "v_mfma_f32_16x16x32_bf16 v[216:219], v[40:43], a[244:247], v[216:219]\n\t"
    "ds_read_b128 a[132:135], v9 offset:45056\n\t"
    "v_mfma_f32_16x16x32_bf16 v[220:223], v[36:39], a[248:251], v[220:223]\n\t"
    "ds_read_b128 a[136:139], v10 offset:45056\n\t"
    "v_mfma_f32_16x16x32_bf16 v[220:223], v[40:43], a[252:255], v[220:223]\n\t"
    "ds_read_b128 a[140:143], v11 offset:45056\n\t"
    "v_smfmac_f32_16x16x64_bf16 a[224:227], a[208:211], v[36:43], v24\n\t"
    "s_waitcnt lgkmcnt(7)\n\t"
    "v_mfma_f32_16x16x32_bf16 v[64:67], a[80:83], a[0:3], v[44:47]\n\t"
    "ds_read_b64 v[36:37], v27 offset:512\n\t"
    "v_mfma_f32_16x16x32_bf16 v[64:67], a[84:87], a[4:7], v[64:67]\n\t"
    "v_mfma_f32_16x16x32_bf16 v[64:67], a[88:91], a[8:11], v[64:67]\n\t"
    "v_cvt_pk_bf16_f32 v38, v84, v85\n\t"
    "v_mfma_f32_16x16x32_bf16 v[64:67], a[92:95], a[12:15], v[64:67]\n\t"
    "v_cvt_pk_bf16_f32 v39, v86, v87\n\t"
    "v_mfma_f32_16x16x32_bf16 v[68:71], a[96:99], a[0:3], v[44:47]\n\t"
    "v_cvt_pk_bf16_f32 v40, v88, v89\n\t"
    "v_mfma_f32_16x16x32_bf16 v[68:71], a[100:103], a[4:7], v[68:71]\n\t"
    "v_cvt_pk_bf16_f32 v41, v90, v91\n\t"
    "v_mfma_f32_16x16x32_bf16 v[68:71], a[104:107], a[8:11], v[68:71]\n\t"
    "v_cvt_pk_bf16_f32 v42, v92, v93\n\t"
    "v_mfma_f32_16x16x32_bf16 v[68:71], a[108:111], a[12:15], v[68:71]\n\t"
    "v_cvt_pk_bf16_f32 v43, v94, v95\n\t"
    "s_sub_u32 s57, s57, 1\n\t"
    "s_cmp_lg_u32 s57, 0\n\t"
    "s_cbranch_scc1 LOOPA%=\n\t"
    "; TILE 30\n\t"
    "v_mfma_f32_16x16x32_bf16 v[72:75], a[112:115], a[0:3], v[44:47]\n\t"
    "s_waitcnt lgkmcnt(0)\n\t"
    "v_exp_f32 v64, v64\n\t"
    "v_mfma_f32_16x16x32_bf16 v[72:75], a[116:119], a[4:7], v[72:75]\n\t"
    "v_exp_f32 v65, v65\n\t"
    "v_mfma_f32_16x16x32_bf16 v[72:75], a[120:123], a[8:11], v[72:75]\n\t"
    "v_exp_f32 v66, v66\n\t"
    "v_mfma_f32_16x16x32_bf16 v[72:75], a[124:127], a[12:15], v[72:75]\n\t"
    "v_exp_f32 v67, v67\n\t"
    "v_mfma_f32_16x16x32_bf16 v[76:79], a[128:131], a[0:3], v[44:47]\n\t"
    "v_exp_f32 v68, v68\n\t"
    "v_mfma_f32_16x16x32_bf16 v[76:79], a[132:135], a[4:7], v[76:79]\n\t"
    "v_exp_f32 v69, v69\n\t"
    "v_mfma_f32_16x16x32_bf16 v[76:79], a[136:139], a[8:11], v[76:79]\n\t"
    "v_exp_f32 v70, v70\n\t"
    "v_mfma_f32_16x16x32_bf16 v[76:79], a[140:143], a[12:15], v[76:79]\n\t"
    "v_exp_f32 v71, v71\n\t"
    "v_mfma_f32_16x16x32_bf16 v[224:227], v[36:39], a[144:147], v[224:227]\n\t"
    "v_exp_f32 v72, v72\n\t"
    "v_mfma_f32_16x16x32_bf16 v[224:227], v[40:43], a[148:151], v[224:227]\n\t"
    "v_exp_f32 v73, v73\n\t"
    "v_mfma_f32_16x16x32_bf16 v[228:231], v[36:39], a[152:155], v[228:231]\n\t"
    "v_exp_f32 v74, v74\n\t"
    "v_mfma_f32_16x16x32_bf16 v[228:231], v[40:43], a[156:159], v[228:231]\n\t"
    "v_exp_f32 v75, v75\n\t"
    "v_mfma_f32_16x16x32_bf16 v[232:235], v[36:39], a[160:163], v[232:235]\n\t"
    "v_exp_f32 v76, v76\n\t"
    "v_mfma_f32_16x16x32_bf16 v[232:235], v[40:43], a[164:167], v[232:235]\n\t"
    "v_exp_f32 v77, v77\n\t"
    "v_mfma_f32_16x16x32_bf16 v[236:239], v[36:39], a[168:171], v[236:239]\n\t"
    "v_exp_f32 v78, v78\n\t"
    "v_mfma_f32_16x16x32_bf16 v[236:239], v[40:43], a[172:175], v[236:239]\n\t"
    "v_exp_f32 v79, v79\n\t"
    "v_mfma_f32_16x16x32_bf16 v[240:243], v[36:39], a[176:179], v[240:243]\n\t"
    "ds_read_b64_tr_b16 a[144:145], v12 offset:32768\n\t"
    "ds_read_b64_tr_b16 a[146:147], v12 offset:36864\n\t"
    "v_mfma_f32_16x16x32_bf16 v[240:243], v[40:43], a[180:183], v[240:243]\n\t"
    "ds_read_b64_tr_b16 a[148:149], v12 offset:40960\n\t"
    "ds_read_b64_tr_b16 a[150:151], v12 offset:45056\n\t"
    "v_mfma_f32_16x16x32_bf16 v[244:247], v[36:39], a[184:187], v[244:247]\n\t"
    "ds_read_b64_tr_b16 a[152:153], v13 offset:32768\n\t"
    "ds_read_b64_tr_b16 a[154:155], v13 offset:36864\n\t"
    "v_mfma_f32_16x16x32_bf16 v[244:247], v[40:43], a[188:191], v[244:247]\n\t"
    "ds_read_b64_tr_b16 a[156:157], v13 offset:40960\n\t"
    "ds_read_b64_tr_b16 a[158:159], v13 offset:45056\n\t"
    "v_mfma_f32_16x16x32_bf16 v[248:251], v[36:39], a[240:243], v[248:251]\n\t"
    "ds_read_b64_tr_b16 a[160:161], v14 offset:32768\n\t"
    "ds_read_b64_tr_b16 a[162:163], v14 offset:36864\n\t"
    "v_mfma_f32_16x16x32_bf16 v[248:251], v[40:43], a[244:247], v[248:251]\n\t"
    "ds_read_b64_tr_b16 a[164:165], v14 offset:40960\n\t"
    "ds_read_b64_tr_b16 a[166:167], v14 offset:45056\n\t"
    "v_mfma_f32_16x16x32_bf16 v[252:255], v[36:39], a[248:251], v[252:255]\n\t"
    "ds_read_b64_tr_b16 a[168:169], v15 offset:32768\n\t"
    "ds_read_b64_tr_b16 a[170:171], v15 offset:36864\n\t"
    "v_mfma_f32_16x16x32_bf16 v[252:255], v[40:43], a[252:255], v[252:255]\n\t"
    "ds_read_b64_tr_b16 a[172:173], v15 offset:40960\n\t"
    "ds_read_b64_tr_b16 a[174:175], v15 offset:45056\n\t"
    "v_smfmac_f32_16x16x64_bf16 a[228:231], a[208:211], v[36:43], v24\n\t"
    "v_mfma_f32_16x16x32_bf16 v[80:83], a[80:83], a[16:19], v[48:51]\n\t"
    "ds_read_b64_tr_b16 a[176:177], v16 offset:32768\n\t"
    "v_cvt_pk_bf16_f32 v36, v64, v65\n\t"
    "v_mfma_f32_16x16x32_bf16 v[80:83], a[84:87], a[20:23], v[80:83]\n\t"
    "ds_read_b64_tr_b16 a[178:179], v16 offset:36864\n\t"
    "v_cvt_pk_bf16_f32 v37, v66, v67\n\t"
    "v_mfma_f32_16x16x32_bf16 v[80:83], a[88:91], a[24:27], v[80:83]\n\t"
    "ds_read_b64_tr_b16 a[180:181], v16 offset:40960\n\t"
    "v_cvt_pk_bf16_f32 v38, v68, v69\n\t"
    "v_mfma_f32_16x16x32_bf16 v[80:83], a[92:95], a[28:31], v[80:83]\n\t"
    "ds_read_b64_tr_b16 a[182:183], v16 offset:45056\n\t"
    "v_cvt_pk_bf16_f32 v39, v70, v71\n\t"
    "v_mfma_f32_16x16x32_bf16 v[84:87], a[96:99], a[16:19], v[48:51]\n\t"
    "ds_read_b64_tr_b16 a[184:185], v17 offset:32768\n\t"
    "v_cvt_pk_bf16_f32 v40, v72, v73\n\t"
    "v_mfma_f32_16x16x32_bf16 v[84:87], a[100:103], a[20:23], v[84:87]\n\t"
    "ds_read_b64_tr_b16 a[186:187], v17 offset:36864\n\t"
    "v_cvt_pk_bf16_f32 v41, v74, v75\n\t"
    "v_mfma_f32_16x16x32_bf16 v[84:87], a[104:107], a[24:27], v[84:87]\n\t"
    "ds_read_b64_tr_b16 a[188:189], v17 offset:40960\n\t"
    "v_cvt_pk_bf16_f32 v42, v76, v77\n\t"
    "v_mfma_f32_16x16x32_bf16 v[84:87], a[108:111], a[28:31], v[84:87]\n\t"
    "ds_read_b64_tr_b16 a[190:191], v17 offset:45056\n\t"
    "v_cvt_pk_bf16_f32 v43, v78, v79\n\t"
    "v_mfma_f32_16x16x32_bf16 v[88:91], a[112:115], a[16:19], v[48:51]\n\t"
    "v_exp_f32 v80, v80\n\t"
    "v_mfma_f32_16x16x32_bf16 v[88:91], a[116:119], a[20:23], v[88:91]\n\t"
    "v_exp_f32 v81, v81\n\t"
    "v_mfma_f32_16x16x32_bf16 v[88:91], a[120:123], a[24:27], v[88:91]\n\t"
    "v_exp_f32 v82, v82\n\t"
    "v_mfma_f32_16x16x32_bf16 v[88:91], a[124:127], a[28:31], v[88:91]\n\t"
    "v_exp_f32 v83, v83\n\t"
    "v_mfma_f32_16x16x32_bf16 v[92:95], a[128:131], a[16:19], v[48:51]\n\t"
    "v_exp_f32 v84, v84\n\t"
    "v_mfma_f32_16x16x32_bf16 v[92:95], a[132:135], a[20:23], v[92:95]\n\t"
    "v_exp_f32 v85, v85\n\t"
    "v_mfma_f32_16x16x32_bf16 v[92:95], a[136:139], a[24:27], v[92:95]\n\t"
    "v_exp_f32 v86, v86\n\t"
    "v_mfma_f32_16x16x32_bf16 v[92:95], a[140:143], a[28:31], v[92:95]\n\t"
    "s_waitcnt lgkmcnt(0)\n\t"
    "v_exp_f32 v87, v87\n\t"
    "v_mfma_f32_16x16x32_bf16 v[96:99], v[36:39], a[144:147], v[96:99]\n\t"
    "v_exp_f32 v88, v88\n\t"
    "v_mfma_f32_16x16x32_bf16 v[96:99], v[40:43], a[148:151], v[96:99]\n\t"
    "v_exp_f32 v89, v89\n\t"
    "v_mfma_f32_16x16x32_bf16 v[100:103], v[36:39], a[152:155], v[100:103]\n\t"
    "v_exp_f32 v90, v90\n\t"
    "v_mfma_f32_16x16x32_bf16 v[100:103], v[40:43], a[156:159], v[100:103]\n\t"
    "v_exp_f32 v91, v91\n\t"
    "v_mfma_f32_16x16x32_bf16 v[104:107], v[36:39], a[160:163], v[104:107]\n\t"
    "v_exp_f32 v92, v92\n\t"
    "v_mfma_f32_16x16x32_bf16 v[104:107], v[40:43], a[164:167], v[104:107]\n\t"
    "v_exp_f32 v93, v93\n\t"
    "v_mfma_f32_16x16x32_bf16 v[108:111], v[36:39], a[168:171], v[108:111]\n\t"
    "v_exp_f32 v94, v94\n\t"
    "v_mfma_f32_16x16x32_bf16 v[108:111], v[40:43], a[172:175], v[108:111]\n\t"
    "v_exp_f32 v95, v95\n\t"
    "v_mfma_f32_16x16x32_bf16 v[112:115], v[36:39], a[176:179], v[112:115]\n\t"
    "v_add_u32 v35, 1, v35\n\t"
    "v_mfma_f32_16x16x32_bf16 v[112:115], v[40:43], a[180:183], v[112:115]\n\t"
    "ds_write_b32 v27, v35 offset:6144\n\t"
    "v_mfma_f32_16x16x32_bf16 v[116:119], v[36:39], a[184:187], v[116:119]\n\t"
    "v_mfma_f32_16x16x32_bf16 v[116:119], v[40:43], a[188:191], v[116:119]\n\t"
    "v_mfma_f32_16x16x32_bf16 v[120:123], v[36:39], a[192:195], v[120:123]\n\t"
    "v_mfma_f32_16x16x32_bf16 v[120:123], v[40:43], a[196:199], v[120:123]\n\t"
    "v_mfma_f32_16x16x32_bf16 v[124:127], v[36:39], a[200:203], v[124:127]\n\t"
    "v_mfma_f32_16x16x32_bf16 v[124:127], v[40:43], a[204:207], v[124:127]\n\t"
    "ds_read_b32 v33, v27 offset:3072\n\t"
    "v_smfmac_f32_16x16x64_bf16 a[212:215], a[208:211], v[36:43], v24\n\t"
    "v_mfma_f32_16x16x32_bf16 v[64:67], a[80:83], a[32:35], v[52:55]\n\t"
    "v_cvt_pk_bf16_f32 v36, v80, v81\n\t"
    "v_mfma_f32_16x16x32_bf16 v[64:67], a[84:87], a[36:39], v[64:67]\n\t"
    "v_cvt_pk_bf16_f32 v37, v82, v83\n\t"
    "v_mfma_f32_16x16x32_bf16 v[64:67], a[88:91], a[40:43], v[64:67]\n\t"
    "v_cvt_pk_bf16_f32 v38, v84, v85\n\t"
    "v_mfma_f32_16x16x32_bf16 v[64:67], a[92:95], a[44:47], v[64:67]\n\t"
    "v_cvt_pk_bf16_f32 v39, v86, v87\n\t"
    "v_mfma_f32_16x16x32_bf16 v[68:71], a[96:99], a[32:35], v[52:55]\n\t"
    "v_cvt_pk_bf16_f32 v40, v88, v89\n\t"
    "v_mfma_f32_16x16x32_bf16 v[68:71], a[100:103], a[36:39], v[68:71]\n\t"
    "v_cvt_pk_bf16_f32 v41, v90, v91\n\t"
    "v_mfma_f32_16x16x32_bf16 v[68:71], a[104:107], a[40:43], v[68:71]\n\t"
    "v_cvt_pk_bf16_f32 v42, v92, v93\n\t"
    "v_mfma_f32_16x16x32_bf16 v[68:71], a[108:111], a[44:47], v[68:71]\n\t"
    "v_cvt_pk_bf16_f32 v43, v94, v95\n\t"
    "v_mfma_f32_16x16x32_bf16 v[72:75], a[112:115], a[32:35], v[52:55]\n\t"
    "v_exp_f32 v64, v64\n\t"
    "v_mfma_f32_16x16x32_bf16 v[72:75], a[116:119], a[36:39], v[72:75]\n\t"
    "v_exp_f32 v65, v65\n\t"
    "v_mfma_f32_16x16x32_bf16 v[72:75], a[120:123], a[40:43], v[72:75]\n\t"
    "v_exp_f32 v66, v66\n\t"
    "v_mfma_f32_16x16x32_bf16 v[72:75], a[124:127], a[44:47], v[72:75]\n\t"
    "v_exp_f32 v67, v67\n\t"
    "v_mfma_f32_16x16x32_bf16 v[76:79], a[128:131], a[32:35], v[52:55]\n\t"
    "v_exp_f32 v68, v68\n\t"
    "v_mfma_f32_16x16x32_bf16 v[76:79], a[132:135], a[36:39], v[76:79]\n\t"
    "v_exp_f32 v69, v69\n\t"
    "v_mfma_f32_16x16x32_bf16 v[76:79], a[136:139], a[40:43], v[76:79]\n\t"
    "v_exp_f32 v70, v70\n\t"
    "v_mfma_f32_16x16x32_bf16 v[76:79], a[140:143], a[44:47], v[76:79]\n\t"
    "v_exp_f32 v71, v71\n\t"
    "v_mfma_f32_16x16x32_bf16 v[128:131], v[36:39], a[144:147], v[128:131]\n\t"
    "v_exp_f32 v72, v72\n\t"
    "v_mfma_f32_16x16x32_bf16 v[128:131], v[40:43], a[148:151], v[128:131]\n\t"
    "v_exp_f32 v73, v73\n\t"
    "v_mfma_f32_16x16x32_bf16 v[132:135], v[36:39], a[152:155], v[132:135]\n\t"
    "v_exp_f32 v74, v74\n\t"
    "v_mfma_f32_16x16x32_bf16 v[132:135], v[40:43], a[156:159], v[132:135]\n\t"
    "v_exp_f32 v75, v75\n\t"
    "v_mfma_f32_16x16x32_bf16 v[136:139], v[36:39], a[160:163], v[136:139]\n\t"
    "v_exp_f32 v76, v76\n\t"
    "v_mfma_f32_16x16x32_bf16 v[136:139], v[40:43], a[164:167], v[136:139]\n\t"
    "v_exp_f32 v77, v77\n\t"
    "v_mfma_f32_16x16x32_bf16 v[140:143], v[36:39], a[168:171], v[140:143]\n\t"
    "v_exp_f32 v78, v78\n\t"
    "v_mfma_f32_16x16x32_bf16 v[140:143], v[40:43], a[172:175], v[140:143]\n\t"
    "v_exp_f32 v79, v79\n\t"
    "v_mfma_f32_16x16x32_bf16 v[144:147], v[36:39], a[176:179], v[144:147]\n\t"
    "v_mfma_f32_16x16x32_bf16 v[144:147], v[40:43], a[180:183], v[144:147]\n\t"
    "v_mfma_f32_16x16x32_bf16 v[148:151], v[36:39], a[184:187], v[148:151]\n\t"
    "s_waitcnt lgkmcnt(0)\n\t"
    "v_cmp_gt_u32 vcc, v35, v33\n\t"
    "v_mfma_f32_16x16x32_bf16 v[148:151], v[40:43], a[188:191], v[148:151]\n\t"
    "v_mfma_f32_16x16x32_bf16 v[152:155], v[36:39], a[192:195], v[152:155]\n\t"
    "v_mfma_f32_16x16x32_bf16 v[152:155], v[40:43], a[196:199], v[152:155]\n\t"
    "v_mfma_f32_16x16x32_bf16 v[156:159], v[36:39], a[200:203], v[156:159]\n\t"
    "s_cbranch_vccnz SPIN5P30%=\n\t"
    "RET5P30%=:\n\t"
    "v_mfma_f32_16x16x32_bf16 v[156:159], v[40:43], a[204:207], v[156:159]\n\t"
    "v_smfmac_f32_16x16x64_bf16 a[216:219], a[208:211], v[36:43], v24\n\t"
    "v_mfma_f32_16x16x32_bf16 v[80:83], a[80:83], a[48:51], v[56:59]\n\t"
    "ds_read_b64_tr_b16 a[240:241], v18 offset:49152\n\t"
    "v_cvt_pk_bf16_f32 v36, v64, v65\n\t"
    "v_mfma_f32_16x16x32_bf16 v[80:83], a[84:87], a[52:55], v[80:83]\n\t"
    "ds_read_b64_tr_b16 a[242:243], v18 offset:53248\n\t"
    "v_cvt_pk_bf16_f32 v37, v66, v67\n\t"
    "v_mfma_f32_16x16x32_bf16 v[80:83], a[88:91], a[56:59], v[80:83]\n\t"
    "ds_read_b64_tr_b16 a[244:245], v18 offset:57344\n\t"
    "v_cvt_pk_bf16_f32 v38, v68, v69\n\t"
    "v_mfma_f32_16x16x32_bf16 v[80:83], a[92:95], a[60:63], v[80:83]\n\t"
    "ds_read_b64_tr_b16 a[246:247], v18 offset:61440\n\t"
    "v_cvt_pk_bf16_f32 v39, v70, v71\n\t"
    "v_mfma_f32_16x16x32_bf16 v[84:87], a[96:99], a[48:51], v[56:59]\n\t"
    "ds_read_b64_tr_b16 a[248:249], v19 offset:49152\n\t"
    "v_cvt_pk_bf16_f32 v40, v72, v73\n\t"
    "v_mfma_f32_16x16x32_bf16 v[84:87], a[100:103], a[52:55], v[84:87]\n\t"
    "ds_read_b64_tr_b16 a[250:251], v19 offset:53248\n\t"
    "v_cvt_pk_bf16_f32 v41, v74, v75\n\t"
    "v_mfma_f32_16x16x32_bf16 v[84:87], a[104:107], a[56:59], v[84:87]\n\t"
    "ds_read_b64_tr_b16 a[252:253], v19 offset:57344\n\t"
    "v_cvt_pk_bf16_f32 v42, v76, v77\n\t"
    "v_mfma_f32_16x16x32_bf16 v[84:87], a[108:111], a[60:63], v[84:87]\n\t"
    "ds_read_b64_tr_b16 a[254:255], v19 offset:61440\n\t"
    "v_cvt_pk_bf16_f32 v43, v78, v79\n\t"
    "v_mfma_f32_16x16x32_bf16 v[88:91], a[112:115], a[48:51], v[56:59]\n\t"
    "v_exp_f32 v80, v80\n\t"
    "v_mfma_f32_16x16x32_bf16 v[88:91], a[116:119], a[52:55], v[88:91]\n\t"
    "v_exp_f32 v81, v81\n\t"
    "v_mfma_f32_16x16x32_bf16 v[88:91], a[120:123], a[56:59], v[88:91]\n\t"
    "v_exp_f32 v82, v82\n\t"
    "v_mfma_f32_16x16x32_bf16 v[88:91], a[124:127], a[60:63], v[88:91]\n\t"
    "v_exp_f32 v83, v83\n\t"
    "v_mfma_f32_16x16x32_bf16 v[92:95], a[128:131], a[48:51], v[56:59]\n\t"
    "v_exp_f32 v84, v84\n\t"
    "v_mfma_f32_16x16x32_bf16 v[92:95], a[132:135], a[52:55], v[92:95]\n\t"
    "v_exp_f32 v85, v85\n\t"
    "v_mfma_f32_16x16x32_bf16 v[92:95], a[136:139], a[56:59], v[92:95]\n\t"
    "v_exp_f32 v86, v86\n\t"
    "v_mfma_f32_16x16x32_bf16 v[92:95], a[140:143], a[60:63], v[92:95]\n\t"
    "v_exp_f32 v87, v87\n\t"
    "v_mfma_f32_16x16x32_bf16 v[160:163], v[36:39], a[144:147], v[160:163]\n\t"
    "v_exp_f32 v88, v88\n\t"
    "v_mfma_f32_16x16x32_bf16 v[160:163], v[40:43], a[148:151], v[160:163]\n\t"
    "v_exp_f32 v89, v89\n\t"
    "v_mfma_f32_16x16x32_bf16 v[164:167], v[36:39], a[152:155], v[164:167]\n\t"
    "v_exp_f32 v90, v90\n\t"
    "v_mfma_f32_16x16x32_bf16 v[164:167], v[40:43], a[156:159], v[164:167]\n\t"
    "v_exp_f32 v91, v91\n\t"
    "v_mfma_f32_16x16x32_bf16 v[168:171], v[36:39], a[160:163], v[168:171]\n\t"
    "v_exp_f32 v92, v92\n\t"
    "v_mfma_f32_16x16x32_bf16 v[168:171], v[40:43], a[164:167], v[168:171]\n\t"
    "v_exp_f32 v93, v93\n\t"
    "v_mfma_f32_16x16x32_bf16 v[172:175], v[36:39], a[168:171], v[172:175]\n\t"
    "v_exp_f32 v94, v94\n\t"
    "v_mfma_f32_16x16x32_bf16 v[172:175], v[40:43], a[172:175], v[172:175]\n\t"
    "v_exp_f32 v95, v95\n\t"
    "v_mfma_f32_16x16x32_bf16 v[176:179], v[36:39], a[176:179], v[176:179]\n\t"
    "v_mfma_f32_16x16x32_bf16 v[176:179], v[40:43], a[180:183], v[176:179]\n\t"
    "v_mfma_f32_16x16x32_bf16 v[180:183], v[36:39], a[184:187], v[180:183]\n\t"
    "v_mfma_f32_16x16x32_bf16 v[180:183], v[40:43], a[188:191], v[180:183]\n\t"
    "v_mfma_f32_16x16x32_bf16 v[184:187], v[36:39], a[192:195], v[184:187]\n\t"
    "v_mfma_f32_16x16x32_bf16 v[184:187], v[40:43], a[196:199], v[184:187]\n\t"
    "v_mfma_f32_16x16x32_bf16 v[188:191], v[36:39], a[200:203], v[188:191]\n\t"
    "v_mfma_f32_16x16x32_bf16 v[188:191], v[40:43], a[204:207], v[188:191]\n\t"
    "v_smfmac_f32_16x16x64_bf16 a[220:223], a[208:211], v[36:43], v24\n\t"
    "v_mfma_f32_16x16x32_bf16 v[68:71], a[96:99], a[64:67], v[60:63]\n\t"
    "ds_read_b128 a[80:83], v8 offset:49152\n\t"
    "v_cvt_pk_bf16_f32 v36, v80, v81\n\t"
    "v_mfma_f32_16x16x32_bf16 v[68:71], a[100:103], a[68:71], v[68:71]\n\t"
    "ds_read_b128 a[84:87], v9 offset:49152\n\t"
    "v_cvt_pk_bf16_f32 v37, v82, v83\n\t"
    "v_mfma_f32_16x16x32_bf16 v[68:71], a[104:107], a[72:75], v[68:71]\n\t"
    "ds_read_b128 a[88:91], v10 offset:49152\n\t"
    "v_cvt_pk_bf16_f32 v38, v84, v85\n\t"
    "v_mfma_f32_16x16x32_bf16 v[68:71], a[108:111], a[76:79], v[68:71]\n\t"
    "ds_read_b128 a[92:95], v11 offset:49152\n\t"
    "v_cvt_pk_bf16_f32 v39, v86, v87\n\t"
    "v_mfma_f32_16x16x32_bf16 v[72:75], a[112:115], a[64:67], v[60:63]\n\t"
    "ds_read_b128 a[96:99], v8 offset:53248\n\t"
    "v_cvt_pk_bf16_f32 v40, v88, v89\n\t"
    "v_mfma_f32_16x16x32_bf16 v[72:75], a[116:119], a[68:71], v[72:75]\n\t"
    "ds_read_b128 a[100:103], v9 offset:53248\n\t"
    "v_cvt_pk_bf16_f32 v41, v90, v91\n\t"
    "v_mfma_f32_16x16x32_bf16 v[72:75], a[120:123], a[72:75], v[72:75]\n\t"
    "ds_read_b128 a[104:107], v10 offset:53248\n\t"
    "v_cvt_pk_bf16_f32 v42, v92, v93\n\t"
    "v_mfma_f32_16x16x32_bf16 v[72:75], a[124:127], a[76:79], v[72:75]\n\t"
    "ds_read_b128 a[108:111], v11 offset:53248\n\t"
    "v_cvt_pk_bf16_f32 v43, v94, v95\n\t"
    "v_mfma_f32_16x16x32_bf16 v[76:79], a[128:131], a[64:67], v[60:63]\n\t"
    "v_exp_f32 v68, v68\n\t"
    "v_mfma_f32_16x16x32_bf16 v[76:79], a[132:135], a[68:71], v[76:79]\n\t"
    "v_exp_f32 v69, v69\n\t"
    "v_mfma_f32_16x16x32_bf16 v[76:79], a[136:139], a[72:75], v[76:79]\n\t"
    "v_exp_f32 v70, v70\n\t"
    "v_mfma_f32_16x16x32_bf16 v[76:79], a[140:143], a[76:79], v[76:79]\n\t"
    "v_exp_f32 v71, v71\n\t"
    "v_mfma_f32_16x16x32_bf16 v[192:195], v[36:39], a[144:147], v[192:195]\n\t"
    "v_exp_f32 v72, v72\n\t"
    "v_mfma_f32_16x16x32_bf16 v[192:195], v[40:43], a[148:151], v[192:195]\n\t"
    "v_exp_f32 v73, v73\n\t"
    "v_mfma_f32_16x16x32_bf16 v[196:199], v[36:39], a[152:155], v[196:199]\n\t"
    "v_exp_f32 v74, v74\n\t"
    "v_mfma_f32_16x16x32_bf16 v[196:199], v[40:43], a[156:159], v[196:199]\n\t"
    "v_exp_f32 v75, v75\n\t"
    "v_mfma_f32_16x16x32_bf16 v[200:203], v[36:39], a[160:163], v[200:203]\n\t"
    "v_exp_f32 v76, v76\n\t"
    "v_mfma_f32_16x16x32_bf16 v[200:203], v[40:43], a[164:167], v[200:203]\n\t"
    "v_exp_f32 v77, v77\n\t"
    "v_mfma_f32_16x16x32_bf16 v[204:207], v[36:39], a[168:171], v[204:207]\n\t"
    "v_exp_f32 v78, v78\n\t"
    "v_mfma_f32_16x16x32_bf16 v[204:207], v[40:43], a[172:175], v[204:207]\n\t"
    "v_exp_f32 v79, v79\n\t"
    "v_mfma_f32_16x16x32_bf16 v[208:211], v[36:39], a[176:179], v[208:211]\n\t"
    "ds_read_b128 a[112:115], v8 offset:57344\n\t"
    "v_mfma_f32_16x16x32_bf16 v[208:211], v[40:43], a[180:183], v[208:211]\n\t"
    "ds_read_b128 a[116:119], v9 offset:57344\n\t"
    "v_mfma_f32_16x16x32_bf16 v[212:215], v[36:39], a[184:187], v[212:215]\n\t"
    "ds_read_b128 a[120:123], v10 offset:57344\n\t"
    "v_mfma_f32_16x16x32_bf16 v[212:215], v[40:43], a[188:191], v[212:215]\n\t"
    "ds_read_b128 a[124:127], v11 offset:57344\n\t"
    "v_mfma_f32_16x16x32_bf16 v[216:219], v[36:39], a[192:195], v[216:219]\n\t"
    "ds_read_b128 a[128:131], v8 offset:61440\n\t"
    "v_mfma_f32_16x16x32_bf16 v[216:219], v[40:43], a[196:199], v[216:219]\n\t"
    "ds_read_b128 a[132:135], v9 offset:61440\n\t"
    "v_mfma_f32_16x16x32_bf16 v[220:223], v[36:39], a[200:203], v[220:223]\n\t"
    "ds_read_b128 a[136:139], v10 offset:61440\n\t"
    "v_mfma_f32_16x16x32_bf16 v[220:223], v[40:43], a[204:207], v[220:223]\n\t"
    "ds_read_b128 a[140:143], v11 offset:61440\n\t"
    "v_smfmac_f32_16x16x64_bf16 a[224:227], a[208:211], v[36:43], v24\n\t"
    "s_waitcnt lgkmcnt(7)\n\t"
    "v_mfma_f32_16x16x32_bf16 v[80:83], a[80:83], a[0:3], v[44:47]\n\t"
    "ds_read_b64 v[36:37], v27 offset:0\n\t"
    "v_mfma_f32_16x16x32_bf16 v[80:83], a[84:87], a[4:7], v[80:83]\n\t"
    "v_mfma_f32_16x16x32_bf16 v[80:83], a[88:91], a[8:11], v[80:83]\n\t"
    "v_cvt_pk_bf16_f32 v38, v68, v69\n\t"
    "v_mfma_f32_16x16x32_bf16 v[80:83], a[92:95], a[12:15], v[80:83]\n\t"
    "v_cvt_pk_bf16_f32 v39, v70, v71\n\t"
    "v_mfma_f32_16x16x32_bf16 v[84:87], a[96:99], a[0:3], v[44:47]\n\t"
    "v_cvt_pk_bf16_f32 v40, v72, v73\n\t"
    "v_mfma_f32_16x16x32_bf16 v[84:87], a[100:103], a[4:7], v[84:87]\n\t"
    "v_cvt_pk_bf16_f32 v41, v74, v75\n\t"
    "v_mfma_f32_16x16x32_bf16 v[84:87], a[104:107], a[8:11], v[84:87]\n\t"
    "v_cvt_pk_bf16_f32 v42, v76, v77\n\t"
    "v_mfma_f32_16x16x32_bf16 v[84:87], a[108:111], a[12:15], v[84:87]\n\t"
    "v_cvt_pk_bf16_f32 v43, v78, v79\n\t"
    "; TILE 31\n\t"
    "v_mfma_f32_16x16x32_bf16 v[88:91], a[112:115], a[0:3], v[44:47]\n\t"
    "s_waitcnt lgkmcnt(0)\n\t"
    "v_exp_f32 v80, v80\n\t"
    "v_mfma_f32_16x16x32_bf16 v[88:91], a[116:119], a[4:7], v[88:91]\n\t"
    "v_exp_f32 v81, v81\n\t"
    "v_mfma_f32_16x16x32_bf16 v[88:91], a[120:123], a[8:11], v[88:91]\n\t"
    "v_exp_f32 v82, v82\n\t"
    "v_mfma_f32_16x16x32_bf16 v[88:91], a[124:127], a[12:15], v[88:91]\n\t"
    "v_exp_f32 v83, v83\n\t"
    "v_mfma_f32_16x16x32_bf16 v[92:95], a[128:131], a[0:3], v[44:47]\n\t"
    "v_exp_f32 v84, v84\n\t"
    "v_mfma_f32_16x16x32_bf16 v[92:95], a[132:135], a[4:7], v[92:95]\n\t"
    "v_exp_f32 v85, v85\n\t"
    "v_mfma_f32_16x16x32_bf16 v[92:95], a[136:139], a[8:11], v[92:95]\n\t"
    "v_exp_f32 v86, v86\n\t"
    "v_mfma_f32_16x16x32_bf16 v[92:95], a[140:143], a[12:15], v[92:95]\n\t"
    "v_exp_f32 v87, v87\n\t"
    "v_mfma_f32_16x16x32_bf16 v[224:227], v[36:39], a[144:147], v[224:227]\n\t"
    "v_exp_f32 v88, v88\n\t"
    "v_mfma_f32_16x16x32_bf16 v[224:227], v[40:43], a[148:151], v[224:227]\n\t"
    "v_exp_f32 v89, v89\n\t"
    "v_mfma_f32_16x16x32_bf16 v[228:231], v[36:39], a[152:155], v[228:231]\n\t"
    "v_exp_f32 v90, v90\n\t"
    "v_mfma_f32_16x16x32_bf16 v[228:231], v[40:43], a[156:159], v[228:231]\n\t"
    "v_exp_f32 v91, v91\n\t"
    "v_mfma_f32_16x16x32_bf16 v[232:235], v[36:39], a[160:163], v[232:235]\n\t"
    "v_exp_f32 v92, v92\n\t"
    "v_mfma_f32_16x16x32_bf16 v[232:235], v[40:43], a[164:167], v[232:235]\n\t"
    "v_exp_f32 v93, v93\n\t"
    "v_mfma_f32_16x16x32_bf16 v[236:239], v[36:39], a[168:171], v[236:239]\n\t"
    "v_exp_f32 v94, v94\n\t"
    "v_mfma_f32_16x16x32_bf16 v[236:239], v[40:43], a[172:175], v[236:239]\n\t"
    "v_exp_f32 v95, v95\n\t"
    "v_mfma_f32_16x16x32_bf16 v[240:243], v[36:39], a[176:179], v[240:243]\n\t"
    "ds_read_b64_tr_b16 a[144:145], v12 offset:49152\n\t"
    "ds_read_b64_tr_b16 a[146:147], v12 offset:53248\n\t"
    "v_mfma_f32_16x16x32_bf16 v[240:243], v[40:43], a[180:183], v[240:243]\n\t"
    "ds_read_b64_tr_b16 a[148:149], v12 offset:57344\n\t"
    "ds_read_b64_tr_b16 a[150:151], v12 offset:61440\n\t"
    "v_mfma_f32_16x16x32_bf16 v[244:247], v[36:39], a[184:187], v[244:247]\n\t"
    "ds_read_b64_tr_b16 a[152:153], v13 offset:49152\n\t"
    "ds_read_b64_tr_b16 a[154:155], v13 offset:53248\n\t"
    "v_mfma_f32_16x16x32_bf16 v[244:247], v[40:43], a[188:191], v[244:247]\n\t"
    "ds_read_b64_tr_b16 a[156:157], v13 offset:57344\n\t"
    "ds_read_b64_tr_b16 a[158:159], v13 offset:61440\n\t"
    "v_mfma_f32_16x16x32_bf16 v[248:251], v[36:39], a[192:195], v[248:251]\n\t"
    "ds_read_b64_tr_b16 a[160:161], v14 offset:49152\n\t"
    "ds_read_b64_tr_b16 a[162:163], v14 offset:53248\n\t"
    "v_mfma_f32_16x16x32_bf16 v[248:251], v[40:43], a[196:199], v[248:251]\n\t"
    "ds_read_b64_tr_b16 a[164:165], v14 offset:57344\n\t"
    "ds_read_b64_tr_b16 a[166:167], v14 offset:61440\n\t"
    "v_mfma_f32_16x16x32_bf16 v[252:255], v[36:39], a[200:203], v[252:255]\n\t"
    "ds_read_b64_tr_b16 a[168:169], v15 offset:49152\n\t"
    "ds_read_b64_tr_b16 a[170:171], v15 offset:53248\n\t"
    "v_mfma_f32_16x16x32_bf16 v[252:255], v[40:43], a[204:207], v[252:255]\n\t"
    "ds_read_b64_tr_b16 a[172:173], v15 offset:57344\n\t"
    "ds_read_b64_tr_b16 a[174:175], v15 offset:61440\n\t"
    "v_smfmac_f32_16x16x64_bf16 a[228:231], a[208:211], v[36:43], v24\n\t"
    "v_mfma_f32_16x16x32_bf16 v[64:67], a[80:83], a[16:19], v[48:51]\n\t"
    "ds_read_b64_tr_b16 a[176:177], v16 offset:49152\n\t"
    "v_cvt_pk_bf16_f32 v36, v80, v81\n\t"
    "v_mfma_f32_16x16x32_bf16 v[64:67], a[84:87], a[20:23], v[64:67]\n\t"
    "ds_read_b64_tr_b16 a[178:179], v16 offset:53248\n\t"
    "v_cvt_pk_bf16_f32 v37, v82, v83\n\t"
    "v_mfma_f32_16x16x32_bf16 v[64:67], a[88:91], a[24:27], v[64:67]\n\t"
    "ds_read_b64_tr_b16 a[180:181], v16 offset:57344\n\t"
    "v_cvt_pk_bf16_f32 v38, v84, v85\n\t"
    "v_mfma_f32_16x16x32_bf16 v[64:67], a[92:95], a[28:31], v[64:67]\n\t"
    "ds_read_b64_tr_b16 a[182:183], v16 offset:61440\n\t"
    "v_cvt_pk_bf16_f32 v39, v86, v87\n\t"
    "v_mfma_f32_16x16x32_bf16 v[68:71], a[96:99], a[16:19], v[48:51]\n\t"
    "ds_read_b64_tr_b16 a[184:185], v17 offset:49152\n\t"
    "v_cvt_pk_bf16_f32 v40, v88, v89\n\t"
    "v_mfma_f32_16x16x32_bf16 v[68:71], a[100:103], a[20:23], v[68:71]\n\t"
    "ds_read_b64_tr_b16 a[186:187], v17 offset:53248\n\t"
    "v_cvt_pk_bf16_f32 v41, v90, v91\n\t"
    "v_mfma_f32_16x16x32_bf16 v[68:71], a[104:107], a[24:27], v[68:71]\n\t"
    "ds_read_b64_tr_b16 a[188:189], v17 offset:57344\n\t"
    "v_cvt_pk_bf16_f32 v42, v92, v93\n\t"
    "v_mfma_f32_16x16x32_bf16 v[68:71], a[108:111], a[28:31], v[68:71]\n\t"
    "ds_read_b64_tr_b16 a[190:191], v17 offset:61440\n\t"
    "v_cvt_pk_bf16_f32 v43, v94, v95\n\t"
    "v_mfma_f32_16x16x32_bf16 v[72:75], a[112:115], a[16:19], v[48:51]\n\t"
    "v_exp_f32 v64, v64\n\t"
    "v_mfma_f32_16x16x32_bf16 v[72:75], a[116:119], a[20:23], v[72:75]\n\t"
    "v_exp_f32 v65, v65\n\t"
    "v_mfma_f32_16x16x32_bf16 v[72:75], a[120:123], a[24:27], v[72:75]\n\t"
    "v_exp_f32 v66, v66\n\t"
    "v_mfma_f32_16x16x32_bf16 v[72:75], a[124:127], a[28:31], v[72:75]\n\t"
    "v_exp_f32 v67, v67\n\t"
    "v_mfma_f32_16x16x32_bf16 v[76:79], a[128:131], a[16:19], v[48:51]\n\t"
    "v_exp_f32 v68, v68\n\t"
    "v_mfma_f32_16x16x32_bf16 v[76:79], a[132:135], a[20:23], v[76:79]\n\t"
    "v_exp_f32 v69, v69\n\t"
    "v_mfma_f32_16x16x32_bf16 v[76:79], a[136:139], a[24:27], v[76:79]\n\t"
    "v_exp_f32 v70, v70\n\t"
    "v_mfma_f32_16x16x32_bf16 v[76:79], a[140:143], a[28:31], v[76:79]\n\t"
    "s_waitcnt lgkmcnt(0)\n\t"
    "v_exp_f32 v71, v71\n\t"
    "v_mfma_f32_16x16x32_bf16 v[96:99], v[36:39], a[144:147], v[96:99]\n\t"
    "v_exp_f32 v72, v72\n\t"
    "v_mfma_f32_16x16x32_bf16 v[96:99], v[40:43], a[148:151], v[96:99]\n\t"
    "v_exp_f32 v73, v73\n\t"
    "v_mfma_f32_16x16x32_bf16 v[100:103], v[36:39], a[152:155], v[100:103]\n\t"
    "v_exp_f32 v74, v74\n\t"
    "v_mfma_f32_16x16x32_bf16 v[100:103], v[40:43], a[156:159], v[100:103]\n\t"
    "v_exp_f32 v75, v75\n\t"
    "v_mfma_f32_16x16x32_bf16 v[104:107], v[36:39], a[160:163], v[104:107]\n\t"
    "v_exp_f32 v76, v76\n\t"
    "v_mfma_f32_16x16x32_bf16 v[104:107], v[40:43], a[164:167], v[104:107]\n\t"
    "v_exp_f32 v77, v77\n\t"
    "v_mfma_f32_16x16x32_bf16 v[108:111], v[36:39], a[168:171], v[108:111]\n\t"
    "v_exp_f32 v78, v78\n\t"
    "v_mfma_f32_16x16x32_bf16 v[108:111], v[40:43], a[172:175], v[108:111]\n\t"
    "v_exp_f32 v79, v79\n\t"
    "v_mfma_f32_16x16x32_bf16 v[112:115], v[36:39], a[176:179], v[112:115]\n\t"
    "v_add_u32 v35, 1, v35\n\t"
    "v_mfma_f32_16x16x32_bf16 v[112:115], v[40:43], a[180:183], v[112:115]\n\t"
    "ds_read_b32 v33, v27 offset:3072\n\t"
    "v_mfma_f32_16x16x32_bf16 v[116:119], v[36:39], a[184:187], v[116:119]\n\t"
    "v_mfma_f32_16x16x32_bf16 v[116:119], v[40:43], a[188:191], v[116:119]\n\t"
    "v_mfma_f32_16x16x32_bf16 v[120:123], v[36:39], a[240:243], v[120:123]\n\t"
    "v_mfma_f32_16x16x32_bf16 v[120:123], v[40:43], a[244:247], v[120:123]\n\t"
    "s_waitcnt lgkmcnt(0)\n\t"
    "v_cmp_gt_u32 vcc, v35, v33\n\t"
    "v_mfma_f32_16x16x32_bf16 v[124:127], v[36:39], a[248:251], v[124:127]\n\t"
    "v_mfma_f32_16x16x32_bf16 v[124:127], v[40:43], a[252:255], v[124:127]\n\t"
    "v_smfmac_f32_16x16x64_bf16 a[212:215], a[208:211], v[36:43], v24\n\t"
    "s_cbranch_vccnz SPIN5P31%=\n\t"
    "RET5P31%=:\n\t"
    "v_mfma_f32_16x16x32_bf16 v[80:83], a[80:83], a[32:35], v[52:55]\n\t"
    "v_cvt_pk_bf16_f32 v36, v64, v65\n\t"
    "v_mfma_f32_16x16x32_bf16 v[80:83], a[84:87], a[36:39], v[80:83]\n\t"
    "v_cvt_pk_bf16_f32 v37, v66, v67\n\t"
    "v_mfma_f32_16x16x32_bf16 v[80:83], a[88:91], a[40:43], v[80:83]\n\t"
    "v_cvt_pk_bf16_f32 v38, v68, v69\n\t"
    "v_mfma_f32_16x16x32_bf16 v[80:83], a[92:95], a[44:47], v[80:83]\n\t"
    "v_cvt_pk_bf16_f32 v39, v70, v71\n\t"
    "v_accvgpr_read_b32 v44, a212\n\t"
    "v_accvgpr_read_b32 v45, a213\n\t"
    "s_add_u32 s61, s70, 0\n\t"
    "v_add_f32 v44, v44, v45\n\t"
    "v_mfma_f32_16x16x32_bf16 v[84:87], a[96:99], a[32:35], v[52:55]\n\t"
    "v_cvt_pk_bf16_f32 v40, v72, v73\n\t"
    "v_rcp_f32 v34, v44\n\t"
    "v_add_f32 v31, v31, v44\n\t"
    "v_min_f32 v32, v32, v44\n\t"
    "v_mfma_f32_16x16x32_bf16 v[84:87], a[100:103], a[36:39], v[84:87]\n\t"
    "ds_bpermute_b32 v44, v30, v34 offset:0\n\t"
    "ds_bpermute_b32 v45, v30, v34 offset:4\n\t"
    "ds_bpermute_b32 v46, v30, v34 offset:8\n\t"
    "ds_bpermute_b32 v47, v30, v34 offset:12\n\t"
    "v_cvt_pk_bf16_f32 v41, v74, v75\n\t"
    "v_mfma_f32_16x16x32_bf16 v[84:87], a[104:107], a[40:43], v[84:87]\n\t"
    "v_cvt_pk_bf16_f32 v42, v76, v77\n\t"
    "v_mfma_f32_16x16x32_bf16 v[84:87], a[108:111], a[44:47], v[84:87]\n\t"
    "v_cvt_pk_bf16_f32 v43, v78, v79\n\t"
    "v_mfma_f32_16x16x32_bf16 v[88:91], a[112:115], a[32:35], v[52:55]\n\t"
    "v_exp_f32 v80, v80\n\t"
    "v_mfma_f32_16x16x32_bf16 v[88:91], a[116:119], a[36:39], v[88:91]\n\t"
    "v_exp_f32 v81, v81\n\t"
    "v_mfma_f32_16x16x32_bf16 v[88:91], a[120:123], a[40:43], v[88:91]\n\t"
    "v_exp_f32 v82, v82\n\t"
    "v_mfma_f32_16x16x32_bf16 v[88:91], a[124:127], a[44:47], v[88:91]\n\t"
    "v_exp_f32 v83, v83\n\t"
    "v_mfma_f32_16x16x32_bf16 v[92:95], a[128:131], a[32:35], v[52:55]\n\t"
    "v_exp_f32 v84, v84\n\t"
    "v_mfma_f32_16x16x32_bf16 v[92:95], a[132:135], a[36:39], v[92:95]\n\t"
    "s_waitcnt lgkmcnt(0)\n\t"
    "v_mul_f32 v96, v96, v44\n\t"
    "v_mul_f32 v97, v97, v45\n\t"
    "v_mul_f32 v100, v100, v44\n\t"
    "v_exp_f32 v85, v85\n\t"
    "v_mfma_f32_16x16x32_bf16 v[92:95], a[136:139], a[40:43], v[92:95]\n\t"
    "v_mul_f32 v101, v101, v45\n\t"
    "v_mul_f32 v104, v104, v44\n\t"
    "v_mul_f32 v105, v105, v45\n\t"
    "v_exp_f32 v86, v86\n\t"
    "v_mfma_f32_16x16x32_bf16 v[92:95], a[140:143], a[44:47], v[92:95]\n\t"
    "v_mul_f32 v108, v108, v44\n\t"
    "v_mul_f32 v109, v109, v45\n\t"
    "v_cvt_pk_bf16_f32 v8, v96, v100\n\t"
    "v_exp_f32 v87, v87\n\t"
    "v_mfma_f32_16x16x32_bf16 v[128:131], v[36:39], a[144:147], v[128:131]\n\t"
    "v_cvt_pk_bf16_f32 v9, v104, v108\n\t"
    "v_exp_f32 v88, v88\n\t"
    "v_mfma_f32_16x16x32_bf16 v[128:131], v[40:43], a[148:151], v[128:131]\n\t"
    "ds_write_b64 v26, v[8:9] offset:0\n\t"
    "v_cvt_pk_bf16_f32 v10, v97, v101\n\t"
    "v_cvt_pk_bf16_f32 v11, v105, v109\n\t"
    "v_exp_f32 v89, v89\n\t"
    "v_mfma_f32_16x16x32_bf16 v[132:135], v[36:39], a[152:155], v[132:135]\n\t"
    "ds_write_b64 v26, v[10:11] offset:256\n\t"
    "v_mul_f32 v112, v112, v44\n\t"
    "v_mul_f32 v113, v113, v45\n\t"
    "v_exp_f32 v90, v90\n\t"
    "v_mfma_f32_16x16x32_bf16 v[132:135], v[40:43], a[156:159], v[132:135]\n\t"
    "v_mul_f32 v116, v116, v44\n\t"
    "v_mul_f32 v117, v117, v45\n\t"
    "v_mul_f32 v120, v120, v44\n\t"
    "v_exp_f32 v91, v91\n\t"
    "v_mfma_f32_16x16x32_bf16 v[136:139], v[36:39], a[160:163], v[136:139]\n\t"
    "v_mul_f32 v121, v121, v45\n\t"
    "v_mul_f32 v124, v124, v44\n\t"
    "v_mul_f32 v125, v125, v45\n\t"
    "v_exp_f32 v92, v92\n\t"
    "v_mfma_f32_16x16x32_bf16 v[136:139], v[40:43], a[164:167], v[136:139]\n\t"
    "v_cvt_pk_bf16_f32 v20, v112, v116\n\t"
    "v_cvt_pk_bf16_f32 v21, v120, v124\n\t"
    "v_exp_f32 v93, v93\n\t"
    "v_mfma_f32_16x16x32_bf16 v[140:143], v[36:39], a[168:171], v[140:143]\n\t"
    "ds_write_b64 v26, v[20:21] offset:128\n\t"
    "v_cvt_pk_bf16_f32 v22, v113, v117\n\t"
    "v_cvt_pk_bf16_f32 v23, v121, v125\n\t"
    "v_exp_f32 v94, v94\n\t"
    "v_mfma_f32_16x16x32_bf16 v[140:143], v[40:43], a[172:175], v[140:143]\n\t"
    "ds_write_b64 v26, v[22:23] offset:384\n\t"
    "ds_read_b64_tr_b16 a[192:193], v28 offset:0\n\t"
    "ds_read_b64_tr_b16 a[194:195], v29 offset:0\n\t"
    "v_exp_f32 v95, v95\n\t"
    "v_mfma_f32_16x16x32_bf16 v[144:147], v[36:39], a[176:179], v[144:147]\n\t"
    "ds_read_b64_tr_b16 a[196:197], v28 offset:256\n\t"
    "ds_read_b64_tr_b16 a[198:199], v29 offset:256\n\t"
    "v_mfma_f32_16x16x32_bf16 v[144:147], v[40:43], a[180:183], v[144:147]\n\t"
    "v_mfma_f32_16x16x32_bf16 v[148:151], v[36:39], a[184:187], v[148:151]\n\t"
    "v_mfma_f32_16x16x32_bf16 v[148:151], v[40:43], a[188:191], v[148:151]\n\t"
    "v_mfma_f32_16x16x32_bf16 v[152:155], v[36:39], a[240:243], v[152:155]\n\t"
    "v_mfma_f32_16x16x32_bf16 v[152:155], v[40:43], a[244:247], v[152:155]\n\t"
    "v_mfma_f32_16x16x32_bf16 v[156:159], v[36:39], a[248:251], v[156:159]\n\t"
    "v_mfma_f32_16x16x32_bf16 v[156:159], v[40:43], a[252:255], v[156:159]\n\t"
    "v_smfmac_f32_16x16x64_bf16 a[216:219], a[208:211], v[36:43], v24\n\t"
    "v_mfma_f32_16x16x32_bf16 v[64:67], a[80:83], a[48:51], v[56:59]\n\t"
    "v_cvt_pk_bf16_f32 v36, v80, v81\n\t"
    "v_mfma_f32_16x16x32_bf16 v[64:67], a[84:87], a[52:55], v[64:67]\n\t"
    "v_cvt_pk_bf16_f32 v37, v82, v83\n\t"
    "v_mfma_f32_16x16x32_bf16 v[64:67], a[88:91], a[56:59], v[64:67]\n\t"
    "v_cvt_pk_bf16_f32 v38, v84, v85\n\t"
    "v_mfma_f32_16x16x32_bf16 v[64:67], a[92:95], a[60:63], v[64:67]\n\t"
    "v_cvt_pk_bf16_f32 v39, v86, v87\n\t"
    "v_accvgpr_read_b32 v48, a216\n\t"
    "v_accvgpr_read_b32 v49, a217\n\t"
    "s_add_u32 s62, s70, 4096\n\t"
    "v_add_f32 v48, v48, v49\n\t"
    "v_mfma_f32_16x16x32_bf16 v[68:71], a[96:99], a[48:51], v[56:59]\n\t"
    "v_cvt_pk_bf16_f32 v40, v88, v89\n\t"
    "v_rcp_f32 v35, v48\n\t"
    "v_add_f32 v31, v31, v48\n\t"
    "v_min_f32 v32, v32, v48\n\t"
    "v_mfma_f32_16x16x32_bf16 v[68:71], a[100:103], a[52:55], v[68:71]\n\t"
    "ds_bpermute_b32 v48, v30, v35 offset:0\n\t"
    "ds_bpermute_b32 v49, v30, v35 offset:4\n\t"
    "ds_bpermute_b32 v50, v30, v35 offset:8\n\t"
    "ds_bpermute_b32 v51, v30, v35 offset:12\n\t"
    "v_cvt_pk_bf16_f32 v41, v90, v91\n\t"
    "v_mfma_f32_16x16x32_bf16 v[68:71], a[104:107], a[56:59], v[68:71]\n\t"
    "v_cvt_pk_bf16_f32 v42, v92, v93\n\t"
    "v_mul_f32 v98, v98, v46\n\t"
    "v_mul_f32 v99, v99, v47\n\t"
    "v_mul_f32 v102, v102, v46\n\t"
    "v_mul_f32 v103, v103, v47\n\t"
    "v_mfma_f32_16x16x32_bf16 v[68:71], a[108:111], a[60:63], v[68:71]\n\t"
    "v_cvt_pk_bf16_f32 v43, v94, v95\n\t"
    "v_mul_f32 v106, v106, v46\n\t"
    "v_mul_f32 v107, v107, v47\n\t"
    "v_mul_f32 v110, v110, v46\n\t"
    "v_mul_f32 v111, v111, v47\n\t"
    "v_mfma_f32_16x16x32_bf16 v[72:75], a[112:115], a[48:51], v[56:59]\n\t"
    "v_cvt_pk_bf16_f32 v8, v98, v102\n\t"
    "v_cvt_pk_bf16_f32 v9, v106, v110\n\t"
    "v_exp_f32 v64, v64\n\t"
    "v_mfma_f32_16x16x32_bf16 v[72:75], a[116:119], a[52:55], v[72:75]\n\t"
    "ds_write_b64 v26, v[8:9] offset:512\n\t"
    "v_cvt_pk_bf16_f32 v10, v99, v103\n\t"
    "v_cvt_pk_bf16_f32 v11, v107, v111\n\t"
    "v_exp_f32 v65, v65\n\t"
    "v_mfma_f32_16x16x32_bf16 v[72:75], a[120:123], a[56:59], v[72:75]\n\t"
    "ds_write_b64 v26, v[10:11] offset:768\n\t"
    "v_mul_f32 v114, v114, v46\n\t"
    "v_mul_f32 v115, v115, v47\n\t"
    "v_exp_f32 v66, v66\n\t"
    "v_mfma_f32_16x16x32_bf16 v[72:75], a[124:127], a[60:63], v[72:75]\n\t"
    "v_mul_f32 v118, v118, v46\n\t"
    "v_mul_f32 v119, v119, v47\n\t"
    "v_mul_f32 v122, v122, v46\n\t"
    "v_exp_f32 v67, v67\n\t"
    "v_mfma_f32_16x16x32_bf16 v[76:79], a[128:131], a[48:51], v[56:59]\n\t"
    "v_mul_f32 v123, v123, v47\n\t"
    "v_mul_f32 v126, v126, v46\n\t"
    "v_mul_f32 v127, v127, v47\n\t"
    "v_exp_f32 v68, v68\n\t"
    "v_mfma_f32_16x16x32_bf16 v[76:79], a[132:135], a[52:55], v[76:79]\n\t"
    "v_cvt_pk_bf16_f32 v20, v114, v118\n\t"
    "v_cvt_pk_bf16_f32 v21, v122, v126\n\t"
    "v_exp_f32 v69, v69\n\t"
    "v_mfma_f32_16x16x32_bf16 v[76:79], a[136:139], a[56:59], v[76:79]\n\t"
    "ds_write_b64 v26, v[20:21] offset:640\n\t"
    "v_cvt_pk_bf16_f32 v22, v115, v119\n\t"
    "v_cvt_pk_bf16_f32 v23, v123, v127\n\t"
    "v_exp_f32 v70, v70\n\t"
    "v_mfma_f32_16x16x32_bf16 v[76:79], a[140:143], a[60:63], v[76:79]\n\t"
    "ds_write_b64 v26, v[22:23] offset:896\n\t"
    "ds_read_b64_tr_b16 a[200:201], v28 offset:512\n\t"
    "ds_read_b64_tr_b16 a[202:203], v29 offset:512\n\t"
    "v_exp_f32 v71, v71\n\t"
    "v_mfma_f32_16x16x32_bf16 v[160:163], v[36:39], a[144:147], v[160:163]\n\t"
    "ds_read_b64_tr_b16 a[204:205], v28 offset:768\n\t"
    "ds_read_b64_tr_b16 a[206:207], v29 offset:768\n\t"
    "v_exp_f32 v72, v72\n\t"
    "v_mfma_f32_16x16x32_bf16 v[160:163], v[40:43], a[148:151], v[160:163]\n\t"
    "s_waitcnt lgkmcnt(7)\n\t"
    "buffer_store_dwordx4 a[192:195], v25, s[52:55], s61 offen offset:0 sc0 sc1\n\t"
    "v_mul_f32 v128, v128, v48\n\t"
    "v_exp_f32 v73, v73\n\t"
    "v_mfma_f32_16x16x32_bf16 v[164:167], v[36:39], a[152:155], v[164:167]\n\t"
    "v_mul_f32 v129, v129, v49\n\t"
    "v_mul_f32 v132, v132, v48\n\t"
    "v_mul_f32 v133, v133, v49\n\t"
    "v_exp_f32 v74, v74\n\t"
    "v_mfma_f32_16x16x32_bf16 v[164:167], v[40:43], a[156:159], v[164:167]\n\t"
    "v_mul_f32 v136, v136, v48\n\t"
    "v_mul_f32 v137, v137, v49\n\t"
    "v_mul_f32 v140, v140, v48\n\t"
    "v_exp_f32 v75, v75\n\t"
    "v_mfma_f32_16x16x32_bf16 v[168:171], v[36:39], a[160:163], v[168:171]\n\t"
    "v_mul_f32 v141, v141, v49\n\t"
    "v_cvt_pk_bf16_f32 v8, v128, v132\n\t"
    "v_cvt_pk_bf16_f32 v9, v136, v140\n\t"
    "v_exp_f32 v76, v76\n\t"
    "v_mfma_f32_16x16x32_bf16 v[168:171], v[40:43], a[164:167], v[168:171]\n\t"
    "ds_write_b64 v26, v[8:9] offset:4096\n\t"
    "v_cvt_pk_bf16_f32 v10, v129, v133\n\t"
    "v_cvt_pk_bf16_f32 v11, v137, v141\n\t"
    "v_exp_f32 v77, v77\n\t"
    "v_mfma_f32_16x16x32_bf16 v[172:175], v[36:39], a[168:171], v[172:175]\n\t"
    "ds_write_b64 v26, v[10:11] offset:4352\n\t"
    "buffer_store_dwordx4 a[196:199], v25, s[52:55], s61 offen offset:256 sc0 sc1\n\t"
    "v_exp_f32 v78, v78\n\t"
    "v_mfma_f32_16x16x32_bf16 v[172:175], v[40:43], a[172:175], v[172:175]\n\t"
    "v_mul_f32 v144, v144, v48\n\t"
    "v_mul_f32 v145, v145, v49\n\t"
    "v_mul_f32 v148, v148, v48\n\t"
    "v_exp_f32 v79, v79\n\t"
    "v_mfma_f32_16x16x32_bf16 v[176:179], v[36:39], a[176:179], v[176:179]\n\t"
    "v_mul_f32 v149, v149, v49\n\t"
    "v_mul_f32 v152, v152, v48\n\t"
    "v_mul_f32 v153, v153, v49\n\t"
    "v_mul_f32 v156, v156, v48\n\t"
    "v_mul_f32 v157, v157, v49\n\t"
    "v_mfma_f32_16x16x32_bf16 v[176:179], v[40:43], a[180:183], v[176:179]\n\t"
    "v_cvt_pk_bf16_f32 v20, v144, v148\n\t"
    "v_cvt_pk_bf16_f32 v21, v152, v156\n\t"
    "v_mfma_f32_16x16x32_bf16 v[180:183], v[36:39], a[184:187], v[180:183]\n\t"
    "ds_write_b64 v26, v[20:21] offset:4224\n\t"
    "v_cvt_pk_bf16_f32 v22, v145, v149\n\t"
    "v_cvt_pk_bf16_f32 v23, v153, v157\n\t"
    "v_mfma_f32_16x16x32_bf16 v[180:183], v[40:43], a[188:191], v[180:183]\n\t"
    "ds_write_b64 v26, v[22:23] offset:4480\n\t"
    "s_waitcnt lgkmcnt(4)\n\t"
    "buffer_store_dwordx4 a[200:203], v25, s[52:55], s61 offen offset:512 sc0 sc1\n\t"
    "ds_read_b64_tr_b16 v[96:97], v28 offset:4096\n\t"
    "ds_read_b64_tr_b16 v[98:99], v29 offset:4096\n\t"
    "v_mfma_f32_16x16x32_bf16 v[184:187], v[36:39], a[240:243], v[184:187]\n\t"
    "ds_read_b64_tr_b16 v[100:101], v28 offset:4352\n\t"
    "ds_read_b64_tr_b16 v[102:103], v29 offset:4352\n\t"
    "v_mfma_f32_16x16x32_bf16 v[184:187], v[40:43], a[244:247], v[184:187]\n\t"
    "v_mfma_f32_16x16x32_bf16 v[188:191], v[36:39], a[248:251], v[188:191]\n\t"
    "v_mfma_f32_16x16x32_bf16 v[188:191], v[40:43], a[252:255], v[188:191]\n\t"
    "v_smfmac_f32_16x16x64_bf16 a[220:223], a[208:211], v[36:43], v24\n\t"
    "v_mfma_f32_16x16x32_bf16 v[84:87], a[96:99], a[64:67], v[60:63]\n\t"
    "v_cvt_pk_bf16_f32 v36, v64, v65\n\t"
    "v_mfma_f32_16x16x32_bf16 v[84:87], a[100:103], a[68:71], v[84:87]\n\t"
    "v_cvt_pk_bf16_f32 v37, v66, v67\n\t"
    "v_mfma_f32_16x16x32_bf16 v[84:87], a[104:107], a[72:75], v[84:87]\n\t"
    "v_cvt_pk_bf16_f32 v38, v68, v69\n\t"
    "v_mfma_f32_16x16x32_bf16 v[84:87], a[108:111], a[76:79], v[84:87]\n\t"
    "v_cvt_pk_bf16_f32 v39, v70, v71\n\t"
    "v_accvgpr_read_b32 v52, a220\n\t"
    "v_accvgpr_read_b32 v53, a221\n\t"
    "s_add_u32 s63, s70, 8192\n\t"
    "v_add_f32 v52, v52, v53\n\t"
    "v_mfma_f32_16x16x32_bf16 v[88:91], a[112:115], a[64:67], v[60:63]\n\t"
    "v_cvt_pk_bf16_f32 v40, v72, v73\n\t"
    "v_rcp_f32 v34, v52\n\t"
    "v_add_f32 v31, v31, v52\n\t"
    "v_min_f32 v32, v32, v52\n\t"
    "v_mfma_f32_16x16x32_bf16 v[88:91], a[116:119], a[68:71], v[88:91]\n\t"
    "ds_bpermute_b32 v52, v30, v34 offset:0\n\t"
    "ds_bpermute_b32 v53, v30, v34 offset:4\n\t"
    "ds_bpermute_b32 v54, v30, v34 offset:8\n\t"
    "ds_bpermute_b32 v55, v30, v34 offset:12\n\t"
    "v_cvt_pk_bf16_f32 v41, v74, v75\n\t"
    "v_mfma_f32_16x16x32_bf16 v[88:91], a[120:123], a[72:75], v[88:91]\n\t"
    "v_cvt_pk_bf16_f32 v42, v76, v77\n\t"
    "v_mul_f32 v130, v130, v50\n\t"
    "v_mul_f32 v131, v131, v51\n\t"
    "v_mul_f32 v134, v134, v50\n\t"
    "v_mul_f32 v135, v135, v51\n\t"
    "v_mfma_f32_16x16x32_bf16 v[88:91], a[124:127], a[76:79], v[88:91]\n\t"
    "v_cvt_pk_bf16_f32 v43, v78, v79\n\t"
    "v_mul_f32 v138, v138, v50\n\t"
    "v_mul_f32 v139, v139, v51\n\t"
    "v_mul_f32 v142, v142, v50\n\t"
    "v_mul_f32 v143, v143, v51\n\t"
    "v_mfma_f32_16x16x32_bf16 v[92:95], a[128:131], a[64:67], v[60:63]\n\t"
    "v_cvt_pk_bf16_f32 v8, v130, v134\n\t"
    "v_cvt_pk_bf16_f32 v9, v138, v142\n\t"
    "v_exp_f32 v84, v84\n\t"
    "v_mfma_f32_16x16x32_bf16 v[92:95], a[132:135], a[68:71], v[92:95]\n\t"
    "ds_write_b64 v26, v[8:9] offset:4608\n\t"
    "v_cvt_pk_bf16_f32 v10, v131, v135\n\t"
    "v_cvt_pk_bf16_f32 v11, v139, v143\n\t"
    "v_exp_f32 v85, v85\n\t"
    "v_mfma_f32_16x16x32_bf16 v[92:95], a[136:139], a[72:75], v[92:95]\n\t"
    "ds_write_b64 v26, v[10:11] offset:4864\n\t"
    "buffer_store_dwordx4 a[204:207], v25, s[52:55], s61 offen offset:768 sc0 sc1\n\t"
    "v_exp_f32 v86, v86\n\t"
    "v_mfma_f32_16x16x32_bf16 v[92:95], a[140:143], a[76:79], v[92:95]\n\t"
    "v_mul_f32 v146, v146, v50\n\t"
    "v_mul_f32 v147, v147, v51\n\t"
    "v_mul_f32 v150, v150, v50\n\t"
    "v_exp_f32 v87, v87\n\t"
    "v_mfma_f32_16x16x32_bf16 v[192:195], v[36:39], a[144:147], v[192:195]\n\t"
    "v_mul_f32 v151, v151, v51\n\t"
    "v_mul_f32 v154, v154, v50\n\t"
    "v_mul_f32 v155, v155, v51\n\t"
    "v_exp_f32 v88, v88\n\t"
    "v_mfma_f32_16x16x32_bf16 v[192:195], v[40:43], a[148:151], v[192:195]\n\t"
    "v_mul_f32 v158, v158, v50\n\t"
    "v_mul_f32 v159, v159, v51\n\t"
    "v_cvt_pk_bf16_f32 v20, v146, v150\n\t"
    "v_exp_f32 v89, v89\n\t"
    "v_mfma_f32_16x16x32_bf16 v[196:199], v[36:39], a[152:155], v[196:199]\n\t"
    "v_cvt_pk_bf16_f32 v21, v154, v158\n\t"
    "v_exp_f32 v90, v90\n\t"
    "v_mfma_f32_16x16x32_bf16 v[196:199], v[40:43], a[156:159], v[196:199]\n\t"
    "ds_write_b64 v26, v[20:21] offset:4736\n\t"
    "v_cvt_pk_bf16_f32 v22, v147, v151\n\t"
    "v_cvt_pk_bf16_f32 v23, v155, v159\n\t"
    "v_exp_f32 v91, v91\n\t"
    "v_mfma_f32_16x16x32_bf16 v[200:203], v[36:39], a[160:163], v[200:203]\n\t"
    "ds_write_b64 v26, v[22:23] offset:4992\n\t"
    "ds_read_b64_tr_b16 v[104:105], v28 offset:4608\n\t"
    "ds_read_b64_tr_b16 v[106:107], v29 offset:4608\n\t"
    "v_exp_f32 v92, v92\n\t"
    "v_mfma_f32_16x16x32_bf16 v[200:203], v[40:43], a[164:167], v[200:203]\n\t"
    "ds_read_b64_tr_b16 v[108:109], v28 offset:4864\n\t"
    "ds_read_b64_tr_b16 v[110:111], v29 offset:4864\n\t"
    "v_exp_f32 v93, v93\n\t"
    "v_mfma_f32_16x16x32_bf16 v[204:207], v[36:39], a[168:171], v[204:207]\n\t"
    "s_waitcnt lgkmcnt(6)\n\t"
    "buffer_store_dwordx4 v[96:99], v25, s[52:55], s62 offen offset:0 sc0 sc1\n\t"
    "v_mul_f32 v160, v160, v52\n\t"
    "v_exp_f32 v94, v94\n\t"
    "v_mfma_f32_16x16x32_bf16 v[204:207], v[40:43], a[172:175], v[204:207]\n\t"
    "v_mul_f32 v161, v161, v53\n\t"
    "v_mul_f32 v164, v164, v52\n\t"
    "v_mul_f32 v165, v165, v53\n\t"
    "v_exp_f32 v95, v95\n\t"
    "v_mfma_f32_16x16x32_bf16 v[208:211], v[36:39], a[176:179], v[208:211]\n\t"
    "v_mul_f32 v168, v168, v52\n\t"
    "v_mul_f32 v169, v169, v53\n\t"
    "v_mul_f32 v172, v172, v52\n\t"
    "v_mul_f32 v173, v173, v53\n\t"
    "v_cvt_pk_bf16_f32 v8, v160, v164\n\t"
    "v_mfma_f32_16x16x32_bf16 v[208:211], v[40:43], a[180:183], v[208:211]\n\t"
    "v_cvt_pk_bf16_f32 v9, v168, v172\n\t"
    "v_mfma_f32_16x16x32_bf16 v[212:215], v[36:39], a[184:187], v[212:215]\n\t"
    "ds_write_b64 v26, v[8:9] offset:0\n\t"
    "v_cvt_pk_bf16_f32 v10, v161, v165\n\t"
    "v_cvt_pk_bf16_f32 v11, v169, v173\n\t"
    "v_mfma_f32_16x16x32_bf16 v[212:215], v[40:43], a[188:191], v[212:215]\n\t"
    "ds_write_b64 v26, v[10:11] offset:256\n\t"
    "buffer_store_dwordx4 v[100:103], v25, s[52:55], s62 offen offset:256 sc0 sc1\n\t"
    "v_mul_f32 v176, v176, v52\n\t"
    "v_mul_f32 v177, v177, v53\n\t"
    "v_mfma_f32_16x16x32_bf16 v[216:219], v[36:39], a[240:243], v[216:219]\n\t"
    "v_mul_f32 v180, v180, v52\n\t"
    "v_mul_f32 v181, v181, v53\n\t"
    "v_mul_f32 v184, v184, v52\n\t"
    "v_mul_f32 v185, v185, v53\n\t"
    "v_mul_f32 v188, v188, v52\n\t"
    "v_mfma_f32_16x16x32_bf16 v[216:219], v[40:43], a[244:247], v[216:219]\n\t"
    "v_mul_f32 v189, v189, v53\n\t"
    "v_cvt_pk_bf16_f32 v20, v176, v180\n\t"
    "v_cvt_pk_bf16_f32 v21, v184, v188\n\t"
    "v_mfma_f32_16x16x32_bf16 v[220:223], v[36:39], a[248:251], v[220:223]\n\t"
    "ds_write_b64 v26, v[20:21] offset:128\n\t"
    "v_cvt_pk_bf16_f32 v22, v177, v181\n\t"
    "v_cvt_pk_bf16_f32 v23, v185, v189\n\t"
    "v_mfma_f32_16x16x32_bf16 v[220:223], v[40:43], a[252:255], v[220:223]\n\t"
    "ds_write_b64 v26, v[22:23] offset:384\n\t"
    "s_waitcnt lgkmcnt(4)\n\t"
    "buffer_store_dwordx4 v[104:107], v25, s[52:55], s62 offen offset:512 sc0 sc1\n\t"
    "ds_read_b64_tr_b16 v[128:129], v28 offset:0\n\t"
    "ds_read_b64_tr_b16 v[130:131], v29 offset:0\n\t"
    "v_smfmac_f32_16x16x64_bf16 a[224:227], a[208:211], v[36:43], v24\n\t"
    "ds_read_b64 v[36:37], v27 offset:512\n\t"
    "v_cvt_pk_bf16_f32 v38, v84, v85\n\t"
    "v_cvt_pk_bf16_f32 v39, v86, v87\n\t"
    "v_cvt_pk_bf16_f32 v40, v88, v89\n\t"
    "v_cvt_pk_bf16_f32 v41, v90, v91\n\t"
    "v_cvt_pk_bf16_f32 v42, v92, v93\n\t"
    "v_cvt_pk_bf16_f32 v43, v94, v95\n\t"
    "s_waitcnt lgkmcnt(0)\n\t"
    "s_nop 0\n\t"
    "v_smfmac_f32_16x16x64_bf16 a[228:231], a[208:211], v[36:43], v24\n\t"
    "v_mfma_f32_16x16x32_bf16 v[224:227], v[36:39], a[144:147], v[224:227]\n\t"
    "ds_read_b64_tr_b16 v[132:133], v28 offset:256\n\t"
    "ds_read_b64_tr_b16 v[134:135], v29 offset:256\n\t"
    "v_accvgpr_read_b32 v56, a224\n\t"
    "v_accvgpr_read_b32 v57, a225\n\t"
    "s_add_u32 s61, s70, 12288\n\t"
    "v_add_f32 v56, v56, v57\n\t"
    "v_mfma_f32_16x16x32_bf16 v[224:227], v[40:43], a[148:151], v[224:227]\n\t"
    "v_rcp_f32 v35, v56\n\t"
    "v_add_f32 v31, v31, v56\n\t"
    "v_min_f32 v32, v32, v56\n\t"
    "v_mfma_f32_16x16x32_bf16 v[228:231], v[36:39], a[152:155], v[228:231]\n\t"
    "ds_bpermute_b32 v56, v30, v35 offset:0\n\t"
    "ds_bpermute_b32 v57, v30, v35 offset:4\n\t"
    "ds_bpermute_b32 v58, v30, v35 offset:8\n\t"
    "ds_bpermute_b32 v59, v30, v35 offset:12\n\t"
    "v_mul_f32 v162, v162, v54\n\t"
    "v_mfma_f32_16x16x32_bf16 v[228:231], v[40:43], a[156:159], v[228:231]\n\t"
    "v_mul_f32 v163, v163, v55\n\t"
    "v_mul_f32 v166, v166, v54\n\t"
    "v_mul_f32 v167, v167, v55\n\t"
    "v_mul_f32 v170, v170, v54\n\t"
    "v_mul_f32 v171, v171, v55\n\t"
    "v_mfma_f32_16x16x32_bf16 v[232:235], v[36:39], a[160:163], v[232:235]\n\t"
    "v_mul_f32 v174, v174, v54\n\t"
    "v_mul_f32 v175, v175, v55\n\t"
    "v_cvt_pk_bf16_f32 v8, v162, v166\n\t"
    "v_cvt_pk_bf16_f32 v9, v170, v174\n\t"
    "v_mfma_f32_16x16x32_bf16 v[232:235], v[40:43], a[164:167], v[232:235]\n\t"
    "ds_write_b64 v26, v[8:9] offset:512\n\t"
    "v_cvt_pk_bf16_f32 v10, v163, v167\n\t"
    "v_cvt_pk_bf16_f32 v11, v171, v175\n\t"
    "v_mfma_f32_16x16x32_bf16 v[236:239], v[36:39], a[168:171], v[236:239]\n\t"
    "ds_write_b64 v26, v[10:11] offset:768\n\t"
    "buffer_store_dwordx4 v[108:111], v25, s[52:55], s62 offen offset:768 sc0 sc1\n\t"
    "v_mul_f32 v178, v178, v54\n\t"
    "v_mul_f32 v179, v179, v55\n\t"
    "v_mfma_f32_16x16x32_bf16 v[236:239], v[40:43], a[172:175], v[236:239]\n\t"
    "v_mul_f32 v182, v182, v54\n\t"
    "v_mul_f32 v183, v183, v55\n\t"
    "v_mul_f32 v186, v186, v54\n\t"
    "v_mul_f32 v187, v187, v55\n\t"
    "v_mul_f32 v190, v190, v54\n\t"
    "v_mfma_f32_16x16x32_bf16 v[240:243], v[36:39], a[176:179], v[240:243]\n\t"
    "v_mul_f32 v191, v191, v55\n\t"
    "v_cvt_pk_bf16_f32 v20, v178, v182\n\t"
    "v_cvt_pk_bf16_f32 v21, v186, v190\n\t"
    "v_mfma_f32_16x16x32_bf16 v[240:243], v[40:43], a[180:183], v[240:243]\n\t"
    "ds_write_b64 v26, v[20:21] offset:640\n\t"
    "v_cvt_pk_bf16_f32 v22, v179, v183\n\t"
    "v_cvt_pk_bf16_f32 v23, v187, v191\n\t"
    "v_mfma_f32_16x16x32_bf16 v[244:247], v[36:39], a[184:187], v[244:247]\n\t"
    "ds_write_b64 v26, v[22:23] offset:896\n\t"
    "ds_read_b64_tr_b16 v[136:137], v28 offset:512\n\t"
    "ds_read_b64_tr_b16 v[138:139], v29 offset:512\n\t"
    "ds_read_b64_tr_b16 v[140:141], v28 offset:768\n\t"
    "ds_read_b64_tr_b16 v[142:143], v29 offset:768\n\t"
    "v_mfma_f32_16x16x32_bf16 v[244:247], v[40:43], a[188:191], v[244:247]\n\t"
    "buffer_store_dwordx4 v[128:131], v25, s[52:55], s63 offen offset:0 sc0 sc1\n\t"
    "s_waitcnt lgkmcnt(8)\n\t"
    "v_mul_f32 v192, v192, v56\n\t"
    "v_mul_f32 v193, v193, v57\n\t"
    "v_mul_f32 v196, v196, v56\n\t"
    "v_mfma_f32_16x16x32_bf16 v[248:251], v[36:39], a[240:243], v[248:251]\n\t"
    "v_mul_f32 v197, v197, v57\n\t"
    "v_mul_f32 v200, v200, v56\n\t"
    "v_mul_f32 v201, v201, v57\n\t"
    "v_mul_f32 v204, v204, v56\n\t"
    "v_mul_f32 v205, v205, v57\n\t"
    "v_mfma_f32_16x16x32_bf16 v[248:251], v[40:43], a[244:247], v[248:251]\n\t"
    "v_cvt_pk_bf16_f32 v8, v192, v196\n\t"
    "v_cvt_pk_bf16_f32 v9, v200, v204\n\t"
    "v_mfma_f32_16x16x32_bf16 v[252:255], v[36:39], a[248:251], v[252:255]\n\t"
    "ds_write_b64 v26, v[8:9] offset:4096\n\t"
    "v_cvt_pk_bf16_f32 v10, v193, v197\n\t"
    "v_cvt_pk_bf16_f32 v11, v201, v205\n\t"
    "v_mfma_f32_16x16x32_bf16 v[252:255], v[40:43], a[252:255], v[252:255]\n\t"
    "ds_write_b64 v26, v[10:11] offset:4352\n\t"
    "buffer_store_dwordx4 v[132:135], v25, s[52:55], s63 offen offset:256 sc0 sc1\n\t"
    "v_mul_f32 v208, v208, v56\n\t"
    "v_mul_f32 v209, v209, v57\n\t"
    "; EPILOGUE\n\t"
    "v_mul_f32 v212, v212, v56\n\t"
    "v_mul_f32 v213, v213, v57\n\t"
    "v_mul_f32 v216, v216, v56\n\t"
    "v_mfma_f32_16x16x32_bf16 a[236:239], a[232:235], a[192:195], a[236:239]\n\t"
    "v_mul_f32 v217, v217, v57\n\t"
    "v_mul_f32 v220, v220, v56\n\t"
    "v_mul_f32 v221, v221, v57\n\t"
    "v_mfma_f32_16x16x32_bf16 a[236:239], a[232:235], a[196:199], a[236:239]\n\t"
    "v_cvt_pk_bf16_f32 v20, v208, v212\n\t"
    "v_cvt_pk_bf16_f32 v21, v216, v220\n\t"
    "ds_write_b64 v26, v[20:21] offset:4224\n\t"
    "v_cvt_pk_bf16_f32 v22, v209, v213\n\t"
    "v_mfma_f32_16x16x32_bf16 a[236:239], a[232:235], a[200:203], a[236:239]\n\t"
    "v_cvt_pk_bf16_f32 v23, v217, v221\n\t"
    "ds_write_b64 v26, v[22:23] offset:4480\n\t"
    "s_waitcnt lgkmcnt(4)\n\t"
    "buffer_store_dwordx4 v[136:139], v25, s[52:55], s63 offen offset:512 sc0 sc1\n\t"
    "ds_read_b64_tr_b16 v[160:161], v28 offset:4096\n\t"
    "ds_read_b64_tr_b16 v[162:163], v29 offset:4096\n\t"
    "ds_read_b64_tr_b16 v[164:165], v28 offset:4352\n\t"
    "ds_read_b64_tr_b16 v[166:167], v29 offset:4352\n\t"
    "v_accvgpr_read_b32 v60, a228\n\t"
    "v_accvgpr_read_b32 v61, a229\n\t"
    "v_mfma_f32_16x16x32_bf16 a[236:239], a[232:235], a[204:207], a[236:239]\n\t"
    "s_add_u32 s62, s70, 16384\n\t"
    "v_add_f32 v60, v60, v61\n\t"
    "v_rcp_f32 v34, v60\n\t"
    "v_add_f32 v31, v31, v60\n\t"
    "v_min_f32 v32, v32, v60\n\t"
    "v_mfma_f32_16x16x32_bf16 a[236:239], a[232:235], v[96:99], a[236:239]\n\t"
    "ds_bpermute_b32 v60, v30, v34 offset:0\n\t"
    "ds_bpermute_b32 v61, v30, v34 offset:4\n\t"
    "ds_bpermute_b32 v62, v30, v34 offset:8\n\t"
    "ds_bpermute_b32 v63, v30, v34 offset:12\n\t"
    "v_pk_mul_f32 v[194:195], v[194:195], v[58:59]\n\t"
    "v_pk_mul_f32 v[198:199], v[198:199], v[58:59]\n\t"
    "v_pk_mul_f32 v[202:203], v[202:203], v[58:59]\n\t"
    "v_mfma_f32_16x16x32_bf16 a[236:239], a[232:235], v[100:103], a[236:239]\n\t"
    "v_pk_mul_f32 v[206:207], v[206:207], v[58:59]\n\t"
    "v_cvt_pk_bf16_f32 v8, v194, v198\n\t"
    "v_cvt_pk_bf16_f32 v9, v202, v206\n\t"
    "v_mfma_f32_16x16x32_bf16 a[236:239], a[232:235], v[104:107], a[236:239]\n\t"
    "ds_write_b64 v26, v[8:9] offset:4608\n\t"
    "v_cvt_pk_bf16_f32 v10, v195, v199\n\t"
    "v_cvt_pk_bf16_f32 v11, v203, v207\n\t"
    "ds_write_b64 v26, v[10:11] offset:4864\n\t"
    "buffer_store_dwordx4 v[140:143], v25, s[52:55], s63 offen offset:768 sc0 sc1\n\t"
    "v_pk_mul_f32 v[210:211], v[210:211], v[58:59]\n\t"
    "v_mfma_f32_16x16x32_bf16 a[236:239], a[232:235], v[108:111], a[236:239]\n\t"
    "v_pk_mul_f32 v[214:215], v[214:215], v[58:59]\n\t"
    "v_pk_mul_f32 v[218:219], v[218:219], v[58:59]\n\t"
    "v_pk_mul_f32 v[222:223], v[222:223], v[58:59]\n\t"
    "v_mfma_f32_16x16x32_bf16 a[236:239], a[232:235], v[128:131], a[236:239]\n\t"
    "v_cvt_pk_bf16_f32 v20, v210, v214\n\t"
    "v_cvt_pk_bf16_f32 v21, v218, v222\n\t"
    "ds_write_b64 v26, v[20:21] offset:4736\n\t"
    "v_cvt_pk_bf16_f32 v22, v211, v215\n\t"
    "v_mfma_f32_16x16x32_bf16 a[236:239], a[232:235], v[132:135], a[236:239]\n\t"
    "v_cvt_pk_bf16_f32 v23, v219, v223\n\t"
    "ds_write_b64 v26, v[22:23] offset:4992\n\t"
    "ds_read_b64_tr_b16 v[168:169], v28 offset:4608\n\t"
    "ds_read_b64_tr_b16 v[170:171], v29 offset:4608\n\t"
    "ds_read_b64_tr_b16 v[172:173], v28 offset:4864\n\t"
    "ds_read_b64_tr_b16 v[174:175], v29 offset:4864\n\t"
    "s_waitcnt lgkmcnt(14)\n\t"
    "buffer_store_dwordx4 v[160:163], v25, s[52:55], s61 offen offset:0 sc0 sc1\n\t"
    "v_mfma_f32_16x16x32_bf16 a[236:239], a[232:235], v[160:163], a[236:239]\n\t"
    "s_waitcnt lgkmcnt(10)\n\t"
    "v_pk_mul_f32 v[224:225], v[224:225], v[60:61]\n\t"
    "v_pk_mul_f32 v[228:229], v[228:229], v[60:61]\n\t"
    "v_mfma_f32_16x16x32_bf16 a[236:239], a[232:235], v[136:139], a[236:239]\n\t"
    "v_pk_mul_f32 v[232:233], v[232:233], v[60:61]\n\t"
    "v_pk_mul_f32 v[236:237], v[236:237], v[60:61]\n\t"
    "v_cvt_pk_bf16_f32 v8, v224, v228\n\t"
    "v_mfma_f32_16x16x32_bf16 a[236:239], a[232:235], v[140:143], a[236:239]\n\t"
    "v_cvt_pk_bf16_f32 v9, v232, v236\n\t"
    "ds_write_b64 v26, v[8:9] offset:0\n\t"
    "v_cvt_pk_bf16_f32 v10, v225, v229\n\t"
    "v_cvt_pk_bf16_f32 v11, v233, v237\n\t"
    "ds_write_b64 v26, v[10:11] offset:256\n\t"
    "buffer_store_dwordx4 v[164:167], v25, s[52:55], s61 offen offset:256 sc0 sc1\n\t"
    "v_mfma_f32_16x16x32_bf16 a[236:239], a[232:235], v[164:167], a[236:239]\n\t"
    "v_pk_mul_f32 v[240:241], v[240:241], v[60:61]\n\t"
    "v_pk_mul_f32 v[244:245], v[244:245], v[60:61]\n\t"
    "v_pk_mul_f32 v[248:249], v[248:249], v[60:61]\n\t"
    "v_pk_mul_f32 v[252:253], v[252:253], v[60:61]\n\t"
    "v_cvt_pk_bf16_f32 v20, v240, v244\n\t"
    "v_cvt_pk_bf16_f32 v21, v248, v252\n\t"
    "ds_write_b64 v26, v[20:21] offset:128\n\t"
    "v_cvt_pk_bf16_f32 v22, v241, v245\n\t"
    "v_cvt_pk_bf16_f32 v23, v249, v253\n\t"
    "ds_write_b64 v26, v[22:23] offset:384\n\t"
    "s_waitcnt lgkmcnt(6)\n\t"
    "buffer_store_dwordx4 v[168:171], v25, s[52:55], s61 offen offset:512 sc0 sc1\n\t"
    "v_mfma_f32_16x16x32_bf16 a[236:239], a[232:235], v[168:171], a[236:239]\n\t"
    "ds_read_b64_tr_b16 v[192:193], v28 offset:0\n\t"
    "ds_read_b64_tr_b16 v[194:195], v29 offset:0\n\t"
    "ds_read_b64_tr_b16 v[196:197], v28 offset:256\n\t"
    "ds_read_b64_tr_b16 v[198:199], v29 offset:256\n\t"
    "v_pk_mul_f32 v[226:227], v[226:227], v[62:63]\n\t"
    "v_pk_mul_f32 v[230:231], v[230:231], v[62:63]\n\t"
    "v_pk_mul_f32 v[234:235], v[234:235], v[62:63]\n\t"
    "v_pk_mul_f32 v[238:239], v[238:239], v[62:63]\n\t"
    "v_cvt_pk_bf16_f32 v8, v226, v230\n\t"
    "v_cvt_pk_bf16_f32 v9, v234, v238\n\t"
    "ds_write_b64 v26, v[8:9] offset:512\n\t"
    "v_cvt_pk_bf16_f32 v10, v227, v231\n\t"
    "v_cvt_pk_bf16_f32 v11, v235, v239\n\t"
    "ds_write_b64 v26, v[10:11] offset:768\n\t"
    "s_waitcnt lgkmcnt(10)\n\t"
    "buffer_store_dwordx4 v[172:175], v25, s[52:55], s61 offen offset:768 sc0 sc1\n\t"
    "v_mfma_f32_16x16x32_bf16 a[236:239], a[232:235], v[172:175], a[236:239]\n\t"
    "s_waitcnt lgkmcnt(4)\n\t"
    "buffer_store_dwordx4 v[192:195], v25, s[52:55], s62 offen offset:0 sc0 sc1\n\t"
    "v_mfma_f32_16x16x32_bf16 a[236:239], a[232:235], v[192:195], a[236:239]\n\t"
    "v_pk_mul_f32 v[242:243], v[242:243], v[62:63]\n\t"
    "v_pk_mul_f32 v[246:247], v[246:247], v[62:63]\n\t"
    "v_pk_mul_f32 v[250:251], v[250:251], v[62:63]\n\t"
    "v_pk_mul_f32 v[254:255], v[254:255], v[62:63]\n\t"
    "v_cvt_pk_bf16_f32 v20, v242, v246\n\t"
    "v_cvt_pk_bf16_f32 v21, v250, v254\n\t"
    "ds_write_b64 v26, v[20:21] offset:640\n\t"
    "v_cvt_pk_bf16_f32 v22, v243, v247\n\t"
    "v_cvt_pk_bf16_f32 v23, v251, v255\n\t"
    "ds_write_b64 v26, v[22:23] offset:896\n\t"
    "ds_read_b64_tr_b16 v[200:201], v28 offset:512\n\t"
    "ds_read_b64_tr_b16 v[202:203], v29 offset:512\n\t"
    "ds_read_b64_tr_b16 v[204:205], v28 offset:768\n\t"
    "ds_read_b64_tr_b16 v[206:207], v29 offset:768\n\t"
    "s_waitcnt lgkmcnt(8)\n\t"
    "buffer_store_dwordx4 v[196:199], v25, s[52:55], s62 offen offset:256 sc0 sc1\n\t"
    "v_mfma_f32_16x16x32_bf16 a[236:239], a[232:235], v[196:199], a[236:239]\n\t"
    "s_waitcnt lgkmcnt(2)\n\t"
    "buffer_store_dwordx4 v[200:203], v25, s[52:55], s62 offen offset:512 sc0 sc1\n\t"
    "v_mfma_f32_16x16x32_bf16 a[236:239], a[232:235], v[200:203], a[236:239]\n\t"
    "s_waitcnt lgkmcnt(0)\n\t"
    "buffer_store_dwordx4 v[204:207], v25, s[52:55], s62 offen offset:768 sc0 sc1\n\t"
    "v_mfma_f32_16x16x32_bf16 a[236:239], a[232:235], v[204:207], a[236:239]\n\t"
    "s_mov_b32 s63, 0x5d800000\n\t"
    "s_mov_b32 s64, 0x3a800000\n\t"
    "v_cmp_nge_f32 vcc, s63, v31\n\t"
    "s_nop 1\n\t"
    "s_mov_b64 s[72:73], vcc\n\t"
    "v_cmp_nle_f32 vcc, s64, v32\n\t"
    "s_nop 1\n\t"
    "s_or_b64 s[72:73], s[72:73], vcc\n\t"
    "s_and_b64 s[72:73], s[72:73], 0xffff\n\t"
    "v_accvgpr_read_b32 v33, a236\n\t"
    "v_cmp_u_f32 vcc, v33, v33\n\t"
    "s_nop 1\n\t"
    "s_or_b64 s[72:73], s[72:73], vcc\n\t"
    "s_cmp_lg_u64 s[72:73], 0\n\t"
    "s_cselect_b32 %[flag], 1, 0\n\t"
    "s_branch DONE%=\n\t"
    "SPIN5L0%=:\n\t"
    "s_sleep 1\n\t"
    "ds_read_b32 v33, v27 offset:3072\n\t"
    "s_waitcnt lgkmcnt(0)\n\t"
    "v_cmp_gt_u32 vcc, v35, v33\n\t"
    "s_nop 4\n\t"
    "s_cbranch_vccnz SPIN5L0%=\n\t"
    "s_branch RET5L0%=\n\t"
    "SPIN5L1%=:\n\t"
    "s_sleep 1\n\t"
    "ds_read_b32 v33, v27 offset:3072\n\t"
    "s_waitcnt lgkmcnt(0)\n\t"
    "v_cmp_gt_u32 vcc, v35, v33\n\t"
    "s_nop 4\n\t"
    "s_cbranch_vccnz SPIN5L1%=\n\t"
    "s_branch RET5L1%=\n\t"
    "SPIN5L2%=:\n\t"
    "s_sleep 1\n\t"
    "ds_read_b32 v33, v27 offset:3072\n\t"
    "s_waitcnt lgkmcnt(0)\n\t"
    "v_cmp_gt_u32 vcc, v35, v33\n\t"
    "s_nop 4\n\t"
    "s_cbranch_vccnz SPIN5L2%=\n\t"
    "s_branch RET5L2%=\n\t"
    "SPIN5L3%=:\n\t"
    "s_sleep 1\n\t"
    "ds_read_b32 v33, v27 offset:3072\n\t"
    "s_waitcnt lgkmcnt(0)\n\t"
    "v_cmp_gt_u32 vcc, v35, v33\n\t"
    "s_nop 4\n\t"
    "s_cbranch_vccnz SPIN5L3%=\n\t"
    "s_branch RET5L3%=\n\t"
    "SPIN5P1%=:\n\t"
    "s_sleep 1\n\t"
    "ds_read_b32 v33, v27 offset:3072\n\t"
    "s_waitcnt lgkmcnt(0)\n\t"
    "v_cmp_gt_u32 vcc, v35, v33\n\t"
    "s_nop 4\n\t"
    "s_cbranch_vccnz SPIN5P1%=\n\t"
    "s_branch RET5P1%=\n\t"
    "SPIN5P30%=:\n\t"
    "s_sleep 1\n\t"
    "ds_read_b32 v33, v27 offset:3072\n\t"
    "s_waitcnt lgkmcnt(0)\n\t"
    "v_cmp_gt_u32 vcc, v35, v33\n\t"
    "s_nop 4\n\t"
    "s_cbranch_vccnz SPIN5P30%=\n\t"
    "s_branch RET5P30%=\n\t"
    "SPIN5P31%=:\n\t"
    "s_sleep 1\n\t"
    "ds_read_b32 v33, v27 offset:3072\n\t"
    "s_waitcnt lgkmcnt(0)\n\t"
    "v_cmp_gt_u32 vcc, v35, v33\n\t"
    "s_nop 4\n\t"
    "s_cbranch_vccnz SPIN5P31%=\n\t"
    "s_branch RET5P31%=\n\t"
    "VAR4%=:\n\t"
    "s_mov_b32 s72, s58\n\t"
    "s_add_u32 s58, s58, 16384\n\t"
    "s_and_b32 s58, s58, 0x7ffff\n\t"
    "s_mov_b32 s73, s58\n\t"
    "s_add_u32 s58, s58, 16384\n\t"
    "s_and_b32 s58, s58, 0x7ffff\n\t"
    "s_mov_b32 s74, s58\n\t"
    "s_add_u32 s58, s58, 16384\n\t"
    "s_and_b32 s58, s58, 0x7ffff\n\t"
    "s_waitcnt lgkmcnt(0)\n\t"
    "s_add_u32 s40, s40, %[hkv]\n\t"
    "s_addc_u32 s41, s41, 0\n\t"
    "s_and_b32 s41, s41, 0xffff\n\t"
    "s_add_u32 s48, s48, %[hqo]\n\t"
    "s_addc_u32 s49, s49, 0\n\t"
    "s_and_b32 s49, s49, 0xffff\n\t"
    "s_add_u32 m0, s59, 0\n\t"
    "s_nop 0\n\t"
    "buffer_load_dwordx4 v20, s[40:43], s72 offen offset:0 nt lds\n\t"
    "buffer_load_dwordx4 v21, s[40:43], s72 offen offset:1024 nt lds\n\t"
    "buffer_load_dwordx4 v22, s[40:43], s72 offen offset:2048 nt lds\n\t"
    "buffer_load_dwordx4 v23, s[40:43], s72 offen offset:3072 nt lds\n\t"
    "s_add_u32 s64, s70, 0\n\t"
    "s_add_u32 m0, s71, 0\n\t"
    "s_nop 0\n\t"
    "buffer_load_dwordx4 v20, s[48:51], s64 offen offset:0 nt lds\n\t"
    "buffer_load_dwordx4 v21, s[48:51], s64 offen offset:1024 nt lds\n\t"
    "buffer_load_dwordx4 v22, s[48:51], s64 offen offset:2048 nt lds\n\t"
    "buffer_load_dwordx4 v23, s[48:51], s64 offen offset:3072 nt lds\n\t"
    "s_add_u32 s64, s70, 4096\n\t"
    "s_add_u32 m0, s71, 4096\n\t"
    "s_nop 0\n\t"
    "buffer_load_dwordx4 v20, s[48:51], s64 offen offset:0 nt lds\n\t"
    "buffer_load_dwordx4 v21, s[48:51], s64 offen offset:1024 nt lds\n\t"
    "buffer_load_dwordx4 v22, s[48:51], s64 offen offset:2048 nt lds\n\t"
    "buffer_load_dwordx4 v23, s[48:51], s64 offen offset:3072 nt lds\n\t"
    "s_add_u32 s44, s44, %[hkv]\n\t"
    "s_addc_u32 s45, s45, 0\n\t"
    "s_and_b32 s45, s45, 0xffff\n\t"
    "s_add_u32 s52, s52, %[hqo]\n\t"
    "s_addc_u32 s53, s53, 0\n\t"
    "s_and_b32 s53, s53, 0xffff\n\t"
    "v_xor_b32 v38, v36, v37\n\t"
    "v_lshlrev_b32 v38, 4, v38\n\t"
    "v_lshl_add_u32 v38, v37, 8, v38\n\t"
    "v_xor_b32 v8, 0, v38\n\t"
    "v_xor_b32 v9, 64, v38\n\t"
    "v_xor_b32 v10, 128, v38\n\t"
    "v_xor_b32 v11, 192, v38\n\t"
    "v_add_u32 v160, s71, v8\n\t"
    "v_add_u32 v164, s75, v8\n\t"
    "v_add_u32 v161, s71, v9\n\t"
    "v_add_u32 v165, s75, v9\n\t"
    "v_add_u32 v162, s71, v10\n\t"
    "v_add_u32 v166, s75, v10\n\t"
    "v_add_u32 v163, s71, v11\n\t"
    "v_add_u32 v167, s75, v11\n\t"
    "s_mov_b32 s64, %[lds]\n\t"
    "v_add_u32 v8, s64, v8\n\t"
    "v_add_u32 v9, s64, v9\n\t"
    "v_add_u32 v10, s64, v10\n\t"
    "v_add_u32 v11, s64, v11\n\t"
    "v_bfe_u32 v38, v33, 2, 2\n\t"
    "v_and_b32 v39, 1, v33\n\t"
    "v_lshlrev_b32 v39, 3, v39\n\t"
    "v_lshl_add_u32 v26, v36, 2, v38\n\t"
    "v_lshl_add_u32 v39, v26, 8, v39\n\t"
    "v_bfe_u32 v26, v33, 1, 1\n\t"
    "v_lshlrev_b32 v26, 2, v26\n\t"
    "v_xor_b32 v38, v38, v26\n\t"
    "v_and_b32 v26, 1, v36\n\t"
    "v_lshlrev_b32 v26, 3, v26\n\t"
    "v_xor_b32 v38, v38, v26\n\t"
    "v_lshl_add_u32 v39, v38, 4, v39\n\t"
    "s_add_u32 s64, %[lds], 65536\n\t"
    "v_xor_b32 v12, 0, v39\n\t"
    "v_xor_b32 v13, 16, v39\n\t"
    "v_xor_b32 v14, 32, v39\n\t"
    "v_xor_b32 v15, 48, v39\n\t"
    "v_xor_b32 v16, 128, v39\n\t"
    "v_xor_b32 v17, 144, v39\n\t"
    "v_xor_b32 v18, 160, v39\n\t"
    "v_xor_b32 v19, 176, v39\n\t"
    "v_add_u32 v12, s64, v12\n\t"
    "v_add_u32 v13, s64, v13\n\t"
    "v_add_u32 v14, s64, v14\n\t"
    "v_add_u32 v15, s64, v15\n\t"
    "v_add_u32 v16, s64, v16\n\t"
    "v_add_u32 v17, s64, v17\n\t"
    "v_add_u32 v18, s64, v18\n\t"
    "v_add_u32 v19, s64, v19\n\t"
    "s_lshl_b32 s64, s56, 13\n\t"
    "s_add_u32 s64, s64, %[lds]\n\t"
    "v_and_b32 v38, 3, v33\n\t"
    "v_lshlrev_b32 v38, 3, v38\n\t"
    "v_bfe_u32 v39, v33, 2, 2\n\t"
    "v_lshl_add_u32 v38, v39, 5, v38\n\t"
    "v_lshl_add_u32 v38, v36, 10, v38\n\t"
    "v_and_b32 v39, 1, v36\n\t"
    "v_lshlrev_b32 v39, 5, v39\n\t"
    "v_xor_b32 v38, v39, v38\n\t"
    "v_add_u32 v26, s64, v38\n\t"
    "v_and_b32 v38, 3, v33\n\t"
    "v_lshlrev_b32 v38, 6, v38\n\t"
    "v_bfe_u32 v39, v33, 2, 2\n\t"
    "v_lshl_add_u32 v38, v39, 3, v38\n\t"
    "v_lshl_add_u32 v38, v36, 10, v38\n\t"
    "v_and_b32 v39, 1, v36\n\t"
    "v_lshl_add_u32 v38, v39, 5, v38\n\t"
    "v_add_u32 v28, s64, v38\n\t"
    "v_xor_b32 v38, 32, v38\n\t"
    "v_add_u32 v29, s64, v38\n\t"
    "v_lshlrev_b32 v25, 4, v37\n\t"
    "v_lshl_add_u32 v25, v36, 10, v25\n\t"
    "v_lshlrev_b32 v30, 4, v36\n\t"
    "s_mov_b32 s64, %[lds]\n\t"
    "s_add_u32 s64, s64, 147456\n\t"
    "v_lshl_add_u32 v27, v33, 3, s64\n\t"
    "s_mov_b32 s64, %[lds]\n\t"
    "s_add_u32 s64, s64, 131072\n\t"
    "v_lshl_add_u32 v34, v33, 4, s64\n\t"
    "v_mov_b32 v244, 1\n\t"
    "v_min_u32 v246, 2, v36\n\t"
    "v_lshl_add_u32 v246, v246, 10, v27\n\t"
    "v_add_u32 v246, 6144, v246\n\t"
    "v_add_u32 v39, %[lds], v30\n\t"
    "v_mov_b32 v31, 0\n\t"
    "v_mov_b32 v32, 0x7f000000\n\t"
    "v_accvgpr_write_b32 a232, 0\n\t"
    "v_accvgpr_write_b32 a236, 0\n\t"
    "v_accvgpr_write_b32 a233, 0\n\t"
    "v_accvgpr_write_b32 a237, 0\n\t"
    "v_accvgpr_write_b32 a234, 0\n\t"
    "v_accvgpr_write_b32 a238, 0\n\t"
    "v_accvgpr_write_b32 a235, 0\n\t"
    "v_accvgpr_write_b32 a239, 0\n\t"
    "v_cmp_gt_u32 vcc, 2, v37\n\t"
    "v_mov_b32 v38, 0x3f803f80\n\t"
    "s_nop 0\n\t"
    "v_cndmask_b32 v38, 0, v38, vcc\n\t"
    "v_accvgpr_write_b32 a208, v38\n\t"
    "v_accvgpr_write_b32 a209, v38\n\t"
    "v_accvgpr_write_b32 a210, v38\n\t"
    "v_accvgpr_write_b32 a211, v38\n\t"
    "v_and_b32 v38, 1, v33\n\t"
    "v_cmp_eq_u32 vcc, 0, v38\n\t"
    "v_mov_b32 v38, 0x44444444\n\t"
    "v_mov_b32 v24, 0xeeeeeeee\n\t"
    "v_cndmask_b32 v24, v24, v38, vcc\n\t"
    "v_accvgpr_write_b32 a212, 0\n\t"
    "v_accvgpr_write_b32 a213, 0\n\t"
    "v_accvgpr_write_b32 a214, 0\n\t"
    "v_accvgpr_write_b32 a215, 0\n\t"
    "v_accvgpr_write_b32 a216, 0\n\t"
    "v_accvgpr_write_b32 a217, 0\n\t"
    "v_accvgpr_write_b32 a218, 0\n\t"
    "v_accvgpr_write_b32 a219, 0\n\t"
    "v_accvgpr_write_b32 a220, 0\n\t"
    "v_accvgpr_write_b32 a221, 0\n\t"
    "v_accvgpr_write_b32 a222, 0\n\t"
    "v_accvgpr_write_b32 a223, 0\n\t"
    "v_accvgpr_write_b32 a224, 0\n\t"
    "v_accvgpr_write_b32 a225, 0\n\t"
    "v_accvgpr_write_b32 a226, 0\n\t"
    "v_accvgpr_write_b32 a227, 0\n\t"
    "s_mov_b32 s66, 0x3e0293ee\n\t"
    "s_mov_b32 s67, 0x3e0293ee\n\t"
    "s_mov_b32 s69, 0xffff0000\n\t"
    "s_mov_b32 s63, 0x80008000\n\t"
    "s_waitcnt vmcnt(8)\n\t"
    "s_barrier\n\t"
    "; PSTAMP 1\n\t"
    "s_add_u32 m0, s60, 0\n\t"
    "s_nop 0\n\t"
    "buffer_load_dwordx4 v20, s[44:47], s72 offen offset:0 nt lds\n\t"
    "buffer_load_dwordx4 v22, s[44:47], s72 offen offset:1024 nt lds\n\t"
    "buffer_load_dwordx4 v20, s[44:47], s72 offen offset:2048 nt lds\n\t"
    "buffer_load_dwordx4 v22, s[44:47], s72 offen offset:3072 nt lds\n\t"
    "ds_read_b128 v[192:195], v39 offset:0\n\t"
    "ds_read_b128 v[196:199], v39 offset:64\n\t"
    "ds_read_b128 v[200:203], v39 offset:128\n\t"
    "ds_read_b128 v[204:207], v39 offset:192\n\t"
    "ds_read_b128 a[80:83], v8 offset:0\n\t"
    "ds_read_b128 a[84:87], v9 offset:0\n\t"
    "ds_read_b128 a[88:91], v10 offset:0\n\t"
    "ds_read_b128 a[92:95], v11 offset:0\n\t"
    "ds_read_b128 a[96:99], v8 offset:4096\n\t"
    "ds_read_b128 a[100:103], v9 offset:4096\n\t"
    "ds_read_b128 a[104:107], v10 offset:4096\n\t"
    "ds_read_b128 a[108:111], v11 offset:4096\n\t"
    "ds_read_b128 a[112:115], v8 offset:8192\n\t"
    "ds_read_b128 a[116:119], v9 offset:8192\n\t"
    "ds_read_b128 a[120:123], v10 offset:8192\n\t"
    "ds_read_b128 a[124:127], v11 offset:8192\n\t"
    "ds_read_b128 a[128:131], v8 offset:12288\n\t"
    "ds_read_b128 a[132:135], v9 offset:12288\n\t"
    "ds_read_b128 a[136:139], v10 offset:12288\n\t"
    "ds_read_b128 a[140:143], v11 offset:12288\n\t"
    "s_waitcnt vmcnt(8)\n\t"
    "; PSTAMP 2\n\t"
    "ds_read_b128 v[208:211], v160 offset:0\n\t"
    "ds_read_b128 v[212:215], v161 offset:0\n\t"
    "ds_read_b128 v[216:219], v162 offset:0\n\t"
    "ds_read_b128 v[220:223], v163 offset:0\n\t"
    "s_add_u32 s64, s70, 8192\n\t"
    "s_add_u32 m0, s75, 0\n\t"
    "s_nop 0\n\t"
    "buffer_load_dwordx4 v20, s[48:51], s64 offen offset:0 nt lds\n\t"
    "buffer_load_dwordx4 v21, s[48:51], s64 offen offset:1024 nt lds\n\t"
    "buffer_load_dwordx4 v22, s[48:51], s64 offen offset:2048 nt lds\n\t"
    "buffer_load_dwordx4 v23, s[48:51], s64 offen offset:3072 nt lds\n\t"
    "s_waitcnt lgkmcnt(15)\n\t"
    "v_xor_b32 v192, s63, v192\n\t"
    "v_xor_b32 v193, s63, v193\n\t"
    "v_xor_b32 v194, s63, v194\n\t"
    "v_xor_b32 v195, s63, v195\n\t"
    "s_waitcnt lgkmcnt(15)\n\t"
    "v_xor_b32 v196, s63, v196\n\t"
    "v_xor_b32 v197, s63, v197\n\t"
    "v_xor_b32 v198, s63, v198\n\t"
    "v_xor_b32 v199, s63, v199\n\t"
    "s_waitcnt lgkmcnt(15)\n\t"
    "v_xor_b32 v200, s63, v200\n\t"
    "v_xor_b32 v201, s63, v201\n\t"
    "v_xor_b32 v202, s63, v202\n\t"
    "v_xor_b32 v203, s63, v203\n\t"
    "s_waitcnt lgkmcnt(15)\n\t"
    "v_xor_b32 v204, s63, v204\n\t"
    "v_xor_b32 v205, s63, v205\n\t"
    "v_xor_b32 v206, s63, v206\n\t"
    "v_xor_b32 v207, s63, v207\n\t"
    "; PSTAMP 3\n\t"
    "s_waitcnt lgkmcnt(3)\n\t"
    "v_lshlrev_b32 v168, 16, v208\n\t"
    "v_and_b32 v169, s69, v208\n\t"
    "v_lshlrev_b32 v170, 16, v209\n\t"
    "v_and_b32 v171, s69, v209\n\t"
    "v_pk_mul_f32 v[168:169], v[168:169], s[66:67]\n\t"
    "v_pk_mul_f32 v[170:171], v[170:171], s[66:67]\n\t"
    "v_cvt_pk_bf16_f32 v208, v168, v169\n\t"
    "v_cvt_pk_bf16_f32 v209, v170, v171\n\t"
    "v_accvgpr_write_b32 a0, v208\n\t"
    "v_accvgpr_write_b32 a1, v209\n\t"
    "v_lshlrev_b32 v172, 16, v210\n\t"
    "v_and_b32 v173, s69, v210\n\t"
    "v_lshlrev_b32 v174, 16, v211\n\t"
    "v_and_b32 v175, s69, v211\n\t"
    "v_pk_mul_f32 v[172:173], v[172:173], s[66:67]\n\t"
    "v_pk_mul_f32 v[174:175], v[174:175], s[66:67]\n\t"
    "v_cvt_pk_bf16_f32 v210, v172, v173\n\t"
    "v_cvt_pk_bf16_f32 v211, v174, v175\n\t"
    "v_accvgpr_write_b32 a2, v210\n\t"
    "v_accvgpr_write_b32 a3, v211\n\t"
    "s_waitcnt lgkmcnt(2)\n\t"
    "v_lshlrev_b32 v168, 16, v212\n\t"
    "v_and_b32 v169, s69, v212\n\t"
    "v_lshlrev_b32 v170, 16, v213\n\t"
    "v_and_b32 v171, s69, v213\n\t"
    "v_pk_mul_f32 v[168:169], v[168:169], s[66:67]\n\t"
    "v_pk_mul_f32 v[170:171], v[170:171], s[66:67]\n\t"
    "v_cvt_pk_bf16_f32 v212, v168, v169\n\t"
    "v_cvt_pk_bf16_f32 v213, v170, v171\n\t"
    "v_accvgpr_write_b32 a4, v212\n\t"
    "v_accvgpr_write_b32 a5, v213\n\t"
    "v_lshlrev_b32 v172, 16, v214\n\t"
    "v_and_b32 v173, s69, v214\n\t"
    "v_lshlrev_b32 v174, 16, v215\n\t"
    "v_and_b32 v175, s69, v215\n\t"
    "v_pk_mul_f32 v[172:173], v[172:173], s[66:67]\n\t"
    "v_pk_mul_f32 v[174:175], v[174:175], s[66:67]\n\t"
    "v_cvt_pk_bf16_f32 v214, v172, v173\n\t"
    "v_cvt_pk_bf16_f32 v215, v174, v175\n\t"
    "v_accvgpr_write_b32 a6, v214\n\t"
    "v_accvgpr_write_b32 a7, v215\n\t"
    "v_mfma_f32_16x16x32_bf16 v[44:47], v[192:195], a[0:3], -4.0\n\t"
    "s_waitcnt lgkmcnt(1)\n\t"
    "v_lshlrev_b32 v168, 16, v216\n\t"
    "v_and_b32 v169, s69, v216\n\t"
    "v_lshlrev_b32 v170, 16, v217\n\t"
    "v_and_b32 v171, s69, v217\n\t"
    "v_pk_mul_f32 v[168:169], v[168:169], s[66:67]\n\t"
    "v_pk_mul_f32 v[170:171], v[170:171], s[66:67]\n\t"
    "v_cvt_pk_bf16_f32 v216, v168, v169\n\t"
    "v_cvt_pk_bf16_f32 v217, v170, v171\n\t"
    "v_accvgpr_write_b32 a8, v216\n\t"
    "v_accvgpr_write_b32 a9, v217\n\t"
    "v_lshlrev_b32 v172, 16, v218\n\t"
    "v_and_b32 v173, s69, v218\n\t"
    "v_lshlrev_b32 v174, 16, v219\n\t"
    "v_and_b32 v175, s69, v219\n\t"
    "v_pk_mul_f32 v[172:173], v[172:173], s[66:67]\n\t"
    "v_pk_mul_f32 v[174:175], v[174:175], s[66:67]\n\t"
    "v_cvt_pk_bf16_f32 v218, v172, v173\n\t"
    "v_cvt_pk_bf16_f32 v219, v174, v175\n\t"
    "v_accvgpr_write_b32 a10, v218\n\t"
    "v_accvgpr_write_b32 a11, v219\n\t"
    "v_mfma_f32_16x16x32_bf16 v[44:47], v[196:199], a[4:7], v[44:47]\n\t"
    "s_waitcnt lgkmcnt(0)\n\t"
    "v_lshlrev_b32 v168, 16, v220\n\t"
    "v_and_b32 v169, s69, v220\n\t"
    "v_lshlrev_b32 v170, 16, v221\n\t"
    "v_and_b32 v171, s69, v221\n\t"
    "v_pk_mul_f32 v[168:169], v[168:169], s[66:67]\n\t"
    "v_pk_mul_f32 v[170:171], v[170:171], s[66:67]\n\t"
    "v_cvt_pk_bf16_f32 v220, v168, v169\n\t"
    "v_cvt_pk_bf16_f32 v221, v170, v171\n\t"
    "v_accvgpr_write_b32 a12, v220\n\t"
    "v_accvgpr_write_b32 a13, v221\n\t"
    "v_lshlrev_b32 v172, 16, v222\n\t"
    "v_and_b32 v173, s69, v222\n\t"
    "v_lshlrev_b32 v174, 16, v223\n\t"
    "v_and_b32 v175, s69, v223\n\t"
    "v_pk_mul_f32 v[172:173], v[172:173], s[66:67]\n\t"
    "v_pk_mul_f32 v[174:175], v[174:175], s[66:67]\n\t"
    "v_cvt_pk_bf16_f32 v222, v172, v173\n\t"
    "v_cvt_pk_bf16_f32 v223, v174, v175\n\t"
    "v_accvgpr_write_b32 a14, v222\n\t"
    "v_accvgpr_write_b32 a15, v223\n\t"
    "v_mfma_f32_16x16x32_bf16 v[44:47], v[200:203], a[8:11], v[44:47]\n\t"
    "; PSTAMP 4\n\t"
    "s_nop 0\n\t"
    "v_mfma_f32_16x16x32_bf16 v[44:47], v[204:207], a[12:15], v[44:47]\n\t"
    "; TILE 0\n\t"
    "v_mfma_f32_16x16x32_bf16 v[64:67], a[80:83], a[0:3], v[44:47]\n\t"
    "s_waitcnt vmcnt(8)\n\t"
    "ds_read_b128 v[208:211], v160 offset:4096\n\t"
    "ds_read_b128 v[212:215], v161 offset:4096\n\t"
    "ds_read_b128 v[216:219], v162 offset:4096\n\t"
    "ds_read_b128 v[220:223], v163 offset:4096\n\t"
    "v_mfma_f32_16x16x32_bf16 v[64:67], a[84:87], a[4:7], v[64:67]\n\t"
    "s_add_u32 s64, s70, 12288\n\t"
    "s_add_u32 m0, s75, 4096\n\t"
    "v_mfma_f32_16x16x32_bf16 v[64:67], a[88:91], a[8:11], v[64:67]\n\t"
    "buffer_load_dwordx4 v20, s[48:51], s64 offen offset:0 nt lds\n\t"
    "v_mfma_f32_16x16x32_bf16 v[64:67], a[92:95], a[12:15], v[64:67]\n\t"
    "buffer_load_dwordx4 v21, s[48:51], s64 offen offset:1024 nt lds\n\t"
    "v_mfma_f32_16x16x32_bf16 v[68:71], a[96:99], a[0:3], v[44:47]\n\t"
    "buffer_load_dwordx4 v22, s[48:51], s64 offen offset:2048 nt lds\n\t"
    "s_waitcnt lgkmcnt(3)\n\t"
    "v_lshlrev_b32 v168, 16, v208\n\t"
    "v_and_b32 v169, s69, v208\n\t"
    "v_lshlrev_b32 v170, 16, v209\n\t"
    "v_and_b32 v171, s69, v209\n\t"
    "v_mfma_f32_16x16x32_bf16 v[68:71], a[100:103], a[4:7], v[68:71]\n\t"
    "buffer_load_dwordx4 v23, s[48:51], s64 offen offset:3072 nt lds\n\t"
    "v_pk_mul_f32 v[168:169], v[168:169], s[66:67]\n\t"
    "v_pk_mul_f32 v[170:171], v[170:171], s[66:67]\n\t"
    "v_cvt_pk_bf16_f32 v208, v168, v169\n\t"
    "v_mfma_f32_16x16x32_bf16 v[68:71], a[104:107], a[8:11], v[68:71]\n\t"
    "v_cvt_pk_bf16_f32 v209, v170, v171\n\t"
    "v_accvgpr_write_b32 a16, v208\n\t"
    "v_accvgpr_write_b32 a17, v209\n\t"
    "v_mfma_f32_16x16x32_bf16 v[68:71], a[108:111], a[12:15], v[68:71]\n\t"
    "v_lshlrev_b32 v172, 16, v210\n\t"
    "v_and_b32 v173, s69, v210\n\t"
    "v_lshlrev_b32 v174, 16, v211\n\t"
    "v_and_b32 v175, s69, v211\n\t"
    "v_mfma_f32_16x16x32_bf16 v[72:75], a[112:115], a[0:3], v[44:47]\n\t"
    "v_pk_mul_f32 v[172:173], v[172:173], s[66:67]\n\t"
    "v_pk_mul_f32 v[174:175], v[174:175], s[66:67]\n\t"
    "v_cvt_pk_bf16_f32 v210, v172, v173\n\t"
    "v_exp_f32 v64, v64\n\t"
    "v_mfma_f32_16x16x32_bf16 v[72:75], a[116:119], a[4:7], v[72:75]\n\t"
    "v_cvt_pk_bf16_f32 v211, v174, v175\n\t"
    "v_accvgpr_write_b32 a18, v210\n\t"
    "v_accvgpr_write_b32 a19, v211\n\t"
    "v_exp_f32 v65, v65\n\t"
    "v_mfma_f32_16x16x32_bf16 v[72:75], a[120:123], a[8:11], v[72:75]\n\t"
    "s_waitcnt lgkmcnt(0)\n\t"
    "v_lshlrev_b32 v168, 16, v212\n\t"
    "v_and_b32 v169, s69, v212\n\t"
    "v_lshlrev_b32 v170, 16, v213\n\t"
    "v_and_b32 v171, s69, v213\n\t"
    "v_exp_f32 v66, v66\n\t"
    "v_mfma_f32_16x16x32_bf16 v[72:75], a[124:127], a[12:15], v[72:75]\n\t"
    "v_pk_mul_f32 v[168:169], v[168:169], s[66:67]\n\t"
    "v_pk_mul_f32 v[170:171], v[170:171], s[66:67]\n\t"
    "v_cvt_pk_bf16_f32 v212, v168, v169\n\t"
    "v_exp_f32 v67, v67\n\t"
    "v_mfma_f32_16x16x32_bf16 v[76:79], a[128:131], a[0:3], v[44:47]\n\t"
    "v_cvt_pk_bf16_f32 v213, v170, v171\n\t"
    "v_accvgpr_write_b32 a20, v212\n\t"
    "v_accvgpr_write_b32 a21, v213\n\t"
    "v_exp_f32 v68, v68\n\t"
    "v_mfma_f32_16x16x32_bf16 v[76:79], a[132:135], a[4:7], v[76:79]\n\t"
    "v_lshlrev_b32 v172, 16, v214\n\t"
    "v_and_b32 v173, s69, v214\n\t"
    "v_lshlrev_b32 v174, 16, v215\n\t"
    "v_and_b32 v175, s69, v215\n\t"
    "v_exp_f32 v69, v69\n\t"
    "v_mfma_f32_16x16x32_bf16 v[76:79], a[136:139], a[8:11], v[76:79]\n\t"
    "v_pk_mul_f32 v[172:173], v[172:173], s[66:67]\n\t"
    "v_pk_mul_f32 v[174:175], v[174:175], s[66:67]\n\t"
    "v_cvt_pk_bf16_f32 v214, v172, v173\n\t"
    "v_exp_f32 v70, v70\n\t"
    "v_mfma_f32_16x16x32_bf16 v[76:79], a[140:143], a[12:15], v[76:79]\n\t"
    "v_cvt_pk_bf16_f32 v215, v174, v175\n\t"
    "v_accvgpr_write_b32 a22, v214\n\t"
    "v_accvgpr_write_b32 a23, v215\n\t"
    "v_exp_f32 v71, v71\n\t"
    "v_lshlrev_b32 v168, 16, v216\n\t"
    "v_and_b32 v169, s69, v216\n\t"
    "v_lshlrev_b32 v170, 16, v217\n\t"
    "v_and_b32 v171, s69, v217\n\t"
    "v_exp_f32 v72, v72\n\t"
    "v_pk_mul_f32 v[168:169], v[168:169], s[66:67]\n\t"
    "v_pk_mul_f32 v[170:171], v[170:171], s[66:67]\n\t"
    "v_cvt_pk_bf16_f32 v216, v168, v169\n\t"
    "v_exp_f32 v73, v73\n\t"
    "v_cvt_pk_bf16_f32 v217, v170, v171\n\t"
    "v_accvgpr_write_b32 a24, v216\n\t"
    "v_accvgpr_write_b32 a25, v217\n\t"
    "v_exp_f32 v74, v74\n\t"
    "v_lshlrev_b32 v172, 16, v218\n\t"
    "v_and_b32 v173, s69, v218\n\t"
    "v_lshlrev_b32 v174, 16, v219\n\t"
    "v_and_b32 v175, s69, v219\n\t"
    "v_exp_f32 v75, v75\n\t"
    "v_pk_mul_f32 v[172:173], v[172:173], s[66:67]\n\t"
    "v_pk_mul_f32 v[174:175], v[174:175], s[66:67]\n\t"
    "v_cvt_pk_bf16_f32 v218, v172, v173\n\t"
    "v_exp_f32 v76, v76\n\t"
    "v_cvt_pk_bf16_f32 v219, v174, v175\n\t"
    "v_accvgpr_write_b32 a26, v218\n\t"
    "v_accvgpr_write_b32 a27, v219\n\t"
    "v_exp_f32 v77, v77\n\t"
    "v_lshlrev_b32 v168, 16, v220\n\t"
    "v_and_b32 v169, s69, v220\n\t"
    "v_lshlrev_b32 v170, 16, v221\n\t"
    "v_and_b32 v171, s69, v221\n\t"
    "v_exp_f32 v78, v78\n\t"
    "s_waitcnt vmcnt(8)\n\t"
    "s_barrier\n\t"
    "s_add_u32 m0, s59, 16384\n\t"
    "v_pk_mul_f32 v[168:169], v[168:169], s[66:67]\n\t"
    "v_pk_mul_f32 v[170:171], v[170:171], s[66:67]\n\t"
    "v_cvt_pk_bf16_f32 v220, v168, v169\n\t"
    "v_exp_f32 v79, v79\n\t"
    "ds_read_b64_tr_b16 a[144:145], v12 offset:0\n\t"
    "ds_read_b64_tr_b16 a[146:147], v12 offset:4096\n\t"
    "buffer_load_dwordx4 v20, s[40:43], s73 offen offset:0 nt lds\n\t"
    "v_cvt_pk_bf16_f32 v221, v170, v171\n\t"
    "v_accvgpr_write_b32 a28, v220\n\t"
    "v_accvgpr_write_b32 a29, v221\n\t"
    "ds_read_b64_tr_b16 a[148:149], v12 offset:8192\n\t"
    "ds_read_b64_tr_b16 a[150:151], v12 offset:12288\n\t"
    "buffer_load_dwordx4 v21, s[40:43], s73 offen offset:1024 nt lds\n\t"
    "v_lshlrev_b32 v172, 16, v222\n\t"
    "v_and_b32 v173, s69, v222\n\t"
    "v_lshlrev_b32 v174, 16, v223\n\t"
    "v_and_b32 v175, s69, v223\n\t"
    "ds_read_b64_tr_b16 a[152:153], v13 offset:0\n\t"
    "ds_read_b64_tr_b16 a[154:155], v13 offset:4096\n\t"
    "buffer_load_dwordx4 v22, s[40:43], s73 offen offset:2048 nt lds\n\t"
    "v_pk_mul_f32 v[172:173], v[172:173], s[66:67]\n\t"
    "v_pk_mul_f32 v[174:175], v[174:175], s[66:67]\n\t"
    "v_cvt_pk_bf16_f32 v222, v172, v173\n\t"
    "ds_read_b64_tr_b16 a[156:157], v13 offset:8192\n\t"
    "ds_read_b64_tr_b16 a[158:159], v13 offset:12288\n\t"
    "buffer_load_dwordx4 v23, s[40:43], s73 offen offset:3072 nt lds\n\t"
    "v_cvt_pk_bf16_f32 v223, v174, v175\n\t"
    "v_accvgpr_write_b32 a30, v222\n\t"
    "v_accvgpr_write_b32 a31, v223\n\t"
    "v_mfma_f32_16x16x32_bf16 v[48:51], v[192:195], a[16:19], -4.0\n\t"
    "ds_read_b64_tr_b16 a[160:161], v14 offset:0\n\t"
    "ds_read_b64_tr_b16 a[162:163], v14 offset:4096\n\t"
    "v_mfma_f32_16x16x32_bf16 v[48:51], v[196:199], a[20:23], v[48:51]\n\t"
    "ds_read_b64_tr_b16 a[164:165], v14 offset:8192\n\t"
    "ds_read_b64_tr_b16 a[166:167], v14 offset:12288\n\t"
    "v_mfma_f32_16x16x32_bf16 v[48:51], v[200:203], a[24:27], v[48:51]\n\t"
    "ds_read_b64_tr_b16 a[168:169], v15 offset:0\n\t"
    "ds_read_b64_tr_b16 a[170:171], v15 offset:4096\n\t"
    "v_mfma_f32_16x16x32_bf16 v[48:51], v[204:207], a[28:31], v[48:51]\n\t"
    "ds_read_b64_tr_b16 a[172:173], v15 offset:8192\n\t"
    "ds_read_b64_tr_b16 a[174:175], v15 offset:12288\n\t"
    "v_mfma_f32_16x16x32_bf16 v[80:83], a[80:83], a[16:19], v[48:51]\n\t"
    "s_waitcnt vmcnt(8)\n\t"
    "ds_read_b64_tr_b16 a[176:177], v16 offset:0\n\t"
    "ds_read_b128 v[208:211], v164 offset:0\n\t"
    "ds_read_b128 v[212:215], v165 offset:0\n\t"
    "ds_read_b128 v[216:219], v166 offset:0\n\t"
    "ds_read_b128 v[220:223], v167 offset:0\n\t"
    "v_cvt_pk_bf16_f32 v36, v64, v65\n\t"
    "v_mfma_f32_16x16x32_bf16 v[80:83], a[84:87], a[20:23], v[80:83]\n\t"
    "s_add_u32 m0, s60, 16384\n\t"
    "ds_read_b64_tr_b16 a[178:179], v16 offset:4096\n\t"
    "v_cvt_pk_bf16_f32 v37, v66, v67\n\t"
    "v_mfma_f32_16x16x32_bf16 v[80:83], a[88:91], a[24:27], v[80:83]\n\t"
    "ds_read_b64_tr_b16 a[180:181], v16 offset:8192\n\t"
    "buffer_load_dwordx4 v20, s[44:47], s73 offen offset:0 nt lds\n\t"
    "v_cvt_pk_bf16_f32 v38, v68, v69\n\t"
    "v_mfma_f32_16x16x32_bf16 v[80:83], a[92:95], a[28:31], v[80:83]\n\t"
    "ds_read_b64_tr_b16 a[182:183], v16 offset:12288\n\t"
    "buffer_load_dwordx4 v22, s[44:47], s73 offen offset:1024 nt lds\n\t"
    "v_cvt_pk_bf16_f32 v39, v70, v71\n\t"
    "v_mfma_f32_16x16x32_bf16 v[84:87], a[96:99], a[16:19], v[48:51]\n\t"
    "ds_read_b64_tr_b16 a[184:185], v17 offset:0\n\t"
    "buffer_load_dwordx4 v20, s[44:47], s73 offen offset:2048 nt lds\n\t"
    "v_cvt_pk_bf16_f32 v40, v72, v73\n\t"
    "s_waitcnt lgkmcnt(7)\n\t"
    "v_lshlrev_b32 v168, 16, v208\n\t"
    "v_and_b32 v169, s69, v208\n\t"
    "v_lshlrev_b32 v170, 16, v209\n\t"
    "v_and_b32 v171, s69, v209\n\t"
    "v_mfma_f32_16x16x32_bf16 v[84:87], a[100:103], a[20:23], v[84:87]\n\t"
    "ds_read_b64_tr_b16 a[186:187], v17 offset:4096\n\t"
    "buffer_load_dwordx4 v22, s[44:47], s73 offen offset:3072 nt lds\n\t"
    "v_cvt_pk_bf16_f32 v41, v74, v75\n\t"
    "v_pk_mul_f32 v[168:169], v[168:169], s[66:67]\n\t"
    "v_pk_mul_f32 v[170:171], v[170:171], s[66:67]\n\t"
    "v_cvt_pk_bf16_f32 v208, v168, v169\n\t"
    "v_mfma_f32_16x16x32_bf16 v[84:87], a[104:107], a[24:27], v[84:87]\n\t"
    "ds_read_b64_tr_b16 a[188:189], v17 offset:8192\n\t"
    "v_cvt_pk_bf16_f32 v42, v76, v77\n\t"
    "v_cvt_pk_bf16_f32 v209, v170, v171\n\t"
    "v_accvgpr_write_b32 a32, v208\n\t"
    "v_accvgpr_write_b32 a33, v209\n\t"
    "v_mfma_f32_16x16x32_bf16 v[84:87], a[108:111], a[28:31], v[84:87]\n\t"
    "ds_read_b64_tr_b16 a[190:191], v17 offset:12288\n\t"
    "v_cvt_pk_bf16_f32 v43, v78, v79\n\t"
    "v_lshlrev_b32 v172, 16, v210\n\t"
    "v_and_b32 v173, s69, v210\n\t"
    "v_lshlrev_b32 v174, 16, v211\n\t"
    "v_and_b32 v175, s69, v211\n\t"
    "v_mfma_f32_16x16x32_bf16 v[88:91], a[112:115], a[16:19], v[48:51]\n\t"
    "ds_read_b64_tr_b16 a[192:193], v18 offset:0\n\t"
    "v_pk_mul_f32 v[172:173], v[172:173], s[66:67]\n\t"
    "v_pk_mul_f32 v[174:175], v[174:175], s[66:67]\n\t"
    "v_cvt_pk_bf16_f32 v210, v172, v173\n\t"
    "v_exp_f32 v80, v80\n\t"
    "v_mfma_f32_16x16x32_bf16 v[88:91], a[116:119], a[20:23], v[88:91]\n\t"
    "ds_read_b64_tr_b16 a[194:195], v18 offset:4096\n\t"
    "v_cvt_pk_bf16_f32 v211, v174, v175\n\t"
    "v_accvgpr_write_b32 a34, v210\n\t"
    "v_accvgpr_write_b32 a35, v211\n\t"
    "v_exp_f32 v81, v81\n\t"
    "v_mfma_f32_16x16x32_bf16 v[88:91], a[120:123], a[24:27], v[88:91]\n\t"
    "ds_read_b64_tr_b16 a[196:197], v18 offset:8192\n\t"
    "s_waitcnt lgkmcnt(8)\n\t"
    "v_lshlrev_b32 v168, 16, v212\n\t"
    "v_and_b32 v169, s69, v212\n\t"
    "v_lshlrev_b32 v170, 16, v213\n\t"
    "v_and_b32 v171, s69, v213\n\t"
    "v_exp_f32 v82, v82\n\t"
    "v_mfma_f32_16x16x32_bf16 v[88:91], a[124:127], a[28:31], v[88:91]\n\t"
    "ds_read_b64_tr_b16 a[198:199], v18 offset:12288\n\t"
    "v_pk_mul_f32 v[168:169], v[168:169], s[66:67]\n\t"
    "v_pk_mul_f32 v[170:171], v[170:171], s[66:67]\n\t"
    "v_cvt_pk_bf16_f32 v212, v168, v169\n\t"
    "v_exp_f32 v83, v83\n\t"
    "v_mfma_f32_16x16x32_bf16 v[92:95], a[128:131], a[16:19], v[48:51]\n\t"
    "ds_read_b64_tr_b16 a[200:201], v19 offset:0\n\t"
    "v_cvt_pk_bf16_f32 v213, v170, v171\n\t"
    "v_accvgpr_write_b32 a36, v212\n\t"
    "v_accvgpr_write_b32 a37, v213\n\t"
    "v_exp_f32 v84, v84\n\t"
    "v_mfma_f32_16x16x32_bf16 v[92:95], a[132:135], a[20:23], v[92:95]\n\t"
    "ds_read_b64_tr_b16 a[202:203], v19 offset:4096\n\t"
    "v_lshlrev_b32 v172, 16, v214\n\t"
    "v_and_b32 v173, s69, v214\n\t"
    "v_lshlrev_b32 v174, 16, v215\n\t"
    "v_and_b32 v175, s69, v215\n\t"
    "v_exp_f32 v85, v85\n\t"
    "v_mfma_f32_16x16x32_bf16 v[92:95], a[136:139], a[24:27], v[92:95]\n\t"
    "ds_read_b64_tr_b16 a[204:205], v19 offset:8192\n\t"
    "v_pk_mul_f32 v[172:173], v[172:173], s[66:67]\n\t"
    "v_pk_mul_f32 v[174:175], v[174:175], s[66:67]\n\t"
    "v_cvt_pk_bf16_f32 v214, v172, v173\n\t"
    "v_exp_f32 v86, v86\n\t"
    "v_mfma_f32_16x16x32_bf16 v[92:95], a[140:143], a[28:31], v[92:95]\n\t"
    "ds_read_b64_tr_b16 a[206:207], v19 offset:12288\n\t"
    "v_cvt_pk_bf16_f32 v215, v174, v175\n\t"
    "v_accvgpr_write_b32 a38, v214\n\t"
    "v_accvgpr_write_b32 a39, v215\n\t"
    "v_exp_f32 v87, v87\n\t"
    "v_mfma_f32_16x16x32_bf16 v[96:99], v[36:39], a[144:147], 0\n\t"
    "v_lshlrev_b32 v168, 16, v216\n\t"
    "v_and_b32 v169, s69, v216\n\t"
    "v_lshlrev_b32 v170, 16, v217\n\t"
    "v_and_b32 v171, s69, v217\n\t"
    "v_exp_f32 v88, v88\n\t"
    "v_mfma_f32_16x16x32_bf16 v[96:99], v[40:43], a[148:151], v[96:99]\n\t"
    "v_pk_mul_f32 v[168:169], v[168:169], s[66:67]\n\t"
    "v_pk_mul_f32 v[170:171], v[170:171], s[66:67]\n\t"
    "v_cvt_pk_bf16_f32 v216, v168, v169\n\t"
    "v_exp_f32 v89, v89\n\t"
    "v_mfma_f32_16x16x32_bf16 v[100:103], v[36:39], a[152:155], 0\n\t"
    "v_cvt_pk_bf16_f32 v217, v170, v171\n\t"
    "v_accvgpr_write_b32 a40, v216\n\t"
    "v_accvgpr_write_b32 a41, v217\n\t"
    "v_exp_f32 v90, v90\n\t"
    "v_mfma_f32_16x16x32_bf16 v[100:103], v[40:43], a[156:159], v[100:103]\n\t"
    "v_lshlrev_b32 v172, 16, v218\n\t"
    "v_and_b32 v173, s69, v218\n\t"
    "v_lshlrev_b32 v174, 16, v219\n\t"
    "v_and_b32 v175, s69, v219\n\t"
    "v_exp_f32 v91, v91\n\t"
    "v_mfma_f32_16x16x32_bf16 v[104:107], v[36:39], a[160:163], 0\n\t"
    "v_pk_mul_f32 v[172:173], v[172:173], s[66:67]\n\t"
    "v_pk_mul_f32 v[174:175], v[174:175], s[66:67]\n\t"
    "v_cvt_pk_bf16_f32 v218, v172, v173\n\t"
    "v_exp_f32 v92, v92\n\t"
    "v_mfma_f32_16x16x32_bf16 v[104:107], v[40:43], a[164:167], v[104:107]\n\t"
    "v_cvt_pk_bf16_f32 v219, v174, v175\n\t"
    "v_accvgpr_write_b32 a42, v218\n\t"
    "v_accvgpr_write_b32 a43, v219\n\t"
    "v_exp_f32 v93, v93\n\t"
    "v_mfma_f32_16x16x32_bf16 v[108:111], v[36:39], a[168:171], 0\n\t"
    "v_lshlrev_b32 v168, 16, v220\n\t"
    "v_and_b32 v169, s69, v220\n\t"
    "v_lshlrev_b32 v170, 16, v221\n\t"
    "v_and_b32 v171, s69, v221\n\t"
    "v_exp_f32 v94, v94\n\t"
    "v_mfma_f32_16x16x32_bf16 v[108:111], v[40:43], a[172:175], v[108:111]\n\t"
    "v_pk_mul_f32 v[168:169], v[168:169], s[66:67]\n\t"
    "v_pk_mul_f32 v[170:171], v[170:171], s[66:67]\n\t"
    "v_cvt_pk_bf16_f32 v220, v168, v169\n\t"
    "v_exp_f32 v95, v95\n\t"
    "v_mfma_f32_16x16x32_bf16 v[112:115], v[36:39], a[176:179], 0\n\t"
    "s_waitcnt lgkmcnt(0)\n\t"
    "v_cvt_pk_bf16_f32 v221, v170, v171\n\t"
    "v_accvgpr_write_b32 a44, v220\n\t"
    "v_accvgpr_write_b32 a45, v221\n\t"
    "v_mfma_f32_16x16x32_bf16 v[112:115], v[40:43], a[180:183], v[112:115]\n\t"
    "v_lshlrev_b32 v172, 16, v222\n\t"
    "v_and_b32 v173, s69, v222\n\t"
    "v_lshlrev_b32 v174, 16, v223\n\t"
    "v_and_b32 v175, s69, v223\n\t"
    "v_mfma_f32_16x16x32_bf16 v[116:119], v[36:39], a[184:187], 0\n\t"
    "v_pk_mul_f32 v[172:173], v[172:173], s[66:67]\n\t"
    "v_pk_mul_f32 v[174:175], v[174:175], s[66:67]\n\t"
    "v_cvt_pk_bf16_f32 v222, v172, v173\n\t"
    "v_mfma_f32_16x16x32_bf16 v[116:119], v[40:43], a[188:191], v[116:119]\n\t"
    "v_cvt_pk_bf16_f32 v223, v174, v175\n\t"
    "v_accvgpr_write_b32 a46, v222\n\t"
    "v_accvgpr_write_b32 a47, v223\n\t"
    "v_mfma_f32_16x16x32_bf16 v[120:123], v[36:39], a[192:195], 0\n\t"
    "v_mfma_f32_16x16x32_bf16 v[52:55], v[192:195], a[32:35], -4.0\n\t"
    "v_mfma_f32_16x16x32_bf16 v[120:123], v[40:43], a[196:199], v[120:123]\n\t"
    "v_mfma_f32_16x16x32_bf16 v[52:55], v[196:199], a[36:39], v[52:55]\n\t"
    "v_mfma_f32_16x16x32_bf16 v[124:127], v[36:39], a[200:203], 0\n\t"
    "v_mfma_f32_16x16x32_bf16 v[52:55], v[200:203], a[40:43], v[52:55]\n\t"
    "v_mfma_f32_16x16x32_bf16 v[124:127], v[40:43], a[204:207], v[124:127]\n\t"
    "v_mfma_f32_16x16x32_bf16 v[52:55], v[204:207], a[44:47], v[52:55]\n\t"
    "v_smfmac_f32_16x16x64_bf16 a[212:215], a[208:211], v[36:43], v24\n\t"
    "v_mfma_f32_16x16x32_bf16 v[64:67], a[80:83], a[32:35], v[52:55]\n\t"
    "s_waitcnt vmcnt(8)\n\t"
    "ds_read_b128 v[208:211], v164 offset:4096\n\t"
    "ds_read_b128 v[212:215], v165 offset:4096\n\t"
    "ds_read_b128 v[216:219], v166 offset:4096\n\t"
    "ds_read_b128 v[220:223], v167 offset:4096\n\t"
    "v_cvt_pk_bf16_f32 v36, v80, v81\n\t"
    "v_mfma_f32_16x16x32_bf16 v[64:67], a[84:87], a[36:39], v[64:67]\n\t"
    "s_add_u32 m0, s59, 32768\n\t"
    "v_cvt_pk_bf16_f32 v37, v82, v83\n\t"
    "v_mfma_f32_16x16x32_bf16 v[64:67], a[88:91], a[40:43], v[64:67]\n\t"
    "buffer_load_dwordx4 v20, s[40:43], s74 offen offset:0 nt lds\n\t"
    "v_cvt_pk_bf16_f32 v38, v84, v85\n\t"
    "v_mfma_f32_16x16x32_bf16 v[64:67], a[92:95], a[44:47], v[64:67]\n\t"
    "buffer_load_dwordx4 v21, s[40:43], s74 offen offset:1024 nt lds\n\t"
    "v_cvt_pk_bf16_f32 v39, v86, v87\n\t"
    "v_mfma_f32_16x16x32_bf16 v[68:71], a[96:99], a[32:35], v[52:55]\n\t"
    "buffer_load_dwordx4 v22, s[40:43], s74 offen offset:2048 nt lds\n\t"
    "v_cvt_pk_bf16_f32 v40, v88, v89\n\t"
    "s_waitcnt lgkmcnt(3)\n\t"
    "v_lshlrev_b32 v168, 16, v208\n\t"
    "v_and_b32 v169, s69, v208\n\t"
    "v_lshlrev_b32 v170, 16, v209\n\t"
    "v_and_b32 v171, s69, v209\n\t"
    "v_mfma_f32_16x16x32_bf16 v[68:71], a[100:103], a[36:39], v[68:71]\n\t"
    "buffer_load_dwordx4 v23, s[40:43], s74 offen offset:3072 nt lds\n\t"
    "v_cvt_pk_bf16_f32 v41, v90, v91\n\t"
    "v_pk_mul_f32 v[168:169], v[168:169], s[66:67]\n\t"
    "v_pk_mul_f32 v[170:171], v[170:171], s[66:67]\n\t"
    "v_cvt_pk_bf16_f32 v208, v168, v169\n\t"
    "v_mfma_f32_16x16x32_bf16 v[68:71], a[104:107], a[40:43], v[68:71]\n\t"
    "v_cvt_pk_bf16_f32 v42, v92, v93\n\t"
    "v_cvt_pk_bf16_f32 v209, v170, v171\n\t"
    "v_accvgpr_write_b32 a48, v208\n\t"
    "v_accvgpr_write_b32 a49, v209\n\t"
    "v_mfma_f32_16x16x32_bf16 v[68:71], a[108:111], a[44:47], v[68:71]\n\t"
    "v_cvt_pk_bf16_f32 v43, v94, v95\n\t"
    "v_lshlrev_b32 v172, 16, v210\n\t"
    "v_and_b32 v173, s69, v210\n\t"
    "v_lshlrev_b32 v174, 16, v211\n\t"
    "v_and_b32 v175, s69, v211\n\t"
    "v_mfma_f32_16x16x32_bf16 v[72:75], a[112:115], a[32:35], v[52:55]\n\t"
    "v_pk_mul_f32 v[172:173], v[172:173], s[66:67]\n\t"
    "v_pk_mul_f32 v[174:175], v[174:175], s[66:67]\n\t"
    "v_cvt_pk_bf16_f32 v210, v172, v173\n\t"
    "v_exp_f32 v64, v64\n\t"
    "v_mfma_f32_16x16x32_bf16 v[72:75], a[116:119], a[36:39], v[72:75]\n\t"
    "v_cvt_pk_bf16_f32 v211, v174, v175\n\t"
    "v_accvgpr_write_b32 a50, v210\n\t"
    "v_accvgpr_write_b32 a51, v211\n\t"
    "v_exp_f32 v65, v65\n\t"
    "v_mfma_f32_16x16x32_bf16 v[72:75], a[120:123], a[40:43], v[72:75]\n\t"
    "s_waitcnt lgkmcnt(0)\n\t"
    "v_lshlrev_b32 v168, 16, v212\n\t"
    "v_and_b32 v169, s69, v212\n\t"
    "v_lshlrev_b32 v170, 16, v213\n\t"
    "v_and_b32 v171, s69, v213\n\t"
    "v_exp_f32 v66, v66\n\t"
    "v_mfma_f32_16x16x32_bf16 v[72:75], a[124:127], a[44:47], v[72:75]\n\t"
    "v_pk_mul_f32 v[168:169], v[168:169], s[66:67]\n\t"
    "v_pk_mul_f32 v[170:171], v[170:171], s[66:67]\n\t"
    "v_cvt_pk_bf16_f32 v212, v168, v169\n\t"
    "v_exp_f32 v67, v67\n\t"
    "v_mfma_f32_16x16x32_bf16 v[76:79], a[128:131], a[32:35], v[52:55]\n\t"
    "v_cvt_pk_bf16_f32 v213, v170, v171\n\t"
    "v_accvgpr_write_b32 a52, v212\n\t"
    "v_accvgpr_write_b32 a53, v213\n\t"
    "v_exp_f32 v68, v68\n\t"
    "v_mfma_f32_16x16x32_bf16 v[76:79], a[132:135], a[36:39], v[76:79]\n\t"
    "v_lshlrev_b32 v172, 16, v214\n\t"
    "v_and_b32 v173, s69, v214\n\t"
    "v_lshlrev_b32 v174, 16, v215\n\t"
    "v_and_b32 v175, s69, v215\n\t"
    "v_exp_f32 v69, v69\n\t"
    "v_mfma_f32_16x16x32_bf16 v[76:79], a[136:139], a[40:43], v[76:79]\n\t"
    "v_pk_mul_f32 v[172:173], v[172:173], s[66:67]\n\t"
    "v_pk_mul_f32 v[174:175], v[174:175], s[66:67]\n\t"
    "v_cvt_pk_bf16_f32 v214, v172, v173\n\t"
    "v_exp_f32 v70, v70\n\t"
    "v_mfma_f32_16x16x32_bf16 v[76:79], a[140:143], a[44:47], v[76:79]\n\t"
    "v_cvt_pk_bf16_f32 v215, v174, v175\n\t"
    "v_accvgpr_write_b32 a54, v214\n\t"
    "v_accvgpr_write_b32 a55, v215\n\t"
    "v_exp_f32 v71, v71\n\t"
    "v_mfma_f32_16x16x32_bf16 v[128:131], v[36:39], a[144:147], 0\n\t"
    "s_add_u32 m0, s60, 32768\n\t"
    "v_lshlrev_b32 v168, 16, v216\n\t"
    "v_and_b32 v169, s69, v216\n\t"
    "v_lshlrev_b32 v170, 16, v217\n\t"
    "v_and_b32 v171, s69, v217\n\t"
    "v_exp_f32 v72, v72\n\t"
    "v_mfma_f32_16x16x32_bf16 v[128:131], v[40:43], a[148:151], v[128:131]\n\t"
    "buffer_load_dwordx4 v20, s[44:47], s74 offen offset:0 nt lds\n\t"
    "v_pk_mul_f32 v[168:169], v[168:169], s[66:67]\n\t"
    "v_pk_mul_f32 v[170:171], v[170:171], s[66:67]\n\t"
    "v_cvt_pk_bf16_f32 v216, v168, v169\n\t"
    "v_exp_f32 v73, v73\n\t"
    "v_mfma_f32_16x16x32_bf16 v[132:135], v[36:39], a[152:155], 0\n\t"
    "buffer_load_dwordx4 v22, s[44:47], s74 offen offset:1024 nt lds\n\t"
    "v_cvt_pk_bf16_f32 v217, v170, v171\n\t"
    "v_accvgpr_write_b32 a56, v216\n\t"
    "v_accvgpr_write_b32 a57, v217\n\t"
    "v_exp_f32 v74, v74\n\t"
    "v_mfma_f32_16x16x32_bf16 v[132:135], v[40:43], a[156:159], v[132:135]\n\t"
    "buffer_load_dwordx4 v20, s[44:47], s74 offen offset:2048 nt lds\n\t"
    "v_lshlrev_b32 v172, 16, v218\n\t"
    "v_and_b32 v173, s69, v218\n\t"
    "v_lshlrev_b32 v174, 16, v219\n\t"
    "v_and_b32 v175, s69, v219\n\t"
    "v_exp_f32 v75, v75\n\t"
    "v_mfma_f32_16x16x32_bf16 v[136:139], v[36:39], a[160:163], 0\n\t"
    "buffer_load_dwordx4 v22, s[44:47], s74 offen offset:3072 nt lds\n\t"
    "v_pk_mul_f32 v[172:173], v[172:173], s[66:67]\n\t"
    "v_pk_mul_f32 v[174:175], v[174:175], s[66:67]\n\t"
    "v_cvt_pk_bf16_f32 v218, v172, v173\n\t"
    "v_exp_f32 v76, v76\n\t"
    "v_mfma_f32_16x16x32_bf16 v[136:139], v[40:43], a[164:167], v[136:139]\n\t"
    "v_cvt_pk_bf16_f32 v219, v174, v175\n\t"
    "v_accvgpr_write_b32 a58, v218\n\t"
    "v_accvgpr_write_b32 a59, v219\n\t"
    "v_exp_f32 v77, v77\n\t"
    "v_mfma_f32_16x16x32_bf16 v[140:143], v[36:39], a[168:171], 0\n\t"
    "v_lshlrev_b32 v168, 16, v220\n\t"
    "v_and_b32 v169, s69, v220\n\t"
    "v_lshlrev_b32 v170, 16, v221\n\t"
    "v_and_b32 v171, s69, v221\n\t"
    "v_exp_f32 v78, v78\n\t"
    "v_mfma_f32_16x16x32_bf16 v[140:143], v[40:43], a[172:175], v[140:143]\n\t"
    "v_pk_mul_f32 v[168:169], v[168:169], s[66:67]\n\t"
    "v_pk_mul_f32 v[170:171], v[170:171], s[66:67]\n\t"
    "v_cvt_pk_bf16_f32 v220, v168, v169\n\t"
    "v_exp_f32 v79, v79\n\t"
    "v_mfma_f32_16x16x32_bf16 v[144:147], v[36:39], a[176:179], 0\n\t"
    "v_cvt_pk_bf16_f32 v221, v170, v171\n\t"
    "v_accvgpr_write_b32 a60, v220\n\t"
    "v_accvgpr_write_b32 a61, v221\n\t"
    "v_mfma_f32_16x16x32_bf16 v[144:147], v[40:43], a[180:183], v[144:147]\n\t"
    "v_lshlrev_b32 v172, 16, v222\n\t"
    "v_and_b32 v173, s69, v222\n\t"
    "v_lshlrev_b32 v174, 16, v223\n\t"
    "v_and_b32 v175, s69, v223\n\t"
    "v_mfma_f32_16x16x32_bf16 v[148:151], v[36:39], a[184:187], 0\n\t"
    "v_pk_mul_f32 v[172:173], v[172:173], s[66:67]\n\t"
    "v_pk_mul_f32 v[174:175], v[174:175], s[66:67]\n\t"
    "v_cvt_pk_bf16_f32 v222, v172, v173\n\t"
    "v_mfma_f32_16x16x32_bf16 v[148:151], v[40:43], a[188:191], v[148:151]\n\t"
    "v_cvt_pk_bf16_f32 v223, v174, v175\n\t"
    "v_accvgpr_write_b32 a62, v222\n\t"
    "v_accvgpr_write_b32 a63, v223\n\t"
    "v_mfma_f32_16x16x32_bf16 v[152:155], v[36:39], a[192:195], 0\n\t"
    "v_mfma_f32_16x16x32_bf16 v[56:59], v[192:195], a[48:51], -4.0\n\t"
    "v_mfma_f32_16x16x32_bf16 v[152:155], v[40:43], a[196:199], v[152:155]\n\t"
    "v_mfma_f32_16x16x32_bf16 v[56:59], v[196:199], a[52:55], v[56:59]\n\t"
    "v_mfma_f32_16x16x32_bf16 v[156:159], v[36:39], a[200:203], 0\n\t"
    "v_mfma_f32_16x16x32_bf16 v[56:59], v[200:203], a[56:59], v[56:59]\n\t"
    "v_mfma_f32_16x16x32_bf16 v[156:159], v[40:43], a[204:207], v[156:159]\n\t"
    "v_mfma_f32_16x16x32_bf16 v[56:59], v[204:207], a[60:63], v[56:59]\n\t"
    "v_smfmac_f32_16x16x64_bf16 a[216:219], a[208:211], v[36:43], v24\n\t"
    "s_waitcnt vmcnt(8)\n\t"
    "s_barrier\n\t"
    "v_mfma_f32_16x16x32_bf16 v[80:83], a[80:83], a[48:51], v[56:59]\n\t"
    "ds_read_b128 a[80:83], v8 offset:16384\n\t"
    "v_cvt_pk_bf16_f32 v36, v64, v65\n\t"
    "v_mfma_f32_16x16x32_bf16 v[80:83], a[84:87], a[52:55], v[80:83]\n\t"
    "ds_read_b128 a[84:87], v9 offset:16384\n\t"
    "v_cvt_pk_bf16_f32 v37, v66, v67\n\t"
    "v_mfma_f32_16x16x32_bf16 v[80:83], a[88:91], a[56:59], v[80:83]\n\t"
    "ds_read_b128 a[88:91], v10 offset:16384\n\t"
    "v_cvt_pk_bf16_f32 v38, v68, v69\n\t"
    "v_mfma_f32_16x16x32_bf16 v[80:83], a[92:95], a[60:63], v[80:83]\n\t"
    "ds_read_b128 a[92:95], v11 offset:16384\n\t"
    "v_cvt_pk_bf16_f32 v39, v70, v71\n\t"
    "v_mfma_f32_16x16x32_bf16 v[84:87], a[96:99], a[48:51], v[56:59]\n\t"
    "ds_read_b128 a[96:99], v8 offset:20480\n\t"
    "v_cvt_pk_bf16_f32 v40, v72, v73\n\t"
    "v_mov_b32 v192, 0\n\t"
    "v_mov_b32 v193, 0\n\t"
    "v_mfma_f32_16x16x32_bf16 v[84:87], a[100:103], a[52:55], v[84:87]\n\t"
    "ds_read_b128 a[100:103], v9 offset:20480\n\t"
    "v_cvt_pk_bf16_f32 v41, v74, v75\n\t"
    "v_mov_b32 v194, 0\n\t"
    "v_mfma_f32_16x16x32_bf16 v[84:87], a[104:107], a[56:59], v[84:87]\n\t"
    "ds_read_b128 a[104:107], v10 offset:20480\n\t"
    "v_cvt_pk_bf16_f32 v42, v76, v77\n\t"
    "v_mov_b32 v195, 0\n\t"
    "v_mfma_f32_16x16x32_bf16 v[84:87], a[108:111], a[60:63], v[84:87]\n\t"
    "ds_read_b128 a[108:111], v11 offset:20480\n\t"
    "v_cvt_pk_bf16_f32 v43, v78, v79\n\t"
    "v_mov_b32 v196, 0\n\t"
    "v_mov_b32 v197, 0\n\t"
    "v_mfma_f32_16x16x32_bf16 v[88:91], a[112:115], a[48:51], v[56:59]\n\t"
    "ds_read_b64_tr_b16 a[240:241], v18 offset:16384\n\t"
    "v_mov_b32 v198, 0\n\t"
    "v_exp_f32 v80, v80\n\t"
    "v_mfma_f32_16x16x32_bf16 v[88:91], a[116:119], a[52:55], v[88:91]\n\t"
    "ds_read_b64_tr_b16 a[242:243], v18 offset:20480\n\t"
    "v_mov_b32 v199, 0\n\t"
    "v_exp_f32 v81, v81\n\t"
    "v_mfma_f32_16x16x32_bf16 v[88:91], a[120:123], a[56:59], v[88:91]\n\t"
    "ds_read_b64_tr_b16 a[244:245], v18 offset:24576\n\t"
    "v_mov_b32 v200, 0\n\t"
    "v_mov_b32 v201, 0\n\t"
    "v_exp_f32 v82, v82\n\t"
    "v_mfma_f32_16x16x32_bf16 v[88:91], a[124:127], a[60:63], v[88:91]\n\t"
    "ds_read_b64_tr_b16 a[246:247], v18 offset:28672\n\t"
    "v_mov_b32 v202, 0\n\t"
    "v_exp_f32 v83, v83\n\t"
    "v_mfma_f32_16x16x32_bf16 v[92:95], a[128:131], a[48:51], v[56:59]\n\t"
    "ds_read_b64_tr_b16 a[248:249], v19 offset:16384\n\t"
    "v_mov_b32 v203, 0\n\t"
    "v_exp_f32 v84, v84\n\t"
    "v_mfma_f32_16x16x32_bf16 v[92:95], a[132:135], a[52:55], v[92:95]\n\t"
    "ds_read_b64_tr_b16 a[250:251], v19 offset:20480\n\t"
    "v_mov_b32 v204, 0\n\t"
    "v_mov_b32 v205, 0\n\t"
    "v_exp_f32 v85, v85\n\t"
    "v_mfma_f32_16x16x32_bf16 v[92:95], a[136:139], a[56:59], v[92:95]\n\t"
    "ds_read_b64_tr_b16 a[252:253], v19 offset:24576\n\t"
    "v_mov_b32 v206, 0\n\t"
    "v_exp_f32 v86, v86\n\t"
    "v_mfma_f32_16x16x32_bf16 v[92:95], a[140:143], a[60:63], v[92:95]\n\t"
    "ds_read_b64_tr_b16 a[254:255], v19 offset:28672\n\t"
    "v_mov_b32 v207, 0\n\t"
    "v_exp_f32 v87, v87\n\t"
    "v_mfma_f32_16x16x32_bf16 v[160:163], v[36:39], a[144:147], 0\n\t"
    "v_mov_b32 v208, 0\n\t"
    "v_mov_b32 v209, 0\n\t"
    "v_exp_f32 v88, v88\n\t"
    "v_mfma_f32_16x16x32_bf16 v[160:163], v[40:43], a[148:151], v[160:163]\n\t"
    "v_mov_b32 v210, 0\n\t"
    "v_exp_f32 v89, v89\n\t"
    "v_mfma_f32_16x16x32_bf16 v[164:167], v[36:39], a[152:155], 0\n\t"
    "v_mov_b32 v211, 0\n\t"
    "v_exp_f32 v90, v90\n\t"
    "v_mfma_f32_16x16x32_bf16 v[164:167], v[40:43], a[156:159], v[164:167]\n\t"
    "v_mov_b32 v212, 0\n\t"
    "v_mov_b32 v213, 0\n\t"
    "v_exp_f32 v91, v91\n\t"
    "v_mfma_f32_16x16x32_bf16 v[168:171], v[36:39], a[160:163], 0\n\t"
    "v_mov_b32 v214, 0\n\t"
    "v_exp_f32 v92, v92\n\t"
    "v_mfma_f32_16x16x32_bf16 v[168:171], v[40:43], a[164:167], v[168:171]\n\t"
    "v_mov_b32 v215, 0\n\t"
    "v_exp_f32 v93, v93\n\t"
    "v_mfma_f32_16x16x32_bf16 v[172:175], v[36:39], a[168:171], 0\n\t"
    "v_mov_b32 v216, 0\n\t"
    "v_mov_b32 v217, 0\n\t"
    "v_exp_f32 v94, v94\n\t"
    "v_mfma_f32_16x16x32_bf16 v[172:175], v[40:43], a[172:175], v[172:175]\n\t"
    "v_mov_b32 v218, 0\n\t"
    "v_exp_f32 v95, v95\n\t"
    "v_mfma_f32_16x16x32_bf16 v[176:179], v[36:39], a[176:179], 0\n\t"
    "ds_read_b128 a[112:115], v8 offset:24576\n\t"
    "v_mov_b32 v219, 0\n\t"
    "v_mfma_f32_16x16x32_bf16 v[176:179], v[40:43], a[180:183], v[176:179]\n\t"
    "ds_read_b128 a[116:119], v9 offset:24576\n\t"
    "v_mov_b32 v220, 0\n\t"
    "v_mov_b32 v221, 0\n\t"
    "v_mfma_f32_16x16x32_bf16 v[180:183], v[36:39], a[184:187], 0\n\t"
    "ds_read_b128 a[120:123], v10 offset:24576\n\t"
    "v_mov_b32 v222, 0\n\t"
    "v_mfma_f32_16x16x32_bf16 v[180:183], v[40:43], a[188:191], v[180:183]\n\t"
    "ds_read_b128 a[124:127], v11 offset:24576\n\t"
    "v_mov_b32 v223, 0\n\t"
    "v_mfma_f32_16x16x32_bf16 v[184:187], v[36:39], a[192:195], 0\n\t"
    "ds_read_b128 a[128:131], v8 offset:28672\n\t"
    "v_mfma_f32_16x16x32_bf16 v[184:187], v[40:43], a[196:199], v[184:187]\n\t"
    "ds_read_b128 a[132:135], v9 offset:28672\n\t"
    "v_mfma_f32_16x16x32_bf16 v[188:191], v[36:39], a[200:203], 0\n\t"
    "ds_read_b128 a[136:139], v10 offset:28672\n\t"
    "v_mfma_f32_16x16x32_bf16 v[188:191], v[40:43], a[204:207], v[188:191]\n\t"
    "ds_read_b128 a[140:143], v11 offset:28672\n\t"
    "v_smfmac_f32_16x16x64_bf16 a[220:223], a[208:211], v[36:43], v24\n\t"
    "s_waitcnt lgkmcnt(7)\n\t"
    "; TILE 1\n\t"
    "v_mfma_f32_16x16x32_bf16 v[64:67], a[80:83], a[0:3], v[44:47]\n\t"
    "s_sub_u32 s72, s58, 12288\n\t"
    "s_sub_u32 s73, s58, 8192\n\t"
    "s_sub_u32 s74, s58, 4096\n\t"
    "s_add_u32 m0, s59, 36864\n\t"
    "v_cvt_pk_bf16_f32 v36, v80, v81\n\t"
    "v_mfma_f32_16x16x32_bf16 v[64:67], a[84:87], a[4:7], v[64:67]\n\t"
    "buffer_load_dwordx4 v20, s[40:43], s72 offen offset:0 nt lds\n\t"
    "v_cvt_pk_bf16_f32 v37, v82, v83\n\t"
    "v_mfma_f32_16x16x32_bf16 v[64:67], a[88:91], a[8:11], v[64:67]\n\t"
    "buffer_load_dwordx4 v21, s[40:43], s72 offen offset:1024 nt lds\n\t"
    "v_cvt_pk_bf16_f32 v38, v84, v85\n\t"
    "v_mfma_f32_16x16x32_bf16 v[64:67], a[92:95], a[12:15], v[64:67]\n\t"
    "buffer_load_dwordx4 v22, s[40:43], s72 offen offset:2048 nt lds\n\t"
    "v_cvt_pk_bf16_f32 v39, v86, v87\n\t"
    "v_mfma_f32_16x16x32_bf16 v[68:71], a[96:99], a[0:3], v[44:47]\n\t"
    "buffer_load_dwordx4 v23, s[40:43], s72 offen offset:3072 nt lds\n\t"
    "s_add_u32 m0, s59, 40960\n\t"
    "v_cvt_pk_bf16_f32 v40, v88, v89\n\t"
    "v_mfma_f32_16x16x32_bf16 v[68:71], a[100:103], a[4:7], v[68:71]\n\t"
    "buffer_load_dwordx4 v20, s[40:43], s73 offen offset:0 nt lds\n\t"
    "v_cvt_pk_bf16_f32 v41, v90, v91\n\t"
    "v_mfma_f32_16x16x32_bf16 v[68:71], a[104:107], a[8:11], v[68:71]\n\t"
    "buffer_load_dwordx4 v21, s[40:43], s73 offen offset:1024 nt lds\n\t"
    "v_cvt_pk_bf16_f32 v42, v92, v93\n\t"
    "v_mfma_f32_16x16x32_bf16 v[68:71], a[108:111], a[12:15], v[68:71]\n\t"
    "buffer_load_dwordx4 v22, s[40:43], s73 offen offset:2048 nt lds\n\t"
    "v_cvt_pk_bf16_f32 v43, v94, v95\n\t"
    "v_mfma_f32_16x16x32_bf16 v[72:75], a[112:115], a[0:3], v[44:47]\n\t"
    "s_waitcnt lgkmcnt(0)\n\t"
    "buffer_load_dwordx4 v23, s[40:43], s73 offen offset:3072 nt lds\n\t"
    "v_exp_f32 v64, v64\n\t"
    "v_mfma_f32_16x16x32_bf16 v[72:75], a[116:119], a[4:7], v[72:75]\n\t"
    "v_exp_f32 v65, v65\n\t"
    "v_mfma_f32_16x16x32_bf16 v[72:75], a[120:123], a[8:11], v[72:75]\n\t"
    "v_exp_f32 v66, v66\n\t"
    "v_mfma_f32_16x16x32_bf16 v[72:75], a[124:127], a[12:15], v[72:75]\n\t"
    "v_exp_f32 v67, v67\n\t"
    "v_mfma_f32_16x16x32_bf16 v[76:79], a[128:131], a[0:3], v[44:47]\n\t"
    "v_exp_f32 v68, v68\n\t"
    "v_mfma_f32_16x16x32_bf16 v[76:79], a[132:135], a[4:7], v[76:79]\n\t"
    "v_exp_f32 v69, v69\n\t"
    "v_mfma_f32_16x16x32_bf16 v[76:79], a[136:139], a[8:11], v[76:79]\n\t"
    "v_exp_f32 v70, v70\n\t"
    "v_mfma_f32_16x16x32_bf16 v[76:79], a[140:143], a[12:15], v[76:79]\n\t"
    "v_exp_f32 v71, v71\n\t"
    "v_mfma_f32_16x16x32_bf16 v[192:195], v[36:39], a[144:147], v[192:195]\n\t"
    "v_exp_f32 v72, v72\n\t"
    "v_mfma_f32_16x16x32_bf16 v[192:195], v[40:43], a[148:151], v[192:195]\n\t"
    "v_exp_f32 v73, v73\n\t"
    "v_mfma_f32_16x16x32_bf16 v[196:199], v[36:39], a[152:155], v[196:199]\n\t"
    "v_exp_f32 v74, v74\n\t"
    "v_mfma_f32_16x16x32_bf16 v[196:199], v[40:43], a[156:159], v[196:199]\n\t"
    "v_exp_f32 v75, v75\n\t"
    "v_mfma_f32_16x16x32_bf16 v[200:203], v[36:39], a[160:163], v[200:203]\n\t"
    "v_exp_f32 v76, v76\n\t"
    "v_mfma_f32_16x16x32_bf16 v[200:203], v[40:43], a[164:167], v[200:203]\n\t"
    "v_exp_f32 v77, v77\n\t"
    "v_mfma_f32_16x16x32_bf16 v[204:207], v[36:39], a[168:171], v[204:207]\n\t"
    "v_exp_f32 v78, v78\n\t"
    "v_mfma_f32_16x16x32_bf16 v[204:207], v[40:43], a[172:175], v[204:207]\n\t"
    "s_add_u32 m0, s59, 45056\n\t"
    "v_exp_f32 v79, v79\n\t"
    "v_mfma_f32_16x16x32_bf16 v[208:211], v[36:39], a[176:179], v[208:211]\n\t"
    "ds_read_b64_tr_b16 a[144:145], v12 offset:16384\n\t"
    "ds_read_b64_tr_b16 a[146:147], v12 offset:20480\n\t"
    "buffer_load_dwordx4 v20, s[40:43], s74 offen offset:0 nt lds\n\t"
    "v_add_u32 v244, 1, v244\n\t"
    "v_mfma_f32_16x16x32_bf16 v[208:211], v[40:43], a[180:183], v[208:211]\n\t"
    "ds_write_b32 v27, v244 offset:3072\n\t"
    "ds_read_b64_tr_b16 a[148:149], v12 offset:24576\n\t"
    "ds_read_b64_tr_b16 a[150:151], v12 offset:28672\n\t"
    "v_mfma_f32_16x16x32_bf16 v[212:215], v[36:39], a[184:187], v[212:215]\n\t"
    "ds_write_b32 v27, v244 offset:4096\n\t"
    "ds_read_b64_tr_b16 a[152:153], v13 offset:16384\n\t"
    "ds_read_b64_tr_b16 a[154:155], v13 offset:20480\n\t"
    "buffer_load_dwordx4 v21, s[40:43], s74 offen offset:1024 nt lds\n\t"
    "v_mfma_f32_16x16x32_bf16 v[212:215], v[40:43], a[188:191], v[212:215]\n\t"
    "ds_write_b32 v27, v244 offset:5120\n\t"
    "ds_read_b64_tr_b16 a[156:157], v13 offset:24576\n\t"
    "ds_read_b64_tr_b16 a[158:159], v13 offset:28672\n\t"
    "v_mfma_f32_16x16x32_bf16 v[216:219], v[36:39], a[192:195], v[216:219]\n\t"
    "ds_read_b64_tr_b16 a[160:161], v14 offset:16384\n\t"
    "ds_read_b64_tr_b16 a[162:163], v14 offset:20480\n\t"
    "buffer_load_dwordx4 v22, s[40:43], s74 offen offset:2048 nt lds\n\t"
    "v_mfma_f32_16x16x32_bf16 v[216:219], v[40:43], a[196:199], v[216:219]\n\t"
    "ds_read_b64_tr_b16 a[164:165], v14 offset:24576\n\t"
    "ds_read_b64_tr_b16 a[166:167], v14 offset:28672\n\t"
    "v_mfma_f32_16x16x32_bf16 v[220:223], v[36:39], a[200:203], v[220:223]\n\t"
    "ds_read_b64_tr_b16 a[168:169], v15 offset:16384\n\t"
    "ds_read_b64_tr_b16 a[170:171], v15 offset:20480\n\t"
    "buffer_load_dwordx4 v23, s[40:43], s74 offen offset:3072 nt lds\n\t"
    "v_mfma_f32_16x16x32_bf16 v[220:223], v[40:43], a[204:207], v[220:223]\n\t"
    "s_add_u32 m0, s59, 49152\n\t"
    "ds_read_b64_tr_b16 a[172:173], v15 offset:24576\n\t"
    "ds_read_b64_tr_b16 a[174:175], v15 offset:28672\n\t"
    "v_smfmac_f32_16x16x64_bf16 a[224:227], a[208:211], v[36:43], v24\n\t"
    "buffer_load_dwordx4 v20, s[40:43], s58 offen offset:0 nt lds\n\t"
    "v_mfma_f32_16x16x32_bf16 v[80:83], a[80:83], a[16:19], v[48:51]\n\t"
    "ds_read_b64_tr_b16 a[176:177], v16 offset:16384\n\t"
    "v_cvt_pk_bf16_f32 v36, v64, v65\n\t"
    "v_mfma_f32_16x16x32_bf16 v[80:83], a[84:87], a[20:23], v[80:83]\n\t"
    "ds_read_b64_tr_b16 a[178:179], v16 offset:20480\n\t"
    "buffer_load_dwordx4 v21, s[40:43], s58 offen offset:1024 nt lds\n\t"
    "v_cvt_pk_bf16_f32 v37, v66, v67\n\t"
    "v_mfma_f32_16x16x32_bf16 v[80:83], a[88:91], a[24:27], v[80:83]\n\t"
    "ds_read_b64_tr_b16 a[180:181], v16 offset:24576\n\t"
    "v_cvt_pk_bf16_f32 v38, v68, v69\n\t"
    "v_mfma_f32_16x16x32_bf16 v[80:83], a[92:95], a[28:31], v[80:83]\n\t"
    "ds_read_b64_tr_b16 a[182:183], v16 offset:28672\n\t"
    "buffer_load_dwordx4 v22, s[40:43], s58 offen offset:2048 nt lds\n\t"
    "v_cvt_pk_bf16_f32 v39, v70, v71\n\t"
    "v_mfma_f32_16x16x32_bf16 v[84:87], a[96:99], a[16:19], v[48:51]\n\t"
    "ds_read_b64_tr_b16 a[184:185], v17 offset:16384\n\t"
    "v_cvt_pk_bf16_f32 v40, v72, v73\n\t"
    "v_mfma_f32_16x16x32_bf16 v[84:87], a[100:103], a[20:23], v[84:87]\n\t"
    "ds_read_b64_tr_b16 a[186:187], v17 offset:20480\n\t"
    "buffer_load_dwordx4 v23, s[40:43], s58 offen offset:3072 nt lds\n\t"
    "v_cvt_pk_bf16_f32 v41, v74, v75\n\t"
    "v_mfma_f32_16x16x32_bf16 v[84:87], a[104:107], a[24:27], v[84:87]\n\t"
    "s_add_u32 m0, s60, 36864\n\t"
    "ds_read_b64_tr_b16 a[188:189], v17 offset:24576\n\t"
    "v_cvt_pk_bf16_f32 v42, v76, v77\n\t"
    "v_mfma_f32_16x16x32_bf16 v[84:87], a[108:111], a[28:31], v[84:87]\n\t"
    "ds_read_b64_tr_b16 a[190:191], v17 offset:28672\n\t"
    "buffer_load_dwordx4 v20, s[44:47], s72 offen offset:0 nt lds\n\t"
    "v_cvt_pk_bf16_f32 v43, v78, v79\n\t"
    "v_mfma_f32_16x16x32_bf16 v[88:91], a[112:115], a[16:19], v[48:51]\n\t"
    "v_exp_f32 v80, v80\n\t"
    "v_mfma_f32_16x16x32_bf16 v[88:91], a[116:119], a[20:23], v[88:91]\n\t"
    "v_exp_f32 v81, v81\n\t"
    "v_mfma_f32_16x16x32_bf16 v[88:91], a[120:123], a[24:27], v[88:91]\n\t"
    "v_exp_f32 v82, v82\n\t"
    "v_mfma_f32_16x16x32_bf16 v[88:91], a[124:127], a[28:31], v[88:91]\n\t"
    "v_exp_f32 v83, v83\n\t"
    "v_mfma_f32_16x16x32_bf16 v[92:95], a[128:131], a[16:19], v[48:51]\n\t"
    "v_exp_f32 v84, v84\n\t"
    "v_mfma_f32_16x16x32_bf16 v[92:95], a[132:135], a[20:23], v[92:95]\n\t"
    "v_exp_f32 v85, v85\n\t"
    "v_mfma_f32_16x16x32_bf16 v[92:95], a[136:139], a[24:27], v[92:95]\n\t"
    "v_exp_f32 v86, v86\n\t"
    "v_mfma_f32_16x16x32_bf16 v[92:95], a[140:143], a[28:31], v[92:95]\n\t"
    "s_waitcnt lgkmcnt(0)\n\t"
    "v_exp_f32 v87, v87\n\t"
    "v_mfma_f32_16x16x32_bf16 v[96:99], v[36:39], a[144:147], v[96:99]\n\t"
    "v_exp_f32 v88, v88\n\t"
    "v_mfma_f32_16x16x32_bf16 v[96:99], v[40:43], a[148:151], v[96:99]\n\t"
    "v_exp_f32 v89, v89\n\t"
    "v_mfma_f32_16x16x32_bf16 v[100:103], v[36:39], a[152:155], v[100:103]\n\t"
    "v_exp_f32 v90, v90\n\t"
    "v_mfma_f32_16x16x32_bf16 v[100:103], v[40:43], a[156:159], v[100:103]\n\t"
    "v_exp_f32 v91, v91\n\t"
    "v_mfma_f32_16x16x32_bf16 v[104:107], v[36:39], a[160:163], v[104:107]\n\t"
    "v_exp_f32 v92, v92\n\t"
    "v_mfma_f32_16x16x32_bf16 v[104:107], v[40:43], a[164:167], v[104:107]\n\t"
    "v_exp_f32 v93, v93\n\t"
    "v_mfma_f32_16x16x32_bf16 v[108:111], v[36:39], a[168:171], v[108:111]\n\t"
    "v_exp_f32 v94, v94\n\t"
    "v_mfma_f32_16x16x32_bf16 v[108:111], v[40:43], a[172:175], v[108:111]\n\t"
    "v_exp_f32 v95, v95\n\t"
    "v_mfma_f32_16x16x32_bf16 v[112:115], v[36:39], a[176:179], v[112:115]\n\t"
    "buffer_load_dwordx4 v22, s[44:47], s72 offen offset:1024 nt lds\n\t"
    "v_mfma_f32_16x16x32_bf16 v[112:115], v[40:43], a[180:183], v[112:115]\n\t"
    "buffer_load_dwordx4 v20, s[44:47], s72 offen offset:2048 nt lds\n\t"
    "v_mfma_f32_16x16x32_bf16 v[116:119], v[36:39], a[184:187], v[116:119]\n\t"
    "buffer_load_dwordx4 v22, s[44:47], s72 offen offset:3072 nt lds\n\t"
    "s_add_u32 m0, s60, 40960\n\t"
    "v_mfma_f32_16x16x32_bf16 v[116:119], v[40:43], a[188:191], v[116:119]\n\t"
    "buffer_load_dwordx4 v20, s[44:47], s73 offen offset:0 nt lds\n\t"
    "v_mfma_f32_16x16x32_bf16 v[120:123], v[36:39], a[240:243], v[120:123]\n\t"
    "buffer_load_dwordx4 v22, s[44:47], s73 offen offset:1024 nt lds\n\t"
    "v_mfma_f32_16x16x32_bf16 v[120:123], v[40:43], a[244:247], v[120:123]\n\t"
    "buffer_load_dwordx4 v20, s[44:47], s73 offen offset:2048 nt lds\n\t"
    "v_mfma_f32_16x16x32_bf16 v[124:127], v[36:39], a[248:251], v[124:127]\n\t"
    "buffer_load_dwordx4 v22, s[44:47], s73 offen offset:3072 nt lds\n\t"
    "s_add_u32 m0, s60, 45056\n\t"
    "v_mfma_f32_16x16x32_bf16 v[124:127], v[40:43], a[252:255], v[124:127]\n\t"
    "buffer_load_dwordx4 v20, s[44:47], s74 offen offset:0 nt lds\n\t"
    "v_smfmac_f32_16x16x64_bf16 a[212:215], a[208:211], v[36:43], v24\n\t"
    "s_waitcnt vmcnt(25)\n\t"
    "s_barrier\n\t"
    "v_mfma_f32_16x16x32_bf16 v[64:67], a[80:83], a[32:35], v[52:55]\n\t"
    "ds_read_b128 a[64:67], v34 offset:0\n\t"
    "ds_read_b64_tr_b16 a[192:193], v18 offset:32768\n\t"
    "v_cvt_pk_bf16_f32 v36, v80, v81\n\t"
    "v_mfma_f32_16x16x32_bf16 v[64:67], a[84:87], a[36:39], v[64:67]\n\t"
    "ds_read_b128 a[68:71], v34 offset:1024\n\t"
    "ds_read_b64_tr_b16 a[194:195], v18 offset:36864\n\t"
    "v_cvt_pk_bf16_f32 v37, v82, v83\n\t"
    "v_mfma_f32_16x16x32_bf16 v[64:67], a[88:91], a[40:43], v[64:67]\n\t"
    "ds_read_b128 a[72:75], v34 offset:2048\n\t"
    "ds_read_b64_tr_b16 a[196:197], v18 offset:40960\n\t"
    "v_cvt_pk_bf16_f32 v38, v84, v85\n\t"
    "v_mfma_f32_16x16x32_bf16 v[64:67], a[92:95], a[44:47], v[64:67]\n\t"
    "ds_read_b128 a[76:79], v34 offset:3072\n\t"
    "ds_read_b64_tr_b16 a[198:199], v18 offset:45056\n\t"
    "v_cvt_pk_bf16_f32 v39, v86, v87\n\t"
    "v_mfma_f32_16x16x32_bf16 v[68:71], a[96:99], a[32:35], v[52:55]\n\t"
    "ds_read_b128 v[60:63], v34 offset:4096\n\t"
    "ds_read_b64_tr_b16 a[200:201], v19 offset:32768\n\t"
    "v_cvt_pk_bf16_f32 v40, v88, v89\n\t"
    "v_mfma_f32_16x16x32_bf16 v[68:71], a[100:103], a[36:39], v[68:71]\n\t"
    "ds_read_b128 v[224:227], v34 offset:5120\n\t"
    "ds_read_b64_tr_b16 a[202:203], v19 offset:36864\n\t"
    "v_cvt_pk_bf16_f32 v41, v90, v91\n\t"
    "v_mfma_f32_16x16x32_bf16 v[68:71], a[104:107], a[40:43], v[68:71]\n\t"
    "ds_read_b128 v[228:231], v34 offset:6144\n\t"
    "ds_read_b64_tr_b16 a[204:205], v19 offset:40960\n\t"
    "v_cvt_pk_bf16_f32 v42, v92, v93\n\t"
    "v_mfma_f32_16x16x32_bf16 v[68:71], a[108:111], a[44:47], v[68:71]\n\t"
    "ds_read_b128 v[232:235], v34 offset:7168\n\t"
    "ds_read_b64_tr_b16 a[206:207], v19 offset:45056\n\t"
    "v_cvt_pk_bf16_f32 v43, v94, v95\n\t"
    "v_mfma_f32_16x16x32_bf16 v[72:75], a[112:115], a[32:35], v[52:55]\n\t"
    "ds_read_b128 v[236:239], v34 offset:8192\n\t"
    "v_exp_f32 v64, v64\n\t"
    "v_mfma_f32_16x16x32_bf16 v[72:75], a[116:119], a[36:39], v[72:75]\n\t"
    "ds_read_b128 v[252:255], v34 offset:9216\n\t"
    "v_exp_f32 v65, v65\n\t"
    "v_mfma_f32_16x16x32_bf16 v[72:75], a[120:123], a[40:43], v[72:75]\n\t"
    "ds_read_b128 a[228:231], v34 offset:10240\n\t"
    "v_exp_f32 v66, v66\n\t"
    "v_mfma_f32_16x16x32_bf16 v[72:75], a[124:127], a[44:47], v[72:75]\n\t"
    "ds_read_b128 v[240:243], v34 offset:11264\n\t"
    "v_exp_f32 v67, v67\n\t"
    "v_mfma_f32_16x16x32_bf16 v[76:79], a[128:131], a[32:35], v[52:55]\n\t"
    "ds_read_b128 a[232:235], v34 offset:12288\n\t"
    "v_exp_f32 v68, v68\n\t"
    "v_mfma_f32_16x16x32_bf16 v[76:79], a[132:135], a[36:39], v[76:79]\n\t"
    "ds_read_b128 a[236:239], v34 offset:13312\n\t"
    "v_exp_f32 v69, v69\n\t"
    "v_mfma_f32_16x16x32_bf16 v[76:79], a[136:139], a[40:43], v[76:79]\n\t"
    "ds_read_b128 v[32:35], v34 offset:14336\n\t"
    "v_exp_f32 v70, v70\n\t"
    "v_mfma_f32_16x16x32_bf16 v[76:79], a[140:143], a[44:47], v[76:79]\n\t"
    "v_exp_f32 v71, v71\n\t"
    "v_mfma_f32_16x16x32_bf16 v[128:131], v[36:39], a[144:147], v[128:131]\n\t"
    "v_exp_f32 v72, v72\n\t"
    "v_mfma_f32_16x16x32_bf16 v[128:131], v[40:43], a[148:151], v[128:131]\n\t"
    "v_exp_f32 v73, v73\n\t"
    "v_mfma_f32_16x16x32_bf16 v[132:135], v[36:39], a[152:155], v[132:135]\n\t"
    "v_exp_f32 v74, v74\n\t"
    "v_mfma_f32_16x16x32_bf16 v[132:135], v[40:43], a[156:159], v[132:135]\n\t"
    "v_exp_f32 v75, v75\n\t"
    "v_mfma_f32_16x16x32_bf16 v[136:139], v[36:39], a[160:163], v[136:139]\n\t"
    "v_exp_f32 v76, v76\n\t"
    "v_mfma_f32_16x16x32_bf16 v[136:139], v[40:43], a[164:167], v[136:139]\n\t"
    "v_exp_f32 v77, v77\n\t"
    "v_mfma_f32_16x16x32_bf16 v[140:143], v[36:39], a[168:171], v[140:143]\n\t"
    "v_exp_f32 v78, v78\n\t"
    "v_mfma_f32_16x16x32_bf16 v[140:143], v[40:43], a[172:175], v[140:143]\n\t"
    "v_exp_f32 v79, v79\n\t"
    "v_mfma_f32_16x16x32_bf16 v[144:147], v[36:39], a[176:179], v[144:147]\n\t"
    "buffer_load_dwordx4 v22, s[44:47], s74 offen offset:1024 nt lds\n\t"
    "v_mfma_f32_16x16x32_bf16 v[144:147], v[40:43], a[180:183], v[144:147]\n\t"
    "buffer_load_dwordx4 v20, s[44:47], s74 offen offset:2048 nt lds\n\t"
    "v_mfma_f32_16x16x32_bf16 v[148:151], v[36:39], a[184:187], v[148:151]\n\t"
    "buffer_load_dwordx4 v22, s[44:47], s74 offen offset:3072 nt lds\n\t"
    "s_add_u32 m0, s60, 49152\n\t"
    "v_mfma_f32_16x16x32_bf16 v[148:151], v[40:43], a[188:191], v[148:151]\n\t"
    "buffer_load_dwordx4 v20, s[44:47], s58 offen offset:0 nt lds\n\t"
    "v_mfma_f32_16x16x32_bf16 v[152:155], v[36:39], a[240:243], v[152:155]\n\t"
    "buffer_load_dwordx4 v22, s[44:47], s58 offen offset:1024 nt lds\n\t"
    "v_mfma_f32_16x16x32_bf16 v[152:155], v[40:43], a[244:247], v[152:155]\n\t"
    "buffer_load_dwordx4 v20, s[44:47], s58 offen offset:2048 nt lds\n\t"
    "v_mfma_f32_16x16x32_bf16 v[156:159], v[36:39], a[248:251], v[156:159]\n\t"
    "buffer_load_dwordx4 v22, s[44:47], s58 offen offset:3072 nt lds\n\t"
    "v_mfma_f32_16x16x32_bf16 v[156:159], v[40:43], a[252:255], v[156:159]\n\t"
    "s_add_u32 s58, s58, 16384\n\t"
    "s_and_b32 s58, s58, 0x7ffff\n\t"
    "v_smfmac_f32_16x16x64_bf16 a[216:219], a[208:211], v[36:43], v24\n\t"
    "v_mfma_f32_16x16x32_bf16 v[80:83], a[80:83], a[48:51], v[56:59]\n\t"
    "ds_read_b128 a[80:83], v8 offset:32768\n\t"
    "v_cvt_pk_bf16_f32 v36, v64, v65\n\t"
    "v_mfma_f32_16x16x32_bf16 v[80:83], a[84:87], a[52:55], v[80:83]\n\t"
    "ds_read_b128 a[84:87], v9 offset:32768\n\t"
    "v_cvt_pk_bf16_f32 v37, v66, v67\n\t"
    "v_mfma_f32_16x16x32_bf16 v[80:83], a[88:91], a[56:59], v[80:83]\n\t"
    "ds_read_b128 a[88:91], v10 offset:32768\n\t"
    "v_cvt_pk_bf16_f32 v38, v68, v69\n\t"
    "v_mfma_f32_16x16x32_bf16 v[80:83], a[92:95], a[60:63], v[80:83]\n\t"
    "ds_read_b128 a[92:95], v11 offset:32768\n\t"
    "v_cvt_pk_bf16_f32 v39, v70, v71\n\t"
    "v_mfma_f32_16x16x32_bf16 v[84:87], a[96:99], a[48:51], v[56:59]\n\t"
    "ds_read_b128 a[96:99], v8 offset:36864\n\t"
    "v_cvt_pk_bf16_f32 v40, v72, v73\n\t"
    "v_mfma_f32_16x16x32_bf16 v[84:87], a[100:103], a[52:55], v[84:87]\n\t"
    "ds_read_b128 a[100:103], v9 offset:36864\n\t"
    "v_cvt_pk_bf16_f32 v41, v74, v75\n\t"
    "v_mfma_f32_16x16x32_bf16 v[84:87], a[104:107], a[56:59], v[84:87]\n\t"
    "ds_read_b128 a[104:107], v10 offset:36864\n\t"
    "v_cvt_pk_bf16_f32 v42, v76, v77\n\t"
    "v_mfma_f32_16x16x32_bf16 v[84:87], a[108:111], a[60:63], v[84:87]\n\t"
    "ds_read_b128 a[108:111], v11 offset:36864\n\t"
    "v_cvt_pk_bf16_f32 v43, v78, v79\n\t"
    "v_mfma_f32_16x16x32_bf16 v[88:91], a[112:115], a[48:51], v[56:59]\n\t"
    "v_exp_f32 v80, v80\n\t"
    "v_mfma_f32_16x16x32_bf16 v[88:91], a[116:119], a[52:55], v[88:91]\n\t"
    "v_exp_f32 v81, v81\n\t"
    "v_mfma_f32_16x16x32_bf16 v[88:91], a[120:123], a[56:59], v[88:91]\n\t"
    "v_exp_f32 v82, v82\n\t"
    "v_mfma_f32_16x16x32_bf16 v[88:91], a[124:127], a[60:63], v[88:91]\n\t"
    "v_exp_f32 v83, v83\n\t"
    "v_mfma_f32_16x16x32_bf16 v[92:95], a[128:131], a[48:51], v[56:59]\n\t"
    "v_exp_f32 v84, v84\n\t"
    "v_mfma_f32_16x16x32_bf16 v[92:95], a[132:135], a[52:55], v[92:95]\n\t"
    "v_exp_f32 v85, v85\n\t"
    "v_mfma_f32_16x16x32_bf16 v[92:95], a[136:139], a[56:59], v[92:95]\n\t"
    "v_exp_f32 v86, v86\n\t"
    "v_mfma_f32_16x16x32_bf16 v[92:95], a[140:143], a[60:63], v[92:95]\n\t"
    "s_waitcnt lgkmcnt(0)\n\t"
    "v_exp_f32 v87, v87\n\t"
    "v_mfma_f32_16x16x32_bf16 v[64:67], a[80:83], a[64:67], v[60:63]\n\t"
    "v_exp_f32 v88, v88\n\t"
    "v_mfma_f32_16x16x32_bf16 v[64:67], a[84:87], a[68:71], v[64:67]\n\t"
    "v_exp_f32 v89, v89\n\t"
    "v_mfma_f32_16x16x32_bf16 v[64:67], a[88:91], a[72:75], v[64:67]\n\t"
    "v_exp_f32 v90, v90\n\t"
    "v_mfma_f32_16x16x32_bf16 v[64:67], a[92:95], a[76:79], v[64:67]\n\t"
    "v_exp_f32 v91, v91\n\t"
    "v_mfma_f32_16x16x32_bf16 v[68:71], a[80:83], v[224:227], v[252:255]\n\t"
    "v_exp_f32 v92, v92\n\t"
    "v_mfma_f32_16x16x32_bf16 v[68:71], a[84:87], v[228:231], v[68:71]\n\t"
    "v_exp_f32 v93, v93\n\t"
    "v_mfma_f32_16x16x32_bf16 v[68:71], a[88:91], v[232:235], v[68:71]\n\t"
    "v_exp_f32 v94, v94\n\t"
    "v_mfma_f32_16x16x32_bf16 v[68:71], a[92:95], v[236:239], v[68:71]\n\t"
    "v_exp_f32 v95, v95\n\t"
    "v_mfma_f32_16x16x32_bf16 v[72:75], a[80:83], a[228:231], v[32:35]\n\t"
    "v_exp_f32 v64, v64\n\t"
    "v_mfma_f32_16x16x32_bf16 v[72:75], a[84:87], v[240:243], v[72:75]\n\t"
    "v_exp_f32 v65, v65\n\t"
    "v_mfma_f32_16x16x32_bf16 v[72:75], a[88:91], a[232:235], v[72:75]\n\t"
    "v_exp_f32 v66, v66\n\t"
    "v_mfma_f32_16x16x32_bf16 v[72:75], a[92:95], a[236:239], v[72:75]\n\t"
    "v_exp_f32 v67, v67\n\t"
    "v_mfma_f32_16x16x32_bf16 v[160:163], v[36:39], a[144:147], v[160:163]\n\t"
    "v_exp_f32 v68, v68\n\t"
    "v_mfma_f32_16x16x32_bf16 v[160:163], v[40:43], a[148:151], v[160:163]\n\t"
    "v_exp_f32 v69, v69\n\t"
    "v_mfma_f32_16x16x32_bf16 v[164:167], v[36:39], a[152:155], v[164:167]\n\t"
    "v_exp_f32 v70, v70\n\t"
    "v_mfma_f32_16x16x32_bf16 v[164:167], v[40:43], a[156:159], v[164:167]\n\t"
    "v_exp_f32 v71, v71\n\t"
    "v_mfma_f32_16x16x32_bf16 v[168:171], v[36:39], a[160:163], v[168:171]\n\t"
    "ds_read_b32 v245, v246\n\t"
    "v_exp_f32 v72, v72\n\t"
    "v_mfma_f32_16x16x32_bf16 v[168:171], v[40:43], a[164:167], v[168:171]\n\t"
    "v_exp_f32 v73, v73\n\t"
    "v_mfma_f32_16x16x32_bf16 v[172:175], v[36:39], a[168:171], v[172:175]\n\t"
    "v_exp_f32 v74, v74\n\t"
    "v_mfma_f32_16x16x32_bf16 v[172:175], v[40:43], a[172:175], v[172:175]\n\t"
    "v_exp_f32 v75, v75\n\t"
    "v_mfma_f32_16x16x32_bf16 v[176:179], v[36:39], a[176:179], v[176:179]\n\t"
    "ds_read_b128 a[112:115], v8 offset:40960\n\t"
    "v_cvt_pk_bf16_f32 v64, v64, v65\n\t"
    "v_mfma_f32_16x16x32_bf16 v[176:179], v[40:43], a[180:183], v[176:179]\n\t"
    "ds_read_b128 a[116:119], v9 offset:40960\n\t"
    "v_cvt_pk_bf16_f32 v65, v66, v67\n\t"
    "s_waitcnt lgkmcnt(2)\n\t"
    "v_cmp_gt_u32 vcc, v244, v245\n\t"
    "v_mfma_f32_16x16x32_bf16 v[180:183], v[36:39], a[184:187], v[180:183]\n\t"
    "ds_read_b128 a[120:123], v10 offset:40960\n\t"
    "v_cvt_pk_bf16_f32 v68, v68, v69\n\t"
    "v_mfma_f32_16x16x32_bf16 v[180:183], v[40:43], a[188:191], v[180:183]\n\t"
    "ds_read_b128 a[124:127], v11 offset:40960\n\t"
    "v_cvt_pk_bf16_f32 v69, v70, v71\n\t"
    "v_mfma_f32_16x16x32_bf16 v[184:187], v[36:39], a[240:243], v[184:187]\n\t"
    "s_cbranch_vccnz SPIN4P1%=\n\t"
    "RET4P1%=:\n\t"
    "ds_read_b128 a[128:131], v8 offset:45056\n\t"
    "v_cvt_pk_bf16_f32 v72, v72, v73\n\t"
    "v_mfma_f32_16x16x32_bf16 v[184:187], v[40:43], a[244:247], v[184:187]\n\t"
    "ds_read_b128 a[132:135], v9 offset:45056\n\t"
    "v_cvt_pk_bf16_f32 v73, v74, v75\n\t"
    "v_mfma_f32_16x16x32_bf16 v[188:191], v[36:39], a[248:251], v[188:191]\n\t"
    "ds_read_b128 a[136:139], v10 offset:45056\n\t"
    "ds_write_b64 v27, v[64:65] offset:0\n\t"
    "v_mfma_f32_16x16x32_bf16 v[188:191], v[40:43], a[252:255], v[188:191]\n\t"
    "ds_read_b128 a[140:143], v11 offset:45056\n\t"
    "ds_write_b64 v27, v[68:69] offset:1024\n\t"
    "v_smfmac_f32_16x16x64_bf16 a[220:223], a[208:211], v[36:43], v24\n\t"
    "ds_write_b64 v27, v[72:73] offset:2048\n\t"
    "s_mov_b32 s57, 7\n\t"
    "LOOPB%=:\n\t"
    "; TILE 2\n\t"
    "v_mfma_f32_16x16x32_bf16 v[64:67], a[80:83], a[0:3], v[44:47]\n\t"
    "s_sub_u32 s72, s58, 12288\n\t"
    "s_sub_u32 s73, s58, 8192\n\t"
    "s_sub_u32 s74, s58, 4096\n\t"
    "s_add_u32 m0, s59, 4294955008\n\t"
    "v_cvt_pk_bf16_f32 v36, v80, v81\n\t"
    "v_mfma_f32_16x16x32_bf16 v[64:67], a[84:87], a[4:7], v[64:67]\n\t"
    "buffer_load_dwordx4 v20, s[40:43], s72 offen offset:0 nt lds\n\t"
    "v_cvt_pk_bf16_f32 v37, v82, v83\n\t"
    "v_mfma_f32_16x16x32_bf16 v[64:67], a[88:91], a[8:11], v[64:67]\n\t"
    "buffer_load_dwordx4 v21, s[40:43], s72 offen offset:1024 nt lds\n\t"
    "v_cvt_pk_bf16_f32 v38, v84, v85\n\t"
    "v_mfma_f32_16x16x32_bf16 v[64:67], a[92:95], a[12:15], v[64:67]\n\t"
    "buffer_load_dwordx4 v22, s[40:43], s72 offen offset:2048 nt lds\n\t"
    "v_cvt_pk_bf16_f32 v39, v86, v87\n\t"
    "v_mfma_f32_16x16x32_bf16 v[68:71], a[96:99], a[0:3], v[44:47]\n\t"
    "buffer_load_dwordx4 v23, s[40:43], s72 offen offset:3072 nt lds\n\t"
    "s_add_u32 m0, s59, 4294959104\n\t"
    "v_cvt_pk_bf16_f32 v40, v88, v89\n\t"
    "v_mfma_f32_16x16x32_bf16 v[68:71], a[100:103], a[4:7], v[68:71]\n\t"
    "buffer_load_dwordx4 v20, s[40:43], s73 offen offset:0 nt lds\n\t"
    "v_cvt_pk_bf16_f32 v41, v90, v91\n\t"
    "v_mfma_f32_16x16x32_bf16 v[68:71], a[104:107], a[8:11], v[68:71]\n\t"
    "buffer_load_dwordx4 v21, s[40:43], s73 offen offset:1024 nt lds\n\t"
    "v_cvt_pk_bf16_f32 v42, v92, v93\n\t"
    "v_mfma_f32_16x16x32_bf16 v[68:71], a[108:111], a[12:15], v[68:71]\n\t"
    "s_waitcnt lgkmcnt(0)\n\t"
    "buffer_load_dwordx4 v22, s[40:43], s73 offen offset:2048 nt lds\n\t"
    "v_cvt_pk_bf16_f32 v43, v94, v95\n\t"
    "v_mfma_f32_16x16x32_bf16 v[72:75], a[112:115], a[0:3], v[44:47]\n\t"
    "buffer_load_dwordx4 v23, s[40:43], s73 offen offset:3072 nt lds\n\t"
    "v_exp_f32 v64, v64\n\t"
    "v_mfma_f32_16x16x32_bf16 v[72:75], a[116:119], a[4:7], v[72:75]\n\t"
    "v_exp_f32 v65, v65\n\t"
    "v_mfma_f32_16x16x32_bf16 v[72:75], a[120:123], a[8:11], v[72:75]\n\t"
    "v_exp_f32 v66, v66\n\t"
    "v_mfma_f32_16x16x32_bf16 v[72:75], a[124:127], a[12:15], v[72:75]\n\t"
    "v_exp_f32 v67, v67\n\t"
    "v_mfma_f32_16x16x32_bf16 v[76:79], a[128:131], a[0:3], v[44:47]\n\t"
    "v_exp_f32 v68, v68\n\t"
    "v_mfma_f32_16x16x32_bf16 v[76:79], a[132:135], a[4:7], v[76:79]\n\t"
    "v_exp_f32 v69, v69\n\t"
    "v_mfma_f32_16x16x32_bf16 v[76:79], a[136:139], a[8:11], v[76:79]\n\t"
    "v_exp_f32 v70, v70\n\t"
    "v_mfma_f32_16x16x32_bf16 v[76:79], a[140:143], a[12:15], v[76:79]\n\t"
    "v_exp_f32 v71, v71\n\t"
    "v_mfma_f32_16x16x32_bf16 v[192:195], v[36:39], a[144:147], v[192:195]\n\t"
    "v_exp_f32 v72, v72\n\t"
    "v_mfma_f32_16x16x32_bf16 v[192:195], v[40:43], a[148:151], v[192:195]\n\t"
    "v_exp_f32 v73, v73\n\t"
    "v_mfma_f32_16x16x32_bf16 v[196:199], v[36:39], a[152:155], v[196:199]\n\t"
    "v_exp_f32 v74, v74\n\t"
    "v_mfma_f32_16x16x32_bf16 v[196:199], v[40:43], a[156:159], v[196:199]\n\t"
    "v_exp_f32 v75, v75\n\t"
    "v_mfma_f32_16x16x32_bf16 v[200:203], v[36:39], a[160:163], v[200:203]\n\t"
    "v_exp_f32 v76, v76\n\t"
    "v_mfma_f32_16x16x32_bf16 v[200:203], v[40:43], a[164:167], v[200:203]\n\t"
    "v_exp_f32 v77, v77\n\t"
    "v_mfma_f32_16x16x32_bf16 v[204:207], v[36:39], a[168:171], v[204:207]\n\t"
    "v_exp_f32 v78, v78\n\t"
    "v_mfma_f32_16x16x32_bf16 v[204:207], v[40:43], a[172:175], v[204:207]\n\t"
    "s_add_u32 m0, s59, 4294963200\n\t"
    "v_exp_f32 v79, v79\n\t"
    "v_mfma_f32_16x16x32_bf16 v[208:211], v[36:39], a[176:179], v[208:211]\n\t"
    "ds_read_b64_tr_b16 a[144:145], v12 offset:32768\n\t"
    "ds_read_b64_tr_b16 a[146:147], v12 offset:36864\n\t"
    "buffer_load_dwordx4 v20, s[40:43], s74 offen offset:0 nt lds\n\t"
    "v_mfma_f32_16x16x32_bf16 v[208:211], v[40:43], a[180:183], v[208:211]\n\t"
    "ds_read_b64_tr_b16 a[148:149], v12 offset:40960\n\t"
    "ds_read_b64_tr_b16 a[150:151], v12 offset:45056\n\t"
    "v_mfma_f32_16x16x32_bf16 v[212:215], v[36:39], a[184:187], v[212:215]\n\t"
    "ds_read_b64_tr_b16 a[152:153], v13 offset:32768\n\t"
    "ds_read_b64_tr_b16 a[154:155], v13 offset:36864\n\t"
    "buffer_load_dwordx4 v21, s[40:43], s74 offen offset:1024 nt lds\n\t"
    "v_mfma_f32_16x16x32_bf16 v[212:215], v[40:43], a[188:191], v[212:215]\n\t"
    "ds_read_b64_tr_b16 a[156:157], v13 offset:40960\n\t"
    "ds_read_b64_tr_b16 a[158:159], v13 offset:45056\n\t"
    "v_mfma_f32_16x16x32_bf16 v[216:219], v[36:39], a[240:243], v[216:219]\n\t"
    "ds_read_b64_tr_b16 a[160:161], v14 offset:32768\n\t"
    "ds_read_b64_tr_b16 a[162:163], v14 offset:36864\n\t"
    "buffer_load_dwordx4 v22, s[40:43], s74 offen offset:2048 nt lds\n\t"
    "v_mfma_f32_16x16x32_bf16 v[216:219], v[40:43], a[244:247], v[216:219]\n\t"
    "ds_read_b64_tr_b16 a[164:165], v14 offset:40960\n\t"
    "ds_read_b64_tr_b16 a[166:167], v14 offset:45056\n\t"
    "v_mfma_f32_16x16x32_bf16 v[220:223], v[36:39], a[248:251], v[220:223]\n\t"
    "ds_read_b64_tr_b16 a[168:169], v15 offset:32768\n\t"
    "ds_read_b64_tr_b16 a[170:171], v15 offset:36864\n\t"
    "buffer_load_dwordx4 v23, s[40:43], s74 offen offset:3072 nt lds\n\t"
    "v_mfma_f32_16x16x32_bf16 v[220:223], v[40:43], a[252:255], v[220:223]\n\t"
    "s_add_u32 m0, s59, 0\n\t"
    "ds_read_b64_tr_b16 a[172:173], v15 offset:40960\n\t"
    "ds_read_b64_tr_b16 a[174:175], v15 offset:45056\n\t"
    "v_smfmac_f32_16x16x64_bf16 a[224:227], a[208:211], v[36:43], v24\n\t"
    "buffer_load_dwordx4 v20, s[40:43], s58 offen offset:0 nt lds\n\t"
    "v_mfma_f32_16x16x32_bf16 v[80:83], a[80:83], a[16:19], v[48:51]\n\t"
    "ds_read_b64_tr_b16 a[176:177], v16 offset:32768\n\t"
    "v_cvt_pk_bf16_f32 v36, v64, v65\n\t"
    "v_mfma_f32_16x16x32_bf16 v[80:83], a[84:87], a[20:23], v[80:83]\n\t"
    "ds_read_b64_tr_b16 a[178:179], v16 offset:36864\n\t"
    "buffer_load_dwordx4 v21, s[40:43], s58 offen offset:1024 nt lds\n\t"
    "v_cvt_pk_bf16_f32 v37, v66, v67\n\t"
    "v_mfma_f32_16x16x32_bf16 v[80:83], a[88:91], a[24:27], v[80:83]\n\t"
    "ds_read_b64_tr_b16 a[180:181], v16 offset:40960\n\t"
    "v_cvt_pk_bf16_f32 v38, v68, v69\n\t"
    "v_mfma_f32_16x16x32_bf16 v[80:83], a[92:95], a[28:31], v[80:83]\n\t"
    "ds_read_b64_tr_b16 a[182:183], v16 offset:45056\n\t"
    "buffer_load_dwordx4 v22, s[40:43], s58 offen offset:2048 nt lds\n\t"
    "v_cvt_pk_bf16_f32 v39, v70, v71\n\t"
    "v_mfma_f32_16x16x32_bf16 v[84:87], a[96:99], a[16:19], v[48:51]\n\t"
    "ds_read_b64_tr_b16 a[184:185], v17 offset:32768\n\t"
    "v_cvt_pk_bf16_f32 v40, v72, v73\n\t"
    "v_mfma_f32_16x16x32_bf16 v[84:87], a[100:103], a[20:23], v[84:87]\n\t"
    "ds_read_b64_tr_b16 a[186:187], v17 offset:36864\n\t"
    "buffer_load_dwordx4 v23, s[40:43], s58 offen offset:3072 nt lds\n\t"
    "v_cvt_pk_bf16_f32 v41, v74, v75\n\t"
    "v_mfma_f32_16x16x32_bf16 v[84:87], a[104:107], a[24:27], v[84:87]\n\t"
    "s_add_u32 m0, s60, 4294955008\n\t"
    "ds_read_b64_tr_b16 a[188:189], v17 offset:40960\n\t"
    "v_cvt_pk_bf16_f32 v42, v76, v77\n\t"
    "v_add_u32 v244, 1, v244\n\t"
    "v_mfma_f32_16x16x32_bf16 v[84:87], a[108:111], a[28:31], v[84:87]\n\t"
    "s_waitcnt vmcnt(16)\n\t"
    "ds_write_b32 v27, v244 offset:3072\n\t"
    "ds_read_b64_tr_b16 a[190:191], v17 offset:45056\n\t"
    "buffer_load_dwordx4 v20, s[44:47], s72 offen offset:0 nt lds\n\t"
    "v_cvt_pk_bf16_f32 v43, v78, v79\n\t"
    "v_mfma_f32_16x16x32_bf16 v[88:91], a[112:115], a[16:19], v[48:51]\n\t"
    "ds_write_b32 v27, v244 offset:4096\n\t"
    "v_exp_f32 v80, v80\n\t"
    "v_mfma_f32_16x16x32_bf16 v[88:91], a[116:119], a[20:23], v[88:91]\n\t"
    "ds_write_b32 v27, v244 offset:5120\n\t"
    "v_exp_f32 v81, v81\n\t"
    "v_mfma_f32_16x16x32_bf16 v[88:91], a[120:123], a[24:27], v[88:91]\n\t"
    "v_exp_f32 v82, v82\n\t"
    "v_mfma_f32_16x16x32_bf16 v[88:91], a[124:127], a[28:31], v[88:91]\n\t"
    "v_exp_f32 v83, v83\n\t"
    "v_mfma_f32_16x16x32_bf16 v[92:95], a[128:131], a[16:19], v[48:51]\n\t"
    "v_exp_f32 v84, v84\n\t"
    "v_mfma_f32_16x16x32_bf16 v[92:95], a[132:135], a[20:23], v[92:95]\n\t"
    "v_exp_f32 v85, v85\n\t"
    "v_mfma_f32_16x16x32_bf16 v[92:95], a[136:139], a[24:27], v[92:95]\n\t"
    "v_exp_f32 v86, v86\n\t"
    "v_mfma_f32_16x16x32_bf16 v[92:95], a[140:143], a[28:31], v[92:95]\n\t"
    "s_waitcnt lgkmcnt(2)\n\t"
    "v_exp_f32 v87, v87\n\t"
    "v_mfma_f32_16x16x32_bf16 v[96:99], v[36:39], a[144:147], v[96:99]\n\t"
    "v_exp_f32 v88, v88\n\t"
    "v_mfma_f32_16x16x32_bf16 v[96:99], v[40:43], a[148:151], v[96:99]\n\t"
    "v_exp_f32 v89, v89\n\t"
    "v_mfma_f32_16x16x32_bf16 v[100:103], v[36:39], a[152:155], v[100:103]\n\t"
    "v_exp_f32 v90, v90\n\t"
    "v_mfma_f32_16x16x32_bf16 v[100:103], v[40:43], a[156:159], v[100:103]\n\t"
    "v_exp_f32 v91, v91\n\t"
    "v_mfma_f32_16x16x32_bf16 v[104:107], v[36:39], a[160:163], v[104:107]\n\t"
    "v_exp_f32 v92, v92\n\t"
    "v_mfma_f32_16x16x32_bf16 v[104:107], v[40:43], a[164:167], v[104:107]\n\t"
    "v_exp_f32 v93, v93\n\t"
    "v_mfma_f32_16x16x32_bf16 v[108:111], v[36:39], a[168:171], v[108:111]\n\t"
    "v_exp_f32 v94, v94\n\t"
    "v_mfma_f32_16x16x32_bf16 v[108:111], v[40:43], a[172:175], v[108:111]\n\t"
    "v_exp_f32 v95, v95\n\t"
    "v_mfma_f32_16x16x32_bf16 v[112:115], v[36:39], a[176:179], v[112:115]\n\t"
    "buffer_load_dwordx4 v22, s[44:47], s72 offen offset:1024 nt lds\n\t"
    "v_mfma_f32_16x16x32_bf16 v[112:115], v[40:43], a[180:183], v[112:115]\n\t"
    "buffer_load_dwordx4 v20, s[44:47], s72 offen offset:2048 nt lds\n\t"
    "v_mfma_f32_16x16x32_bf16 v[116:119], v[36:39], a[184:187], v[116:119]\n\t"
    "buffer_load_dwordx4 v22, s[44:47], s72 offen offset:3072 nt lds\n\t"
    "s_add_u32 m0, s60, 4294959104\n\t"
    "v_mfma_f32_16x16x32_bf16 v[116:119], v[40:43], a[188:191], v[116:119]\n\t"
    "buffer_load_dwordx4 v20, s[44:47], s73 offen offset:0 nt lds\n\t"
    "v_mfma_f32_16x16x32_bf16 v[120:123], v[36:39], a[192:195], v[120:123]\n\t"
    "buffer_load_dwordx4 v22, s[44:47], s73 offen offset:1024 nt lds\n\t"
    "v_mfma_f32_16x16x32_bf16 v[120:123], v[40:43], a[196:199], v[120:123]\n\t"
    "buffer_load_dwordx4 v20, s[44:47], s73 offen offset:2048 nt lds\n\t"
    "v_mfma_f32_16x16x32_bf16 v[124:127], v[36:39], a[200:203], v[124:127]\n\t"
    "buffer_load_dwordx4 v22, s[44:47], s73 offen offset:3072 nt lds\n\t"
    "s_add_u32 m0, s60, 4294963200\n\t"
    "v_mfma_f32_16x16x32_bf16 v[124:127], v[40:43], a[204:207], v[124:127]\n\t"
    "buffer_load_dwordx4 v20, s[44:47], s74 offen offset:0 nt lds\n\t"
    "v_smfmac_f32_16x16x64_bf16 a[212:215], a[208:211], v[36:43], v24\n\t"
    "v_mfma_f32_16x16x32_bf16 v[64:67], a[80:83], a[32:35], v[52:55]\n\t"
    "ds_read_b64_tr_b16 a[240:241], v18 offset:49152\n\t"
    "v_cvt_pk_bf16_f32 v36, v80, v81\n\t"
    "v_mfma_f32_16x16x32_bf16 v[64:67], a[84:87], a[36:39], v[64:67]\n\t"
    "ds_read_b64_tr_b16 a[242:243], v18 offset:53248\n\t"
    "v_cvt_pk_bf16_f32 v37, v82, v83\n\t"
    "v_mfma_f32_16x16x32_bf16 v[64:67], a[88:91], a[40:43], v[64:67]\n\t"
    "ds_read_b64_tr_b16 a[244:245], v18 offset:57344\n\t"
    "v_cvt_pk_bf16_f32 v38, v84, v85\n\t"
    "v_mfma_f32_16x16x32_bf16 v[64:67], a[92:95], a[44:47], v[64:67]\n\t"
    "ds_read_b64_tr_b16 a[246:247], v18 offset:61440\n\t"
    "v_cvt_pk_bf16_f32 v39, v86, v87\n\t"
    "v_mfma_f32_16x16x32_bf16 v[68:71], a[96:99], a[32:35], v[52:55]\n\t"
    "ds_read_b64_tr_b16 a[248:249], v19 offset:49152\n\t"
    "v_cvt_pk_bf16_f32 v40, v88, v89\n\t"
    "v_mfma_f32_16x16x32_bf16 v[68:71], a[100:103], a[36:39], v[68:71]\n\t"
    "ds_read_b64_tr_b16 a[250:251], v19 offset:53248\n\t"
    "v_cvt_pk_bf16_f32 v41, v90, v91\n\t"
    "v_mfma_f32_16x16x32_bf16 v[68:71], a[104:107], a[40:43], v[68:71]\n\t"
    "ds_read_b64_tr_b16 a[252:253], v19 offset:57344\n\t"
    "v_cvt_pk_bf16_f32 v42, v92, v93\n\t"
    "v_mfma_f32_16x16x32_bf16 v[68:71], a[108:111], a[44:47], v[68:71]\n\t"
    "ds_read_b64_tr_b16 a[254:255], v19 offset:61440\n\t"
    "v_cvt_pk_bf16_f32 v43, v94, v95\n\t"
    "v_mfma_f32_16x16x32_bf16 v[72:75], a[112:115], a[32:35], v[52:55]\n\t"
    "v_exp_f32 v64, v64\n\t"
    "v_mfma_f32_16x16x32_bf16 v[72:75], a[116:119], a[36:39], v[72:75]\n\t"
    "v_exp_f32 v65, v65\n\t"
    "v_mfma_f32_16x16x32_bf16 v[72:75], a[120:123], a[40:43], v[72:75]\n\t"
    "v_exp_f32 v66, v66\n\t"
    "v_mfma_f32_16x16x32_bf16 v[72:75], a[124:127], a[44:47], v[72:75]\n\t"
    "v_exp_f32 v67, v67\n\t"
    "v_mfma_f32_16x16x32_bf16 v[76:79], a[128:131], a[32:35], v[52:55]\n\t"
    "v_exp_f32 v68, v68\n\t"
    "v_mfma_f32_16x16x32_bf16 v[76:79], a[132:135], a[36:39], v[76:79]\n\t"
    "v_exp_f32 v69, v69\n\t"
    "v_mfma_f32_16x16x32_bf16 v[76:79], a[136:139], a[40:43], v[76:79]\n\t"
    "v_exp_f32 v70, v70\n\t"
    "v_mfma_f32_16x16x32_bf16 v[76:79], a[140:143], a[44:47], v[76:79]\n\t"
    "v_exp_f32 v71, v71\n\t"
    "v_mfma_f32_16x16x32_bf16 v[128:131], v[36:39], a[144:147], v[128:131]\n\t"
    "v_exp_f32 v72, v72\n\t"
    "v_mfma_f32_16x16x32_bf16 v[128:131], v[40:43], a[148:151], v[128:131]\n\t"
    "v_exp_f32 v73, v73\n\t"
    "v_mfma_f32_16x16x32_bf16 v[132:135], v[36:39], a[152:155], v[132:135]\n\t"
    "v_exp_f32 v74, v74\n\t"
    "v_mfma_f32_16x16x32_bf16 v[132:135], v[40:43], a[156:159], v[132:135]\n\t"
    "v_exp_f32 v75, v75\n\t"
    "v_mfma_f32_16x16x32_bf16 v[136:139], v[36:39], a[160:163], v[136:139]\n\t"
    "v_exp_f32 v76, v76\n\t"
    "v_mfma_f32_16x16x32_bf16 v[136:139], v[40:43], a[164:167], v[136:139]\n\t"
    "v_exp_f32 v77, v77\n\t"
    "v_mfma_f32_16x16x32_bf16 v[140:143], v[36:39], a[168:171], v[140:143]\n\t"
    "v_exp_f32 v78, v78\n\t"
    "v_mfma_f32_16x16x32_bf16 v[140:143], v[40:43], a[172:175], v[140:143]\n\t"
    "v_exp_f32 v79, v79\n\t"
    "v_mfma_f32_16x16x32_bf16 v[144:147], v[36:39], a[176:179], v[144:147]\n\t"
    "buffer_load_dwordx4 v22, s[44:47], s74 offen offset:1024 nt lds\n\t"
    "v_mfma_f32_16x16x32_bf16 v[144:147], v[40:43], a[180:183], v[144:147]\n\t"
    "buffer_load_dwordx4 v20, s[44:47], s74 offen offset:2048 nt lds\n\t"
    "v_mfma_f32_16x16x32_bf16 v[148:151], v[36:39], a[184:187], v[148:151]\n\t"
    "buffer_load_dwordx4 v22, s[44:47], s74 offen offset:3072 nt lds\n\t"
    "s_add_u32 m0, s60, 0\n\t"
    "v_mfma_f32_16x16x32_bf16 v[148:151], v[40:43], a[188:191], v[148:151]\n\t"
    "buffer_load_dwordx4 v20, s[44:47], s58 offen offset:0 nt lds\n\t"
    "v_mfma_f32_16x16x32_bf16 v[152:155], v[36:39], a[192:195], v[152:155]\n\t"
    "buffer_load_dwordx4 v22, s[44:47], s58 offen offset:1024 nt lds\n\t"
    "v_mfma_f32_16x16x32_bf16 v[152:155], v[40:43], a[196:199], v[152:155]\n\t"
    "buffer_load_dwordx4 v20, s[44:47], s58 offen offset:2048 nt lds\n\t"
    "v_mfma_f32_16x16x32_bf16 v[156:159], v[36:39], a[200:203], v[156:159]\n\t"
    "buffer_load_dwordx4 v22, s[44:47], s58 offen offset:3072 nt lds\n\t"
    "v_mfma_f32_16x16x32_bf16 v[156:159], v[40:43], a[204:207], v[156:159]\n\t"
    "s_add_u32 s58, s58, 16384\n\t"
    "s_and_b32 s58, s58, 0x7ffff\n\t"
    "v_smfmac_f32_16x16x64_bf16 a[216:219], a[208:211], v[36:43], v24\n\t"
    "v_mfma_f32_16x16x32_bf16 v[80:83], a[80:83], a[48:51], v[56:59]\n\t"
    "ds_read_b128 a[80:83], v8 offset:49152\n\t"
    "v_cvt_pk_bf16_f32 v36, v64, v65\n\t"
    "v_mfma_f32_16x16x32_bf16 v[80:83], a[84:87], a[52:55], v[80:83]\n\t"
    "ds_read_b128 a[84:87], v9 offset:49152\n\t"
    "v_cvt_pk_bf16_f32 v37, v66, v67\n\t"
    "v_mfma_f32_16x16x32_bf16 v[80:83], a[88:91], a[56:59], v[80:83]\n\t"
    "ds_read_b128 a[88:91], v10 offset:49152\n\t"
    "v_cvt_pk_bf16_f32 v38, v68, v69\n\t"
    "v_mfma_f32_16x16x32_bf16 v[80:83], a[92:95], a[60:63], v[80:83]\n\t"
    "ds_read_b128 a[92:95], v11 offset:49152\n\t"
    "v_cvt_pk_bf16_f32 v39, v70, v71\n\t"
    "v_mfma_f32_16x16x32_bf16 v[84:87], a[96:99], a[48:51], v[56:59]\n\t"
    "ds_read_b128 a[96:99], v8 offset:53248\n\t"
    "v_cvt_pk_bf16_f32 v40, v72, v73\n\t"
    "v_mfma_f32_16x16x32_bf16 v[84:87], a[100:103], a[52:55], v[84:87]\n\t"
    "ds_read_b128 a[100:103], v9 offset:53248\n\t"
    "v_cvt_pk_bf16_f32 v41, v74, v75\n\t"
    "v_mfma_f32_16x16x32_bf16 v[84:87], a[104:107], a[56:59], v[84:87]\n\t"
    "ds_read_b128 a[104:107], v10 offset:53248\n\t"
    "v_cvt_pk_bf16_f32 v42, v76, v77\n\t"
    "v_mfma_f32_16x16x32_bf16 v[84:87], a[108:111], a[60:63], v[84:87]\n\t"
    "ds_read_b128 a[108:111], v11 offset:53248\n\t"
    "v_cvt_pk_bf16_f32 v43, v78, v79\n\t"
    "v_mfma_f32_16x16x32_bf16 v[88:91], a[112:115], a[48:51], v[56:59]\n\t"
    "v_exp_f32 v80, v80\n\t"
    "v_mfma_f32_16x16x32_bf16 v[88:91], a[116:119], a[52:55], v[88:91]\n\t"
    "v_exp_f32 v81, v81\n\t"
    "v_mfma_f32_16x16x32_bf16 v[88:91], a[120:123], a[56:59], v[88:91]\n\t"
    "v_exp_f32 v82, v82\n\t"
    "v_mfma_f32_16x16x32_bf16 v[88:91], a[124:127], a[60:63], v[88:91]\n\t"
    "v_exp_f32 v83, v83\n\t"
    "v_mfma_f32_16x16x32_bf16 v[92:95], a[128:131], a[48:51], v[56:59]\n\t"
    "v_exp_f32 v84, v84\n\t"
    "v_mfma_f32_16x16x32_bf16 v[92:95], a[132:135], a[52:55], v[92:95]\n\t"
    "v_exp_f32 v85, v85\n\t"
    "v_mfma_f32_16x16x32_bf16 v[92:95], a[136:139], a[56:59], v[92:95]\n\t"
    "v_exp_f32 v86, v86\n\t"
    "v_mfma_f32_16x16x32_bf16 v[92:95], a[140:143], a[60:63], v[92:95]\n\t"
    "s_waitcnt lgkmcnt(0)\n\t"
    "v_exp_f32 v87, v87\n\t"
    "v_mfma_f32_16x16x32_bf16 v[64:67], a[80:83], a[64:67], v[60:63]\n\t"
    "v_exp_f32 v88, v88\n\t"
    "v_mfma_f32_16x16x32_bf16 v[64:67], a[84:87], a[68:71], v[64:67]\n\t"
    "v_exp_f32 v89, v89\n\t"
    "v_mfma_f32_16x16x32_bf16 v[64:67], a[88:91], a[72:75], v[64:67]\n\t"
    "v_exp_f32 v90, v90\n\t"
    "v_mfma_f32_16x16x32_bf16 v[64:67], a[92:95], a[76:79], v[64:67]\n\t"
    "v_exp_f32 v91, v91\n\t"
    "v_mfma_f32_16x16x32_bf16 v[68:71], a[80:83], v[224:227], v[252:255]\n\t"
    "v_exp_f32 v92, v92\n\t"
    "v_mfma_f32_16x16x32_bf16 v[68:71], a[84:87], v[228:231], v[68:71]\n\t"
    "v_exp_f32 v93, v93\n\t"
    "v_mfma_f32_16x16x32_bf16 v[68:71], a[88:91], v[232:235], v[68:71]\n\t"
    "v_exp_f32 v94, v94\n\t"
    "v_mfma_f32_16x16x32_bf16 v[68:71], a[92:95], v[236:239], v[68:71]\n\t"
    "v_exp_f32 v95, v95\n\t"
    "v_mfma_f32_16x16x32_bf16 v[72:75], a[80:83], a[228:231], v[32:35]\n\t"
    "v_exp_f32 v64, v64\n\t"
    "v_mfma_f32_16x16x32_bf16 v[72:75], a[84:87], v[240:243], v[72:75]\n\t"
    "v_exp_f32 v65, v65\n\t"
    "v_mfma_f32_16x16x32_bf16 v[72:75], a[88:91], a[232:235], v[72:75]\n\t"
    "v_exp_f32 v66, v66\n\t"
    "v_mfma_f32_16x16x32_bf16 v[72:75], a[92:95], a[236:239], v[72:75]\n\t"
    "v_exp_f32 v67, v67\n\t"
    "v_mfma_f32_16x16x32_bf16 v[160:163], v[36:39], a[144:147], v[160:163]\n\t"
    "v_exp_f32 v68, v68\n\t"
    "v_mfma_f32_16x16x32_bf16 v[160:163], v[40:43], a[148:151], v[160:163]\n\t"
    "v_exp_f32 v69, v69\n\t"
    "v_mfma_f32_16x16x32_bf16 v[164:167], v[36:39], a[152:155], v[164:167]\n\t"
    "v_exp_f32 v70, v70\n\t"
    "v_mfma_f32_16x16x32_bf16 v[164:167], v[40:43], a[156:159], v[164:167]\n\t"
    "v_exp_f32 v71, v71\n\t"
    "v_mfma_f32_16x16x32_bf16 v[168:171], v[36:39], a[160:163], v[168:171]\n\t"
    "ds_read_b32 v245, v246\n\t"
    "v_exp_f32 v72, v72\n\t"
    "v_mfma_f32_16x16x32_bf16 v[168:171], v[40:43], a[164:167], v[168:171]\n\t"
    "v_exp_f32 v73, v73\n\t"
    "v_mfma_f32_16x16x32_bf16 v[172:175], v[36:39], a[168:171], v[172:175]\n\t"
    "v_exp_f32 v74, v74\n\t"
    "v_mfma_f32_16x16x32_bf16 v[172:175], v[40:43], a[172:175], v[172:175]\n\t"
    "v_exp_f32 v75, v75\n\t"
    "v_mfma_f32_16x16x32_bf16 v[176:179], v[36:39], a[176:179], v[176:179]\n\t"
    "ds_read_b128 a[112:115], v8 offset:57344\n\t"
    "v_cvt_pk_bf16_f32 v64, v64, v65\n\t"
    "v_mfma_f32_16x16x32_bf16 v[176:179], v[40:43], a[180:183], v[176:179]\n\t"
    "ds_read_b128 a[116:119], v9 offset:57344\n\t"
    "v_cvt_pk_bf16_f32 v65, v66, v67\n\t"
    "s_waitcnt lgkmcnt(2)\n\t"
    "v_cmp_gt_u32 vcc, v244, v245\n\t"
    "v_mfma_f32_16x16x32_bf16 v[180:183], v[36:39], a[184:187], v[180:183]\n\t"
    "ds_read_b128 a[120:123], v10 offset:57344\n\t"
    "v_cvt_pk_bf16_f32 v68, v68, v69\n\t"
    "v_mfma_f32_16x16x32_bf16 v[180:183], v[40:43], a[188:191], v[180:183]\n\t"
    "ds_read_b128 a[124:127], v11 offset:57344\n\t"
    "v_cvt_pk_bf16_f32 v69, v70, v71\n\t"
    "v_mfma_f32_16x16x32_bf16 v[184:187], v[36:39], a[192:195], v[184:187]\n\t"
    "s_cbranch_vccnz SPIN4L2%=\n\t"
    "RET4L2%=:\n\t"
    "ds_read_b128 a[128:131], v8 offset:61440\n\t"
    "v_cvt_pk_bf16_f32 v72, v72, v73\n\t"
    "v_mfma_f32_16x16x32_bf16 v[184:187], v[40:43], a[196:199], v[184:187]\n\t"
    "ds_read_b128 a[132:135], v9 offset:61440\n\t"
    "v_cvt_pk_bf16_f32 v73, v74, v75\n\t"
    "v_mfma_f32_16x16x32_bf16 v[188:191], v[36:39], a[200:203], v[188:191]\n\t"
    "ds_read_b128 a[136:139], v10 offset:61440\n\t"
    "ds_write_b64 v27, v[64:65] offset:512\n\t"
    "v_mfma_f32_16x16x32_bf16 v[188:191], v[40:43], a[204:207], v[188:191]\n\t"
    "ds_read_b128 a[140:143], v11 offset:61440\n\t"
    "ds_write_b64 v27, v[68:69] offset:1536\n\t"
    "v_smfmac_f32_16x16x64_bf16 a[220:223], a[208:211], v[36:43], v24\n\t"
    "ds_write_b64 v27, v[72:73] offset:2560\n\t"
    "; TILE 3\n\t"
    "v_mfma_f32_16x16x32_bf16 v[64:67], a[80:83], a[0:3], v[44:47]\n\t"
    "s_sub_u32 s72, s58, 12288\n\t"
    "s_sub_u32 s73, s58, 8192\n\t"
    "s_sub_u32 s74, s58, 4096\n\t"
    "s_add_u32 m0, s59, 4096\n\t"
    "v_cvt_pk_bf16_f32 v36, v80, v81\n\t"
    "v_mfma_f32_16x16x32_bf16 v[64:67], a[84:87], a[4:7], v[64:67]\n\t"
    "buffer_load_dwordx4 v20, s[40:43], s72 offen offset:0 nt lds\n\t"
    "v_cvt_pk_bf16_f32 v37, v82, v83\n\t"
    "v_mfma_f32_16x16x32_bf16 v[64:67], a[88:91], a[8:11], v[64:67]\n\t"
    "buffer_load_dwordx4 v21, s[40:43], s72 offen offset:1024 nt lds\n\t"
    "v_cvt_pk_bf16_f32 v38, v84, v85\n\t"
    "v_mfma_f32_16x16x32_bf16 v[64:67], a[92:95], a[12:15], v[64:67]\n\t"
    "buffer_load_dwordx4 v22, s[40:43], s72 offen offset:2048 nt lds\n\t"
    "v_cvt_pk_bf16_f32 v39, v86, v87\n\t"
    "v_mfma_f32_16x16x32_bf16 v[68:71], a[96:99], a[0:3], v[44:47]\n\t"
    "buffer_load_dwordx4 v23, s[40:43], s72 offen offset:3072 nt lds\n\t"
    "s_add_u32 m0, s59, 8192\n\t"
    "v_cvt_pk_bf16_f32 v40, v88, v89\n\t"
    "v_mfma_f32_16x16x32_bf16 v[68:71], a[100:103], a[4:7], v[68:71]\n\t"
    "buffer_load_dwordx4 v20, s[40:43], s73 offen offset:0 nt lds\n\t"
    "v_cvt_pk_bf16_f32 v41, v90, v91\n\t"
    "v_mfma_f32_16x16x32_bf16 v[68:71], a[104:107], a[8:11], v[68:71]\n\t"
    "buffer_load_dwordx4 v21, s[40:43], s73 offen offset:1024 nt lds\n\t"
    "v_cvt_pk_bf16_f32 v42, v92, v93\n\t"
    "v_mfma_f32_16x16x32_bf16 v[68:71], a[108:111], a[12:15], v[68:71]\n\t"
    "s_waitcnt lgkmcnt(0)\n\t"
    "buffer_load_dwordx4 v22, s[40:43], s73 offen offset:2048 nt lds\n\t"
    "v_cvt_pk_bf16_f32 v43, v94, v95\n\t"
    "v_mfma_f32_16x16x32_bf16 v[72:75], a[112:115], a[0:3], v[44:47]\n\t"
    "buffer_load_dwordx4 v23, s[40:43], s73 offen offset:3072 nt lds\n\t"
    "v_exp_f32 v64, v64\n\t"
    "v_mfma_f32_16x16x32_bf16 v[72:75], a[116:119], a[4:7], v[72:75]\n\t"
    "v_exp_f32 v65, v65\n\t"
    "v_mfma_f32_16x16x32_bf16 v[72:75], a[120:123], a[8:11], v[72:75]\n\t"
    "v_exp_f32 v66, v66\n\t"
    "v_mfma_f32_16x16x32_bf16 v[72:75], a[124:127], a[12:15], v[72:75]\n\t"
    "v_exp_f32 v67, v67\n\t"
    "v_mfma_f32_16x16x32_bf16 v[76:79], a[128:131], a[0:3], v[44:47]\n\t"
    "v_exp_f32 v68, v68\n\t"
    "v_mfma_f32_16x16x32_bf16 v[76:79], a[132:135], a[4:7], v[76:79]\n\t"
    "v_exp_f32 v69, v69\n\t"
    "v_mfma_f32_16x16x32_bf16 v[76:79], a[136:139], a[8:11], v[76:79]\n\t"
    "v_exp_f32 v70, v70\n\t"
    "v_mfma_f32_16x16x32_bf16 v[76:79], a[140:143], a[12:15], v[76:79]\n\t"
    "v_exp_f32 v71, v71\n\t"
    "v_mfma_f32_16x16x32_bf16 v[192:195], v[36:39], a[144:147], v[192:195]\n\t"
    "v_exp_f32 v72, v72\n\t"
    "v_mfma_f32_16x16x32_bf16 v[192:195], v[40:43], a[148:151], v[192:195]\n\t"
    "v_exp_f32 v73, v73\n\t"
    "v_mfma_f32_16x16x32_bf16 v[196:199], v[36:39], a[152:155], v[196:199]\n\t"
    "v_exp_f32 v74, v74\n\t"
    "v_mfma_f32_16x16x32_bf16 v[196:199], v[40:43], a[156:159], v[196:199]\n\t"
    "v_exp_f32 v75, v75\n\t"
    "v_mfma_f32_16x16x32_bf16 v[200:203], v[36:39], a[160:163], v[200:203]\n\t"
    "v_exp_f32 v76, v76\n\t"
    "v_mfma_f32_16x16x32_bf16 v[200:203], v[40:43], a[164:167], v[200:203]\n\t"
    "v_exp_f32 v77, v77\n\t"
    "v_mfma_f32_16x16x32_bf16 v[204:207], v[36:39], a[168:171], v[204:207]\n\t"
    "v_exp_f32 v78, v78\n\t"
    "v_mfma_f32_16x16x32_bf16 v[204:207], v[40:43], a[172:175], v[204:207]\n\t"
    "s_add_u32 m0, s59, 12288\n\t"
    "v_exp_f32 v79, v79\n\t"
    "v_mfma_f32_16x16x32_bf16 v[208:211], v[36:39], a[176:179], v[208:211]\n\t"
    "ds_read_b64_tr_b16 a[144:145], v12 offset:49152\n\t"
    "ds_read_b64_tr_b16 a[146:147], v12 offset:53248\n\t"
    "buffer_load_dwordx4 v20, s[40:43], s74 offen offset:0 nt lds\n\t"
    "v_mfma_f32_16x16x32_bf16 v[208:211], v[40:43], a[180:183], v[208:211]\n\t"
    "ds_read_b64_tr_b16 a[148:149], v12 offset:57344\n\t"
    "ds_read_b64_tr_b16 a[150:151], v12 offset:61440\n\t"
    "v_mfma_f32_16x16x32_bf16 v[212:215], v[36:39], a[184:187], v[212:215]\n\t"
    "ds_read_b64_tr_b16 a[152:153], v13 offset:49152\n\t"
    "ds_read_b64_tr_b16 a[154:155], v13 offset:53248\n\t"
    "buffer_load_dwordx4 v21, s[40:43], s74 offen offset:1024 nt lds\n\t"
    "v_mfma_f32_16x16x32_bf16 v[212:215], v[40:43], a[188:191], v[212:215]\n\t"
    "ds_read_b64_tr_b16 a[156:157], v13 offset:57344\n\t"
    "ds_read_b64_tr_b16 a[158:159], v13 offset:61440\n\t"
    "v_mfma_f32_16x16x32_bf16 v[216:219], v[36:39], a[192:195], v[216:219]\n\t"
    "ds_read_b64_tr_b16 a[160:161], v14 offset:49152\n\t"
    "ds_read_b64_tr_b16 a[162:163], v14 offset:53248\n\t"
    "buffer_load_dwordx4 v22, s[40:43], s74 offen offset:2048 nt lds\n\t"
    "v_mfma_f32_16x16x32_bf16 v[216:219], v[40:43], a[196:199], v[216:219]\n\t"
    "ds_read_b64_tr_b16 a[164:165], v14 offset:57344\n\t"
    "ds_read_b64_tr_b16 a[166:167], v14 offset:61440\n\t"
    "v_mfma_f32_16x16x32_bf16 v[220:223], v[36:39], a[200:203], v[220:223]\n\t"
    "ds_read_b64_tr_b16 a[168:169], v15 offset:49152\n\t"
    "ds_read_b64_tr_b16 a[170:171], v15 offset:53248\n\t"
    "buffer_load_dwordx4 v23, s[40:43], s74 offen offset:3072 nt lds\n\t"
    "v_mfma_f32_16x16x32_bf16 v[220:223], v[40:43], a[204:207], v[220:223]\n\t"
    "s_add_u32 m0, s59, 16384\n\t"
    "ds_read_b64_tr_b16 a[172:173], v15 offset:57344\n\t"
    "ds_read_b64_tr_b16 a[174:175], v15 offset:61440\n\t"
    "v_smfmac_f32_16x16x64_bf16 a[224:227], a[208:211], v[36:43], v24\n\t"
    "buffer_load_dwordx4 v20, s[40:43], s58 offen offset:0 nt lds\n\t"
    "v_mfma_f32_16x16x32_bf16 v[80:83], a[80:83], a[16:19], v[48:51]\n\t"
    "ds_read_b64_tr_b16 a[176:177], v16 offset:49152\n\t"
    "v_cvt_pk_bf16_f32 v36, v64, v65\n\t"
    "v_mfma_f32_16x16x32_bf16 v[80:83], a[84:87], a[20:23], v[80:83]\n\t"
    "ds_read_b64_tr_b16 a[178:179], v16 offset:53248\n\t"
    "buffer_load_dwordx4 v21, s[40:43], s58 offen offset:1024 nt lds\n\t"
    "v_cvt_pk_bf16_f32 v37, v66, v67\n\t"
    "v_mfma_f32_16x16x32_bf16 v[80:83], a[88:91], a[24:27], v[80:83]\n\t"
    "ds_read_b64_tr_b16 a[180:181], v16 offset:57344\n\t"
    "v_cvt_pk_bf16_f32 v38, v68, v69\n\t"
    "v_mfma_f32_16x16x32_bf16 v[80:83], a[92:95], a[28:31], v[80:83]\n\t"
    "ds_read_b64_tr_b16 a[182:183], v16 offset:61440\n\t"
    "buffer_load_dwordx4 v22, s[40:43], s58 offen offset:2048 nt lds\n\t"
    "v_cvt_pk_bf16_f32 v39, v70, v71\n\t"
    "v_mfma_f32_16x16x32_bf16 v[84:87], a[96:99], a[16:19], v[48:51]\n\t"
    "ds_read_b64_tr_b16 a[184:185], v17 offset:49152\n\t"
    "v_cvt_pk_bf16_f32 v40, v72, v73\n\t"
    "v_mfma_f32_16x16x32_bf16 v[84:87], a[100:103], a[20:23], v[84:87]\n\t"
    "ds_read_b64_tr_b16 a[186:187], v17 offset:53248\n\t"
    "buffer_load_dwordx4 v23, s[40:43], s58 offen offset:3072 nt lds\n\t"
    "v_cvt_pk_bf16_f32 v41, v74, v75\n\t"
    "v_mfma_f32_16x16x32_bf16 v[84:87], a[104:107], a[24:27], v[84:87]\n\t"
    "s_add_u32 m0, s60, 4096\n\t"
    "ds_read_b64_tr_b16 a[188:189], v17 offset:57344\n\t"
    "v_cvt_pk_bf16_f32 v42, v76, v77\n\t"
    "v_add_u32 v244, 1, v244\n\t"
    "v_mfma_f32_16x16x32_bf16 v[84:87], a[108:111], a[28:31], v[84:87]\n\t"
    "s_waitcnt vmcnt(16)\n\t"
    "ds_write_b32 v27, v244 offset:3072\n\t"
    "ds_read_b64_tr_b16 a[190:191], v17 offset:61440\n\t"
    "buffer_load_dwordx4 v20, s[44:47], s72 offen offset:0 nt lds\n\t"
    "v_cvt_pk_bf16_f32 v43, v78, v79\n\t"
    "v_mfma_f32_16x16x32_bf16 v[88:91], a[112:115], a[16:19], v[48:51]\n\t"
    "ds_write_b32 v27, v244 offset:4096\n\t"
    "v_exp_f32 v80, v80\n\t"
    "v_mfma_f32_16x16x32_bf16 v[88:91], a[116:119], a[20:23], v[88:91]\n\t"
    "ds_write_b32 v27, v244 offset:5120\n\t"
    "v_exp_f32 v81, v81\n\t"
    "v_mfma_f32_16x16x32_bf16 v[88:91], a[120:123], a[24:27], v[88:91]\n\t"
    "v_exp_f32 v82, v82\n\t"
    "v_mfma_f32_16x16x32_bf16 v[88:91], a[124:127], a[28:31], v[88:91]\n\t"
    "v_exp_f32 v83, v83\n\t"
    "v_mfma_f32_16x16x32_bf16 v[92:95], a[128:131], a[16:19], v[48:51]\n\t"
    "v_exp_f32 v84, v84\n\t"
    "v_mfma_f32_16x16x32_bf16 v[92:95], a[132:135], a[20:23], v[92:95]\n\t"
    "v_exp_f32 v85, v85\n\t"
    "v_mfma_f32_16x16x32_bf16 v[92:95], a[136:139], a[24:27], v[92:95]\n\t"
    "v_exp_f32 v86, v86\n\t"
    "v_mfma_f32_16x16x32_bf16 v[92:95], a[140:143], a[28:31], v[92:95]\n\t"
    "s_waitcnt lgkmcnt(2)\n\t"
    "v_exp_f32 v87, v87\n\t"
    "v_mfma_f32_16x16x32_bf16 v[96:99], v[36:39], a[144:147], v[96:99]\n\t"
    "v_exp_f32 v88, v88\n\t"
    "v_mfma_f32_16x16x32_bf16 v[96:99], v[40:43], a[148:151], v[96:99]\n\t"
    "v_exp_f32 v89, v89\n\t"
    "v_mfma_f32_16x16x32_bf16 v[100:103], v[36:39], a[152:155], v[100:103]\n\t"
    "v_exp_f32 v90, v90\n\t"
    "v_mfma_f32_16x16x32_bf16 v[100:103], v[40:43], a[156:159], v[100:103]\n\t"
    "v_exp_f32 v91, v91\n\t"
    "v_mfma_f32_16x16x32_bf16 v[104:107], v[36:39], a[160:163], v[104:107]\n\t"
    "v_exp_f32 v92, v92\n\t"
    "v_mfma_f32_16x16x32_bf16 v[104:107], v[40:43], a[164:167], v[104:107]\n\t"
    "v_exp_f32 v93, v93\n\t"
    "v_mfma_f32_16x16x32_bf16 v[108:111], v[36:39], a[168:171], v[108:111]\n\t"
    "v_exp_f32 v94, v94\n\t"
    "v_mfma_f32_16x16x32_bf16 v[108:111], v[40:43], a[172:175], v[108:111]\n\t"
    "v_exp_f32 v95, v95\n\t"
    "v_mfma_f32_16x16x32_bf16 v[112:115], v[36:39], a[176:179], v[112:115]\n\t"
    "buffer_load_dwordx4 v22, s[44:47], s72 offen offset:1024 nt lds\n\t"
    "v_mfma_f32_16x16x32_bf16 v[112:115], v[40:43], a[180:183], v[112:115]\n\t"
    "buffer_load_dwordx4 v20, s[44:47], s72 offen offset:2048 nt lds\n\t"
    "v_mfma_f32_16x16x32_bf16 v[116:119], v[36:39], a[184:187], v[116:119]\n\t"
    "buffer_load_dwordx4 v22, s[44:47], s72 offen offset:3072 nt lds\n\t"
    "s_add_u32 m0, s60, 8192\n\t"
    "v_mfma_f32_16x16x32_bf16 v[116:119], v[40:43], a[188:191], v[116:119]\n\t"
    "buffer_load_dwordx4 v20, s[44:47], s73 offen offset:0 nt lds\n\t"
    "v_mfma_f32_16x16x32_bf16 v[120:123], v[36:39], a[240:243], v[120:123]\n\t"
    "buffer_load_dwordx4 v22, s[44:47], s73 offen offset:1024 nt lds\n\t"
    "v_mfma_f32_16x16x32_bf16 v[120:123], v[40:43], a[244:247], v[120:123]\n\t"
    "buffer_load_dwordx4 v20, s[44:47], s73 offen offset:2048 nt lds\n\t"
    "v_mfma_f32_16x16x32_bf16 v[124:127], v[36:39], a[248:251], v[124:127]\n\t"
    "buffer_load_dwordx4 v22, s[44:47], s73 offen offset:3072 nt lds\n\t"
    "s_add_u32 m0, s60, 12288\n\t"
    "v_mfma_f32_16x16x32_bf16 v[124:127], v[40:43], a[252:255], v[124:127]\n\t"
    "buffer_load_dwordx4 v20, s[44:47], s74 offen offset:0 nt lds\n\t"
    "v_smfmac_f32_16x16x64_bf16 a[212:215], a[208:211], v[36:43], v24\n\t"
    "v_mfma_f32_16x16x32_bf16 v[64:67], a[80:83], a[32:35], v[52:55]\n\t"
    "ds_read_b64_tr_b16 a[192:193], v18 offset:0\n\t"
    "v_cvt_pk_bf16_f32 v36, v80, v81\n\t"
    "v_mfma_f32_16x16x32_bf16 v[64:67], a[84:87], a[36:39], v[64:67]\n\t"
    "ds_read_b64_tr_b16 a[194:195], v18 offset:4096\n\t"
    "v_cvt_pk_bf16_f32 v37, v82, v83\n\t"
    "v_mfma_f32_16x16x32_bf16 v[64:67], a[88:91], a[40:43], v[64:67]\n\t"
    "ds_read_b64_tr_b16 a[196:197], v18 offset:8192\n\t"
    "v_cvt_pk_bf16_f32 v38, v84, v85\n\t"
    "v_mfma_f32_16x16x32_bf16 v[64:67], a[92:95], a[44:47], v[64:67]\n\t"
    "ds_read_b64_tr_b16 a[198:199], v18 offset:12288\n\t"
    "v_cvt_pk_bf16_f32 v39, v86, v87\n\t"
    "v_mfma_f32_16x16x32_bf16 v[68:71], a[96:99], a[32:35], v[52:55]\n\t"
    "ds_read_b64_tr_b16 a[200:201], v19 offset:0\n\t"
    "v_cvt_pk_bf16_f32 v40, v88, v89\n\t"
    "v_mfma_f32_16x16x32_bf16 v[68:71], a[100:103], a[36:39], v[68:71]\n\t"
    "ds_read_b64_tr_b16 a[202:203], v19 offset:4096\n\t"
    "v_cvt_pk_bf16_f32 v41, v90, v91\n\t"
    "v_mfma_f32_16x16x32_bf16 v[68:71], a[104:107], a[40:43], v[68:71]\n\t"
    "ds_read_b64_tr_b16 a[204:205], v19 offset:8192\n\t"
    "v_cvt_pk_bf16_f32 v42, v92, v93\n\t"
    "v_mfma_f32_16x16x32_bf16 v[68:71], a[108:111], a[44:47], v[68:71]\n\t"
    "ds_read_b64_tr_b16 a[206:207], v19 offset:12288\n\t"
    "v_cvt_pk_bf16_f32 v43, v94, v95\n\t"
    "v_mfma_f32_16x16x32_bf16 v[72:75], a[112:115], a[32:35], v[52:55]\n\t"
    "v_exp_f32 v64, v64\n\t"
    "v_mfma_f32_16x16x32_bf16 v[72:75], a[116:119], a[36:39], v[72:75]\n\t"
    "v_exp_f32 v65, v65\n\t"
    "v_mfma_f32_16x16x32_bf16 v[72:75], a[120:123], a[40:43], v[72:75]\n\t"
    "v_exp_f32 v66, v66\n\t"
    "v_mfma_f32_16x16x32_bf16 v[72:75], a[124:127], a[44:47], v[72:75]\n\t"
    "v_exp_f32 v67, v67\n\t"
    "v_mfma_f32_16x16x32_bf16 v[76:79], a[128:131], a[32:35], v[52:55]\n\t"
    "v_exp_f32 v68, v68\n\t"
    "v_mfma_f32_16x16x32_bf16 v[76:79], a[132:135], a[36:39], v[76:79]\n\t"
    "v_exp_f32 v69, v69\n\t"
    "v_mfma_f32_16x16x32_bf16 v[76:79], a[136:139], a[40:43], v[76:79]\n\t"
    "v_exp_f32 v70, v70\n\t"
    "v_mfma_f32_16x16x32_bf16 v[76:79], a[140:143], a[44:47], v[76:79]\n\t"
    "v_exp_f32 v71, v71\n\t"
    "v_mfma_f32_16x16x32_bf16 v[128:131], v[36:39], a[144:147], v[128:131]\n\t"
    "v_exp_f32 v72, v72\n\t"
    "v_mfma_f32_16x16x32_bf16 v[128:131], v[40:43], a[148:151], v[128:131]\n\t"
    "v_exp_f32 v73, v73\n\t"
    "v_mfma_f32_16x16x32_bf16 v[132:135], v[36:39], a[152:155], v[132:135]\n\t"
    "v_exp_f32 v74, v74\n\t"
    "v_mfma_f32_16x16x32_bf16 v[132:135], v[40:43], a[156:159], v[132:135]\n\t"
    "v_exp_f32 v75, v75\n\t"
    "v_mfma_f32_16x16x32_bf16 v[136:139], v[36:39], a[160:163], v[136:139]\n\t"
    "v_exp_f32 v76, v76\n\t"
    "v_mfma_f32_16x16x32_bf16 v[136:139], v[40:43], a[164:167], v[136:139]\n\t"
    "v_exp_f32 v77, v77\n\t"
    "v_mfma_f32_16x16x32_bf16 v[140:143], v[36:39], a[168:171], v[140:143]\n\t"
    "v_exp_f32 v78, v78\n\t"
    "v_mfma_f32_16x16x32_bf16 v[140:143], v[40:43], a[172:175], v[140:143]\n\t"
    "v_exp_f32 v79, v79\n\t"
    "v_mfma_f32_16x16x32_bf16 v[144:147], v[36:39], a[176:179], v[144:147]\n\t"
    "buffer_load_dwordx4 v22, s[44:47], s74 offen offset:1024 nt lds\n\t"
    "v_mfma_f32_16x16x32_bf16 v[144:147], v[40:43], a[180:183], v[144:147]\n\t"
    "buffer_load_dwordx4 v20, s[44:47], s74 offen offset:2048 nt lds\n\t"
    "v_mfma_f32_16x16x32_bf16 v[148:151], v[36:39], a[184:187], v[148:151]\n\t"
    "buffer_load_dwordx4 v22, s[44:47], s74 offen offset:3072 nt lds\n\t"
    "s_add_u32 m0, s60, 16384\n\t"
    "v_mfma_f32_16x16x32_bf16 v[148:151], v[40:43], a[188:191], v[148:151]\n\t"
    "buffer_load_dwordx4 v20, s[44:47], s58 offen offset:0 nt lds\n\t"
    "v_mfma_f32_16x16x32_bf16 v[152:155], v[36:39], a[240:243], v[152:155]\n\t"
    "buffer_load_dwordx4 v22, s[44:47], s58 offen offset:1024 nt lds\n\t"
    "v_mfma_f32_16x16x32_bf16 v[152:155], v[40:43], a[244:247], v[152:155]\n\t"
    "buffer_load_dwordx4 v20, s[44:47], s58 offen offset:2048 nt lds\n\t"
    "v_mfma_f32_16x16x32_bf16 v[156:159], v[36:39], a[248:251], v[156:159]\n\t"
    "buffer_load_dwordx4 v22, s[44:47], s58 offen offset:3072 nt lds\n\t"
    "v_mfma_f32_16x16x32_bf16 v[156:159], v[40:43], a[252:255], v[156:159]\n\t"
    "s_add_u32 s58, s58, 16384\n\t"
    "s_and_b32 s58, s58, 0x7ffff\n\t"
    "v_smfmac_f32_16x16x64_bf16 a[216:219], a[208:211], v[36:43], v24\n\t"
    "v_mfma_f32_16x16x32_bf16 v[80:83], a[80:83], a[48:51], v[56:59]\n\t"
    "ds_read_b128 a[80:83], v8 offset:0\n\t"
    "v_cvt_pk_bf16_f32 v36, v64, v65\n\t"
    "v_mfma_f32_16x16x32_bf16 v[80:83], a[84:87], a[52:55], v[80:83]\n\t"
    "ds_read_b128 a[84:87], v9 offset:0\n\t"
    "v_cvt_pk_bf16_f32 v37, v66, v67\n\t"
    "v_mfma_f32_16x16x32_bf16 v[80:83], a[88:91], a[56:59], v[80:83]\n\t"
    "ds_read_b128 a[88:91], v10 offset:0\n\t"
    "v_cvt_pk_bf16_f32 v38, v68, v69\n\t"
    "v_mfma_f32_16x16x32_bf16 v[80:83], a[92:95], a[60:63], v[80:83]\n\t"
    "ds_read_b128 a[92:95], v11 offset:0\n\t"
    "v_cvt_pk_bf16_f32 v39, v70, v71\n\t"
    "v_mfma_f32_16x16x32_bf16 v[84:87], a[96:99], a[48:51], v[56:59]\n\t"
    "ds_read_b128 a[96:99], v8 offset:4096\n\t"
    "v_cvt_pk_bf16_f32 v40, v72, v73\n\t"
    "v_mfma_f32_16x16x32_bf16 v[84:87], a[100:103], a[52:55], v[84:87]\n\t"
    "ds_read_b128 a[100:103], v9 offset:4096\n\t"
    "v_cvt_pk_bf16_f32 v41, v74, v75\n\t"
    "v_mfma_f32_16x16x32_bf16 v[84:87], a[104:107], a[56:59], v[84:87]\n\t"
    "ds_read_b128 a[104:107], v10 offset:4096\n\t"
    "v_cvt_pk_bf16_f32 v42, v76, v77\n\t"
    "v_mfma_f32_16x16x32_bf16 v[84:87], a[108:111], a[60:63], v[84:87]\n\t"
    "ds_read_b128 a[108:111], v11 offset:4096\n\t"
    "v_cvt_pk_bf16_f32 v43, v78, v79\n\t"
    "v_mfma_f32_16x16x32_bf16 v[88:91], a[112:115], a[48:51], v[56:59]\n\t"
    "v_exp_f32 v80, v80\n\t"
    "v_mfma_f32_16x16x32_bf16 v[88:91], a[116:119], a[52:55], v[88:91]\n\t"
    "v_exp_f32 v81, v81\n\t"
    "v_mfma_f32_16x16x32_bf16 v[88:91], a[120:123], a[56:59], v[88:91]\n\t"
    "v_exp_f32 v82, v82\n\t"
    "v_mfma_f32_16x16x32_bf16 v[88:91], a[124:127], a[60:63], v[88:91]\n\t"
    "v_exp_f32 v83, v83\n\t"
    "v_mfma_f32_16x16x32_bf16 v[92:95], a[128:131], a[48:51], v[56:59]\n\t"
    "v_exp_f32 v84, v84\n\t"
    "v_mfma_f32_16x16x32_bf16 v[92:95], a[132:135], a[52:55], v[92:95]\n\t"
    "v_exp_f32 v85, v85\n\t"
    "v_mfma_f32_16x16x32_bf16 v[92:95], a[136:139], a[56:59], v[92:95]\n\t"
    "v_exp_f32 v86, v86\n\t"
    "v_mfma_f32_16x16x32_bf16 v[92:95], a[140:143], a[60:63], v[92:95]\n\t"
    "s_waitcnt lgkmcnt(0)\n\t"
    "v_exp_f32 v87, v87\n\t"
    "v_mfma_f32_16x16x32_bf16 v[64:67], a[80:83], a[64:67], v[60:63]\n\t"
    "v_exp_f32 v88, v88\n\t"
    "v_mfma_f32_16x16x32_bf16 v[64:67], a[84:87], a[68:71], v[64:67]\n\t"
    "v_exp_f32 v89, v89\n\t"
    "v_mfma_f32_16x16x32_bf16 v[64:67], a[88:91], a[72:75], v[64:67]\n\t"
    "v_exp_f32 v90, v90\n\t"
    "v_mfma_f32_16x16x32_bf16 v[64:67], a[92:95], a[76:79], v[64:67]\n\t"
    "v_exp_f32 v91, v91\n\t"
    "v_mfma_f32_16x16x32_bf16 v[68:71], a[80:83], v[224:227], v[252:255]\n\t"
    "v_exp_f32 v92, v92\n\t"
    "v_mfma_f32_16x16x32_bf16 v[68:71], a[84:87], v[228:231], v[68:71]\n\t"
    "v_exp_f32 v93, v93\n\t"
    "v_mfma_f32_16x16x32_bf16 v[68:71], a[88:91], v[232:235], v[68:71]\n\t"
    "v_exp_f32 v94, v94\n\t"
    "v_mfma_f32_16x16x32_bf16 v[68:71], a[92:95], v[236:239], v[68:71]\n\t"
    "v_exp_f32 v95, v95\n\t"
    "v_mfma_f32_16x16x32_bf16 v[72:75], a[80:83], a[228:231], v[32:35]\n\t"
    "v_exp_f32 v64, v64\n\t"
    "v_mfma_f32_16x16x32_bf16 v[72:75], a[84:87], v[240:243], v[72:75]\n\t"
    "v_exp_f32 v65, v65\n\t"
    "v_mfma_f32_16x16x32_bf16 v[72:75], a[88:91], a[232:235], v[72:75]\n\t"
    "v_exp_f32 v66, v66\n\t"
    "v_mfma_f32_16x16x32_bf16 v[72:75], a[92:95], a[236:239], v[72:75]\n\t"
    "v_exp_f32 v67, v67\n\t"
    "v_mfma_f32_16x16x32_bf16 v[160:163], v[36:39], a[144:147], v[160:163]\n\t"
    "v_exp_f32 v68, v68\n\t"
    "v_mfma_f32_16x16x32_bf16 v[160:163], v[40:43], a[148:151], v[160:163]\n\t"
    "v_exp_f32 v69, v69\n\t"
    "v_mfma_f32_16x16x32_bf16 v[164:167], v[36:39], a[152:155], v[164:167]\n\t"
    "v_exp_f32 v70, v70\n\t"
    "v_mfma_f32_16x16x32_bf16 v[164:167], v[40:43], a[156:159], v[164:167]\n\t"
    "v_exp_f32 v71, v71\n\t"
    "v_mfma_f32_16x16x32_bf16 v[168:171], v[36:39], a[160:163], v[168:171]\n\t"
    "ds_read_b32 v245, v246\n\t"
    "v_exp_f32 v72, v72\n\t"
    "v_mfma_f32_16x16x32_bf16 v[168:171], v[40:43], a[164:167], v[168:171]\n\t"
    "v_exp_f32 v73, v73\n\t"
    "v_mfma_f32_16x16x32_bf16 v[172:175], v[36:39], a[168:171], v[172:175]\n\t"
    "v_exp_f32 v74, v74\n\t"
    "v_mfma_f32_16x16x32_bf16 v[172:175], v[40:43], a[172:175], v[172:175]\n\t"
    "v_exp_f32 v75, v75\n\t"
    "v_mfma_f32_16x16x32_bf16 v[176:179], v[36:39], a[176:179], v[176:179]\n\t"
    "ds_read_b128 a[112:115], v8 offset:8192\n\t"
    "v_cvt_pk_bf16_f32 v64, v64, v65\n\t"
    "v_mfma_f32_16x16x32_bf16 v[176:179], v[40:43], a[180:183], v[176:179]\n\t"
    "ds_read_b128 a[116:119], v9 offset:8192\n\t"
    "v_cvt_pk_bf16_f32 v65, v66, v67\n\t"
    "s_waitcnt lgkmcnt(2)\n\t"
    "v_cmp_gt_u32 vcc, v244, v245\n\t"
    "v_mfma_f32_16x16x32_bf16 v[180:183], v[36:39], a[184:187], v[180:183]\n\t"
    "ds_read_b128 a[120:123], v10 offset:8192\n\t"
    "v_cvt_pk_bf16_f32 v68, v68, v69\n\t"
    "v_mfma_f32_16x16x32_bf16 v[180:183], v[40:43], a[188:191], v[180:183]\n\t"
    "ds_read_b128 a[124:127], v11 offset:8192\n\t"
    "v_cvt_pk_bf16_f32 v69, v70, v71\n\t"
    "v_mfma_f32_16x16x32_bf16 v[184:187], v[36:39], a[240:243], v[184:187]\n\t"
    "s_cbranch_vccnz SPIN4L3%=\n\t"
    "RET4L3%=:\n\t"
    "ds_read_b128 a[128:131], v8 offset:12288\n\t"
    "v_cvt_pk_bf16_f32 v72, v72, v73\n\t"
    "v_mfma_f32_16x16x32_bf16 v[184:187], v[40:43], a[244:247], v[184:187]\n\t"
    "ds_read_b128 a[132:135], v9 offset:12288\n\t"
    "v_cvt_pk_bf16_f32 v73, v74, v75\n\t"
    "v_mfma_f32_16x16x32_bf16 v[188:191], v[36:39], a[248:251], v[188:191]\n\t"
    "ds_read_b128 a[136:139], v10 offset:12288\n\t"
    "ds_write_b64 v27, v[64:65] offset:0\n\t"
    "v_mfma_f32_16x16x32_bf16 v[188:191], v[40:43], a[252:255], v[188:191]\n\t"
    "ds_read_b128 a[140:143], v11 offset:12288\n\t"
    "ds_write_b64 v27, v[68:69] offset:1024\n\t"
    "v_smfmac_f32_16x16x64_bf16 a[220:223], a[208:211], v[36:43], v24\n\t"
    "ds_write_b64 v27, v[72:73] offset:2048\n\t"
    "; TILE 4\n\t"
    "v_mfma_f32_16x16x32_bf16 v[64:67], a[80:83], a[0:3], v[44:47]\n\t"
    "s_sub_u32 s72, s58, 12288\n\t"
    "s_sub_u32 s73, s58, 8192\n\t"
    "s_sub_u32 s74, s58, 4096\n\t"
    "s_add_u32 m0, s59, 20480\n\t"
    "v_cvt_pk_bf16_f32 v36, v80, v81\n\t"
    "v_mfma_f32_16x16x32_bf16 v[64:67], a[84:87], a[4:7], v[64:67]\n\t"
    "buffer_load_dwordx4 v20, s[40:43], s72 offen offset:0 nt lds\n\t"
    "v_cvt_pk_bf16_f32 v37, v82, v83\n\t"
    "v_mfma_f32_16x16x32_bf16 v[64:67], a[88:91], a[8:11], v[64:67]\n\t"
    "buffer_load_dwordx4 v21, s[40:43], s72 offen offset:1024 nt lds\n\t"
    "v_cvt_pk_bf16_f32 v38, v84, v85\n\t"
    "v_mfma_f32_16x16x32_bf16 v[64:67], a[92:95], a[12:15], v[64:67]\n\t"
    "buffer_load_dwordx4 v22, s[40:43], s72 offen offset:2048 nt lds\n\t"
    "v_cvt_pk_bf16_f32 v39, v86, v87\n\t"
    "v_mfma_f32_16x16x32_bf16 v[68:71], a[96:99], a[0:3], v[44:47]\n\t"
    "buffer_load_dwordx4 v23, s[40:43], s72 offen offset:3072 nt lds\n\t"
    "s_add_u32 m0, s59, 24576\n\t"
    "v_cvt_pk_bf16_f32 v40, v88, v89\n\t"
    "v_mfma_f32_16x16x32_bf16 v[68:71], a[100:103], a[4:7], v[68:71]\n\t"
    "buffer_load_dwordx4 v20, s[40:43], s73 offen offset:0 nt lds\n\t"
    "v_cvt_pk_bf16_f32 v41, v90, v91\n\t"
    "v_mfma_f32_16x16x32_bf16 v[68:71], a[104:107], a[8:11], v[68:71]\n\t"
    "buffer_load_dwordx4 v21, s[40:43], s73 offen offset:1024 nt lds\n\t"
    "v_cvt_pk_bf16_f32 v42, v92, v93\n\t"
    "v_mfma_f32_16x16x32_bf16 v[68:71], a[108:111], a[12:15], v[68:71]\n\t"
    "s_waitcnt lgkmcnt(0)\n\t"
    "buffer_load_dwordx4 v22, s[40:43], s73 offen offset:2048 nt lds\n\t"
    "v_cvt_pk_bf16_f32 v43, v94, v95\n\t"
    "v_mfma_f32_16x16x32_bf16 v[72:75], a[112:115], a[0:3], v[44:47]\n\t"
    "buffer_load_dwordx4 v23, s[40:43], s73 offen offset:3072 nt lds\n\t"
    "v_exp_f32 v64, v64\n\t"
    "v_mfma_f32_16x16x32_bf16 v[72:75], a[116:119], a[4:7], v[72:75]\n\t"
    "v_exp_f32 v65, v65\n\t"
    "v_mfma_f32_16x16x32_bf16 v[72:75], a[120:123], a[8:11], v[72:75]\n\t"
    "v_exp_f32 v66, v66\n\t"
    "v_mfma_f32_16x16x32_bf16 v[72:75], a[124:127], a[12:15], v[72:75]\n\t"
    "v_exp_f32 v67, v67\n\t"
    "v_mfma_f32_16x16x32_bf16 v[76:79], a[128:131], a[0:3], v[44:47]\n\t"
    "v_exp_f32 v68, v68\n\t"
    "v_mfma_f32_16x16x32_bf16 v[76:79], a[132:135], a[4:7], v[76:79]\n\t"
    "v_exp_f32 v69, v69\n\t"
    "v_mfma_f32_16x16x32_bf16 v[76:79], a[136:139], a[8:11], v[76:79]\n\t"
    "v_exp_f32 v70, v70\n\t"
    "v_mfma_f32_16x16x32_bf16 v[76:79], a[140:143], a[12:15], v[76:79]\n\t"
    "v_exp_f32 v71, v71\n\t"
    "v_mfma_f32_16x16x32_bf16 v[192:195], v[36:39], a[144:147], v[192:195]\n\t"
    "v_exp_f32 v72, v72\n\t"
    "v_mfma_f32_16x16x32_bf16 v[192:195], v[40:43], a[148:151], v[192:195]\n\t"
    "v_exp_f32 v73, v73\n\t"
    "v_mfma_f32_16x16x32_bf16 v[196:199], v[36:39], a[152:155], v[196:199]\n\t"
    "v_exp_f32 v74, v74\n\t"
    "v_mfma_f32_16x16x32_bf16 v[196:199], v[40:43], a[156:159], v[196:199]\n\t"
    "v_exp_f32 v75, v75\n\t"
    "v_mfma_f32_16x16x32_bf16 v[200:203], v[36:39], a[160:163], v[200:203]\n\t"
    "v_exp_f32 v76, v76\n\t"
    "v_mfma_f32_16x16x32_bf16 v[200:203], v[40:43], a[164:167], v[200:203]\n\t"
    "v_exp_f32 v77, v77\n\t"
    "v_mfma_f32_16x16x32_bf16 v[204:207], v[36:39], a[168:171], v[204:207]\n\t"
    "v_exp_f32 v78, v78\n\t"
    "v_mfma_f32_16x16x32_bf16 v[204:207], v[40:43], a[172:175], v[204:207]\n\t"
    "s_add_u32 m0, s59, 28672\n\t"
    "v_exp_f32 v79, v79\n\t"
    "v_mfma_f32_16x16x32_bf16 v[208:211], v[36:39], a[176:179], v[208:211]\n\t"
    "ds_read_b64_tr_b16 a[144:145], v12 offset:0\n\t"
    "ds_read_b64_tr_b16 a[146:147], v12 offset:4096\n\t"
    "buffer_load_dwordx4 v20, s[40:43], s74 offen offset:0 nt lds\n\t"
    "v_mfma_f32_16x16x32_bf16 v[208:211], v[40:43], a[180:183], v[208:211]\n\t"
    "ds_read_b64_tr_b16 a[148:149], v12 offset:8192\n\t"
    "ds_read_b64_tr_b16 a[150:151], v12 offset:12288\n\t"
    "v_mfma_f32_16x16x32_bf16 v[212:215], v[36:39], a[184:187], v[212:215]\n\t"
    "ds_read_b64_tr_b16 a[152:153], v13 offset:0\n\t"
    "ds_read_b64_tr_b16 a[154:155], v13 offset:4096\n\t"
    "buffer_load_dwordx4 v21, s[40:43], s74 offen offset:1024 nt lds\n\t"
    "v_mfma_f32_16x16x32_bf16 v[212:215], v[40:43], a[188:191], v[212:215]\n\t"
    "ds_read_b64_tr_b16 a[156:157], v13 offset:8192\n\t"
    "ds_read_b64_tr_b16 a[158:159], v13 offset:12288\n\t"
    "v_mfma_f32_16x16x32_bf16 v[216:219], v[36:39], a[240:243], v[216:219]\n\t"
    "ds_read_b64_tr_b16 a[160:161], v14 offset:0\n\t"
    "ds_read_b64_tr_b16 a[162:163], v14 offset:4096\n\t"
    "buffer_load_dwordx4 v22, s[40:43], s74 offen offset:2048 nt lds\n\t"
    "v_mfma_f32_16x16x32_bf16 v[216:219], v[40:43], a[244:247], v[216:219]\n\t"
    "ds_read_b64_tr_b16 a[164:165], v14 offset:8192\n\t"
    "ds_read_b64_tr_b16 a[166:167], v14 offset:12288\n\t"
    "v_mfma_f32_16x16x32_bf16 v[220:223], v[36:39], a[248:251], v[220:223]\n\t"
    "ds_read_b64_tr_b16 a[168:169], v15 offset:0\n\t"
    "ds_read_b64_tr_b16 a[170:171], v15 offset:4096\n\t"
    "buffer_load_dwordx4 v23, s[40:43], s74 offen offset:3072 nt lds\n\t"
    "v_mfma_f32_16x16x32_bf16 v[220:223], v[40:43], a[252:255], v[220:223]\n\t"
    "s_add_u32 m0, s59, 32768\n\t"
    "ds_read_b64_tr_b16 a[172:173], v15 offset:8192\n\t"
    "ds_read_b64_tr_b16 a[174:175], v15 offset:12288\n\t"
    "v_smfmac_f32_16x16x64_bf16 a[224:227], a[208:211], v[36:43], v24\n\t"
    "buffer_load_dwordx4 v20, s[40:43], s58 offen offset:0 nt lds\n\t"
    "v_mfma_f32_16x16x32_bf16 v[80:83], a[80:83], a[16:19], v[48:51]\n\t"
    "ds_read_b64_tr_b16 a[176:177], v16 offset:0\n\t"
    "v_cvt_pk_bf16_f32 v36, v64, v65\n\t"
    "v_mfma_f32_16x16x32_bf16 v[80:83], a[84:87], a[20:23], v[80:83]\n\t"
    "ds_read_b64_tr_b16 a[178:179], v16 offset:4096\n\t"
    "buffer_load_dwordx4 v21, s[40:43], s58 offen offset:1024 nt lds\n\t"
    "v_cvt_pk_bf16_f32 v37, v66, v67\n\t"
    "v_mfma_f32_16x16x32_bf16 v[80:83], a[88:91], a[24:27], v[80:83]\n\t"
    "ds_read_b64_tr_b16 a[180:181], v16 offset:8192\n\t"
    "v_cvt_pk_bf16_f32 v38, v68, v69\n\t"
    "v_mfma_f32_16x16x32_bf16 v[80:83], a[92:95], a[28:31], v[80:83]\n\t"
    "ds_read_b64_tr_b16 a[182:183], v16 offset:12288\n\t"
    "buffer_load_dwordx4 v22, s[40:43], s58 offen offset:2048 nt lds\n\t"
    "v_cvt_pk_bf16_f32 v39, v70, v71\n\t"
    "v_mfma_f32_16x16x32_bf16 v[84:87], a[96:99], a[16:19], v[48:51]\n\t"
    "ds_read_b64_tr_b16 a[184:185], v17 offset:0\n\t"
    "v_cvt_pk_bf16_f32 v40, v72, v73\n\t"
    "v_mfma_f32_16x16x32_bf16 v[84:87], a[100:103], a[20:23], v[84:87]\n\t"
    "ds_read_b64_tr_b16 a[186:187], v17 offset:4096\n\t"
    "buffer_load_dwordx4 v23, s[40:43], s58 offen offset:3072 nt lds\n\t"
    "v_cvt_pk_bf16_f32 v41, v74, v75\n\t"
    "v_mfma_f32_16x16x32_bf16 v[84:87], a[104:107], a[24:27], v[84:87]\n\t"
    "s_add_u32 m0, s60, 20480\n\t"
    "ds_read_b64_tr_b16 a[188:189], v17 offset:8192\n\t"
    "v_cvt_pk_bf16_f32 v42, v76, v77\n\t"
    "v_add_u32 v244, 1, v244\n\t"
    "v_mfma_f32_16x16x32_bf16 v[84:87], a[108:111], a[28:31], v[84:87]\n\t"
    "s_waitcnt vmcnt(16)\n\t"
    "ds_write_b32 v27, v244 offset:3072\n\t"
    "ds_read_b64_tr_b16 a[190:191], v17 offset:12288\n\t"
    "buffer_load_dwordx4 v20, s[44:47], s72 offen offset:0 nt lds\n\t"
    "v_cvt_pk_bf16_f32 v43, v78, v79\n\t"
    "v_mfma_f32_16x16x32_bf16 v[88:91], a[112:115], a[16:19], v[48:51]\n\t"
    "ds_write_b32 v27, v244 offset:4096\n\t"
    "v_exp_f32 v80, v80\n\t"
    "v_mfma_f32_16x16x32_bf16 v[88:91], a[116:119], a[20:23], v[88:91]\n\t"
    "ds_write_b32 v27, v244 offset:5120\n\t"
    "v_exp_f32 v81, v81\n\t"
    "v_mfma_f32_16x16x32_bf16 v[88:91], a[120:123], a[24:27], v[88:91]\n\t"
    "v_exp_f32 v82, v82\n\t"
    "v_mfma_f32_16x16x32_bf16 v[88:91], a[124:127], a[28:31], v[88:91]\n\t"
    "v_exp_f32 v83, v83\n\t"
    "v_mfma_f32_16x16x32_bf16 v[92:95], a[128:131], a[16:19], v[48:51]\n\t"
    "v_exp_f32 v84, v84\n\t"
    "v_mfma_f32_16x16x32_bf16 v[92:95], a[132:135], a[20:23], v[92:95]\n\t"
    "v_exp_f32 v85, v85\n\t"
    "v_mfma_f32_16x16x32_bf16 v[92:95], a[136:139], a[24:27], v[92:95]\n\t"
    "v_exp_f32 v86, v86\n\t"
    "v_mfma_f32_16x16x32_bf16 v[92:95], a[140:143], a[28:31], v[92:95]\n\t"
    "s_waitcnt lgkmcnt(2)\n\t"
    "v_exp_f32 v87, v87\n\t"
    "v_mfma_f32_16x16x32_bf16 v[96:99], v[36:39], a[144:147], v[96:99]\n\t"
    "v_exp_f32 v88, v88\n\t"
    "v_mfma_f32_16x16x32_bf16 v[96:99], v[40:43], a[148:151], v[96:99]\n\t"
    "v_exp_f32 v89, v89\n\t"
    "v_mfma_f32_16x16x32_bf16 v[100:103], v[36:39], a[152:155], v[100:103]\n\t"
    "v_exp_f32 v90, v90\n\t"
    "v_mfma_f32_16x16x32_bf16 v[100:103], v[40:43], a[156:159], v[100:103]\n\t"
    "v_exp_f32 v91, v91\n\t"
    "v_mfma_f32_16x16x32_bf16 v[104:107], v[36:39], a[160:163], v[104:107]\n\t"
    "v_exp_f32 v92, v92\n\t"
    "v_mfma_f32_16x16x32_bf16 v[104:107], v[40:43], a[164:167], v[104:107]\n\t"
    "v_exp_f32 v93, v93\n\t"
    "v_mfma_f32_16x16x32_bf16 v[108:111], v[36:39], a[168:171], v[108:111]\n\t"
    "v_exp_f32 v94, v94\n\t"
    "v_mfma_f32_16x16x32_bf16 v[108:111], v[40:43], a[172:175], v[108:111]\n\t"
    "v_exp_f32 v95, v95\n\t"
    "v_mfma_f32_16x16x32_bf16 v[112:115], v[36:39], a[176:179], v[112:115]\n\t"
    "buffer_load_dwordx4 v22, s[44:47], s72 offen offset:1024 nt lds\n\t"
    "v_mfma_f32_16x16x32_bf16 v[112:115], v[40:43], a[180:183], v[112:115]\n\t"
    "buffer_load_dwordx4 v20, s[44:47], s72 offen offset:2048 nt lds\n\t"
    "v_mfma_f32_16x16x32_bf16 v[116:119], v[36:39], a[184:187], v[116:119]\n\t"
    "buffer_load_dwordx4 v22, s[44:47], s72 offen offset:3072 nt lds\n\t"
    "s_add_u32 m0, s60, 24576\n\t"
    "v_mfma_f32_16x16x32_bf16 v[116:119], v[40:43], a[188:191], v[116:119]\n\t"
    "buffer_load_dwordx4 v20, s[44:47], s73 offen offset:0 nt lds\n\t"
    "v_mfma_f32_16x16x32_bf16 v[120:123], v[36:39], a[192:195], v[120:123]\n\t"
    "buffer_load_dwordx4 v22, s[44:47], s73 offen offset:1024 nt lds\n\t"
    "v_mfma_f32_16x16x32_bf16 v[120:123], v[40:43], a[196:199], v[120:123]\n\t"
    "buffer_load_dwordx4 v20, s[44:47], s73 offen offset:2048 nt lds\n\t"
    "v_mfma_f32_16x16x32_bf16 v[124:127], v[36:39], a[200:203], v[124:127]\n\t"
    "buffer_load_dwordx4 v22, s[44:47], s73 offen offset:3072 nt lds\n\t"
    "s_add_u32 m0, s60, 28672\n\t"
    "v_mfma_f32_16x16x32_bf16 v[124:127], v[40:43], a[204:207], v[124:127]\n\t"
    "buffer_load_dwordx4 v20, s[44:47], s74 offen offset:0 nt lds\n\t"
    "v_smfmac_f32_16x16x64_bf16 a[212:215], a[208:211], v[36:43], v24\n\t"
    "v_mfma_f32_16x16x32_bf16 v[64:67], a[80:83], a[32:35], v[52:55]\n\t"
    "ds_read_b64_tr_b16 a[240:241], v18 offset:16384\n\t"
    "v_cvt_pk_bf16_f32 v36, v80, v81\n\t"
    "v_mfma_f32_16x16x32_bf16 v[64:67], a[84:87], a[36:39], v[64:67]\n\t"
    "ds_read_b64_tr_b16 a[242:243], v18 offset:20480\n\t"
    "v_cvt_pk_bf16_f32 v37, v82, v83\n\t"
    "v_mfma_f32_16x16x32_bf16 v[64:67], a[88:91], a[40:43], v[64:67]\n\t"
    "ds_read_b64_tr_b16 a[244:245], v18 offset:24576\n\t"
    "v_cvt_pk_bf16_f32 v38, v84, v85\n\t"
    "v_mfma_f32_16x16x32_bf16 v[64:67], a[92:95], a[44:47], v[64:67]\n\t"
    "ds_read_b64_tr_b16 a[246:247], v18 offset:28672\n\t"
    "v_cvt_pk_bf16_f32 v39, v86, v87\n\t"
    "v_mfma_f32_16x16x32_bf16 v[68:71], a[96:99], a[32:35], v[52:55]\n\t"
    "ds_read_b64_tr_b16 a[248:249], v19 offset:16384\n\t"
    "v_cvt_pk_bf16_f32 v40, v88, v89\n\t"
    "v_mfma_f32_16x16x32_bf16 v[68:71], a[100:103], a[36:39], v[68:71]\n\t"
    "ds_read_b64_tr_b16 a[250:251], v19 offset:20480\n\t"
    "v_cvt_pk_bf16_f32 v41, v90, v91\n\t"
    "v_mfma_f32_16x16x32_bf16 v[68:71], a[104:107], a[40:43], v[68:71]\n\t"
    "ds_read_b64_tr_b16 a[252:253], v19 offset:24576\n\t"
    "v_cvt_pk_bf16_f32 v42, v92, v93\n\t"
    "v_mfma_f32_16x16x32_bf16 v[68:71], a[108:111], a[44:47], v[68:71]\n\t"
    "ds_read_b64_tr_b16 a[254:255], v19 offset:28672\n\t"
    "v_cvt_pk_bf16_f32 v43, v94, v95\n\t"
    "v_mfma_f32_16x16x32_bf16 v[72:75], a[112:115], a[32:35], v[52:55]\n\t"
    "v_exp_f32 v64, v64\n\t"
    "v_mfma_f32_16x16x32_bf16 v[72:75], a[116:119], a[36:39], v[72:75]\n\t"
    "v_exp_f32 v65, v65\n\t"
    "v_mfma_f32_16x16x32_bf16 v[72:75], a[120:123], a[40:43], v[72:75]\n\t"
    "v_exp_f32 v66, v66\n\t"
    "v_mfma_f32_16x16x32_bf16 v[72:75], a[124:127], a[44:47], v[72:75]\n\t"
    "v_exp_f32 v67, v67\n\t"
    "v_mfma_f32_16x16x32_bf16 v[76:79], a[128:131], a[32:35], v[52:55]\n\t"
    "v_exp_f32 v68, v68\n\t"
    "v_mfma_f32_16x16x32_bf16 v[76:79], a[132:135], a[36:39], v[76:79]\n\t"
    "v_exp_f32 v69, v69\n\t"
    "v_mfma_f32_16x16x32_bf16 v[76:79], a[136:139], a[40:43], v[76:79]\n\t"
    "v_exp_f32 v70, v70\n\t"
    "v_mfma_f32_16x16x32_bf16 v[76:79], a[140:143], a[44:47], v[76:79]\n\t"
    "v_exp_f32 v71, v71\n\t"
    "v_mfma_f32_16x16x32_bf16 v[128:131], v[36:39], a[144:147], v[128:131]\n\t"
    "v_exp_f32 v72, v72\n\t"
    "v_mfma_f32_16x16x32_bf16 v[128:131], v[40:43], a[148:151], v[128:131]\n\t"
    "v_exp_f32 v73, v73\n\t"
    "v_mfma_f32_16x16x32_bf16 v[132:135], v[36:39], a[152:155], v[132:135]\n\t"
    "v_exp_f32 v74, v74\n\t"
    "v_mfma_f32_16x16x32_bf16 v[132:135], v[40:43], a[156:159], v[132:135]\n\t"
    "v_exp_f32 v75, v75\n\t"
    "v_mfma_f32_16x16x32_bf16 v[136:139], v[36:39], a[160:163], v[136:139]\n\t"
    "v_exp_f32 v76, v76\n\t"
    "v_mfma_f32_16x16x32_bf16 v[136:139], v[40:43], a[164:167], v[136:139]\n\t"
    "v_exp_f32 v77, v77\n\t"
    "v_mfma_f32_16x16x32_bf16 v[140:143], v[36:39], a[168:171], v[140:143]\n\t"
    "v_exp_f32 v78, v78\n\t"
    "v_mfma_f32_16x16x32_bf16 v[140:143], v[40:43], a[172:175], v[140:143]\n\t"
    "v_exp_f32 v79, v79\n\t"
    "v_mfma_f32_16x16x32_bf16 v[144:147], v[36:39], a[176:179], v[144:147]\n\t"
    "buffer_load_dwordx4 v22, s[44:47], s74 offen offset:1024 nt lds\n\t"
    "v_mfma_f32_16x16x32_bf16 v[144:147], v[40:43], a[180:183], v[144:147]\n\t"
    "buffer_load_dwordx4 v20, s[44:47], s74 offen offset:2048 nt lds\n\t"
    "v_mfma_f32_16x16x32_bf16 v[148:151], v[36:39], a[184:187], v[148:151]\n\t"
    "buffer_load_dwordx4 v22, s[44:47], s74 offen offset:3072 nt lds\n\t"
    "s_add_u32 m0, s60, 32768\n\t"
    "v_mfma_f32_16x16x32_bf16 v[148:151], v[40:43], a[188:191], v[148:151]\n\t"
    "buffer_load_dwordx4 v20, s[44:47], s58 offen offset:0 nt lds\n\t"
    "v_mfma_f32_16x16x32_bf16 v[152:155], v[36:39], a[192:195], v[152:155]\n\t"
    "buffer_load_dwordx4 v22, s[44:47], s58 offen offset:1024 nt lds\n\t"
    "v_mfma_f32_16x16x32_bf16 v[152:155], v[40:43], a[196:199], v[152:155]\n\t"
    "buffer_load_dwordx4 v20, s[44:47], s58 offen offset:2048 nt lds\n\t"
    "v_mfma_f32_16x16x32_bf16 v[156:159], v[36:39], a[200:203], v[156:159]\n\t"
    "buffer_load_dwordx4 v22, s[44:47], s58 offen offset:3072 nt lds\n\t"
    "v_mfma_f32_16x16x32_bf16 v[156:159], v[40:43], a[204:207], v[156:159]\n\t"
    "s_add_u32 s58, s58, 16384\n\t"
    "s_and_b32 s58, s58, 0x7ffff\n\t"
    "v_smfmac_f32_16x16x64_bf16 a[216:219], a[208:211], v[36:43], v24\n\t"
    "v_mfma_f32_16x16x32_bf16 v[80:83], a[80:83], a[48:51], v[56:59]\n\t"
    "ds_read_b128 a[80:83], v8 offset:16384\n\t"
    "v_cvt_pk_bf16_f32 v36, v64, v65\n\t"
    "v_mfma_f32_16x16x32_bf16 v[80:83], a[84:87], a[52:55], v[80:83]\n\t"
    "ds_read_b128 a[84:87], v9 offset:16384\n\t"
    "v_cvt_pk_bf16_f32 v37, v66, v67\n\t"
    "v_mfma_f32_16x16x32_bf16 v[80:83], a[88:91], a[56:59], v[80:83]\n\t"
    "ds_read_b128 a[88:91], v10 offset:16384\n\t"
    "v_cvt_pk_bf16_f32 v38, v68, v69\n\t"
    "v_mfma_f32_16x16x32_bf16 v[80:83], a[92:95], a[60:63], v[80:83]\n\t"
    "ds_read_b128 a[92:95], v11 offset:16384\n\t"
    "v_cvt_pk_bf16_f32 v39, v70, v71\n\t"
    "v_mfma_f32_16x16x32_bf16 v[84:87], a[96:99], a[48:51], v[56:59]\n\t"
    "ds_read_b128 a[96:99], v8 offset:20480\n\t"
    "v_cvt_pk_bf16_f32 v40, v72, v73\n\t"
    "v_mfma_f32_16x16x32_bf16 v[84:87], a[100:103], a[52:55], v[84:87]\n\t"
    "ds_read_b128 a[100:103], v9 offset:20480\n\t"
    "v_cvt_pk_bf16_f32 v41, v74, v75\n\t"
    "v_mfma_f32_16x16x32_bf16 v[84:87], a[104:107], a[56:59], v[84:87]\n\t"
    "ds_read_b128 a[104:107], v10 offset:20480\n\t"
    "v_cvt_pk_bf16_f32 v42, v76, v77\n\t"
    "v_mfma_f32_16x16x32_bf16 v[84:87], a[108:111], a[60:63], v[84:87]\n\t"
    "ds_read_b128 a[108:111], v11 offset:20480\n\t"
    "v_cvt_pk_bf16_f32 v43, v78, v79\n\t"
    "v_mfma_f32_16x16x32_bf16 v[88:91], a[112:115], a[48:51], v[56:59]\n\t"
    "v_exp_f32 v80, v80\n\t"
    "v_mfma_f32_16x16x32_bf16 v[88:91], a[116:119], a[52:55], v[88:91]\n\t"
    "v_exp_f32 v81, v81\n\t"
    "v_mfma_f32_16x16x32_bf16 v[88:91], a[120:123], a[56:59], v[88:91]\n\t"
    "v_exp_f32 v82, v82\n\t"
    "v_mfma_f32_16x16x32_bf16 v[88:91], a[124:127], a[60:63], v[88:91]\n\t"
    "v_exp_f32 v83, v83\n\t"
    "v_mfma_f32_16x16x32_bf16 v[92:95], a[128:131], a[48:51], v[56:59]\n\t"
    "v_exp_f32 v84, v84\n\t"
    "v_mfma_f32_16x16x32_bf16 v[92:95], a[132:135], a[52:55], v[92:95]\n\t"
    "v_exp_f32 v85, v85\n\t"
    "v_mfma_f32_16x16x32_bf16 v[92:95], a[136:139], a[56:59], v[92:95]\n\t"
    "v_exp_f32 v86, v86\n\t"
    "v_mfma_f32_16x16x32_bf16 v[92:95], a[140:143], a[60:63], v[92:95]\n\t"
    "s_waitcnt lgkmcnt(0)\n\t"
    "v_exp_f32 v87, v87\n\t"
    "v_mfma_f32_16x16x32_bf16 v[64:67], a[80:83], a[64:67], v[60:63]\n\t"
    "v_exp_f32 v88, v88\n\t"
    "v_mfma_f32_16x16x32_bf16 v[64:67], a[84:87], a[68:71], v[64:67]\n\t"
    "v_exp_f32 v89, v89\n\t"
    "v_mfma_f32_16x16x32_bf16 v[64:67], a[88:91], a[72:75], v[64:67]\n\t"
    "v_exp_f32 v90, v90\n\t"
    "v_mfma_f32_16x16x32_bf16 v[64:67], a[92:95], a[76:79], v[64:67]\n\t"
    "v_exp_f32 v91, v91\n\t"
    "v_mfma_f32_16x16x32_bf16 v[68:71], a[80:83], v[224:227], v[252:255]\n\t"
    "v_exp_f32 v92, v92\n\t"
    "v_mfma_f32_16x16x32_bf16 v[68:71], a[84:87], v[228:231], v[68:71]\n\t"
    "v_exp_f32 v93, v93\n\t"
    "v_mfma_f32_16x16x32_bf16 v[68:71], a[88:91], v[232:235], v[68:71]\n\t"
    "v_exp_f32 v94, v94\n\t"
    "v_mfma_f32_16x16x32_bf16 v[68:71], a[92:95], v[236:239], v[68:71]\n\t"
    "v_exp_f32 v95, v95\n\t"
    "v_mfma_f32_16x16x32_bf16 v[72:75], a[80:83], a[228:231], v[32:35]\n\t"
    "v_exp_f32 v64, v64\n\t"
    "v_mfma_f32_16x16x32_bf16 v[72:75], a[84:87], v[240:243], v[72:75]\n\t"
    "v_exp_f32 v65, v65\n\t"
    "v_mfma_f32_16x16x32_bf16 v[72:75], a[88:91], a[232:235], v[72:75]\n\t"
    "v_exp_f32 v66, v66\n\t"
    "v_mfma_f32_16x16x32_bf16 v[72:75], a[92:95], a[236:239], v[72:75]\n\t"
    "v_exp_f32 v67, v67\n\t"
    "v_mfma_f32_16x16x32_bf16 v[160:163], v[36:39], a[144:147], v[160:163]\n\t"
    "v_exp_f32 v68, v68\n\t"
    "v_mfma_f32_16x16x32_bf16 v[160:163], v[40:43], a[148:151], v[160:163]\n\t"
    "v_exp_f32 v69, v69\n\t"
    "v_mfma_f32_16x16x32_bf16 v[164:167], v[36:39], a[152:155], v[164:167]\n\t"
    "v_exp_f32 v70, v70\n\t"
    "v_mfma_f32_16x16x32_bf16 v[164:167], v[40:43], a[156:159], v[164:167]\n\t"
    "v_exp_f32 v71, v71\n\t"
    "v_mfma_f32_16x16x32_bf16 v[168:171], v[36:39], a[160:163], v[168:171]\n\t"
    "ds_read_b32 v245, v246\n\t"
    "v_exp_f32 v72, v72\n\t"
    "v_mfma_f32_16x16x32_bf16 v[168:171], v[40:43], a[164:167], v[168:171]\n\t"
    "v_exp_f32 v73, v73\n\t"
    "v_mfma_f32_16x16x32_bf16 v[172:175], v[36:39], a[168:171], v[172:175]\n\t"
    "v_exp_f32 v74, v74\n\t"
    "v_mfma_f32_16x16x32_bf16 v[172:175], v[40:43], a[172:175], v[172:175]\n\t"
    "v_exp_f32 v75, v75\n\t"
    "v_mfma_f32_16x16x32_bf16 v[176:179], v[36:39], a[176:179], v[176:179]\n\t"
    "ds_read_b128 a[112:115], v8 offset:24576\n\t"
    "v_cvt_pk_bf16_f32 v64, v64, v65\n\t"
    "v_mfma_f32_16x16x32_bf16 v[176:179], v[40:43], a[180:183], v[176:179]\n\t"
    "ds_read_b128 a[116:119], v9 offset:24576\n\t"
    "v_cvt_pk_bf16_f32 v65, v66, v67\n\t"
    "s_waitcnt lgkmcnt(2)\n\t"
    "v_cmp_gt_u32 vcc, v244, v245\n\t"
    "v_mfma_f32_16x16x32_bf16 v[180:183], v[36:39], a[184:187], v[180:183]\n\t"
    "ds_read_b128 a[120:123], v10 offset:24576\n\t"
    "v_cvt_pk_bf16_f32 v68, v68, v69\n\t"
    "v_mfma_f32_16x16x32_bf16 v[180:183], v[40:43], a[188:191], v[180:183]\n\t"
    "ds_read_b128 a[124:127], v11 offset:24576\n\t"
    "v_cvt_pk_bf16_f32 v69, v70, v71\n\t"
    "v_mfma_f32_16x16x32_bf16 v[184:187], v[36:39], a[192:195], v[184:187]\n\t"
    "s_cbranch_vccnz SPIN4L0%=\n\t"
    "RET4L0%=:\n\t"
    "ds_read_b128 a[128:131], v8 offset:28672\n\t"
    "v_cvt_pk_bf16_f32 v72, v72, v73\n\t"
    "v_mfma_f32_16x16x32_bf16 v[184:187], v[40:43], a[196:199], v[184:187]\n\t"
    "ds_read_b128 a[132:135], v9 offset:28672\n\t"
    "v_cvt_pk_bf16_f32 v73, v74, v75\n\t"
    "v_mfma_f32_16x16x32_bf16 v[188:191], v[36:39], a[200:203], v[188:191]\n\t"
    "ds_read_b128 a[136:139], v10 offset:28672\n\t"
    "ds_write_b64 v27, v[64:65] offset:512\n\t"
    "v_mfma_f32_16x16x32_bf16 v[188:191], v[40:43], a[204:207], v[188:191]\n\t"
    "ds_read_b128 a[140:143], v11 offset:28672\n\t"
    "ds_write_b64 v27, v[68:69] offset:1536\n\t"
    "v_smfmac_f32_16x16x64_bf16 a[220:223], a[208:211], v[36:43], v24\n\t"
    "ds_write_b64 v27, v[72:73] offset:2560\n\t"
    "; TILE 5\n\t"
    "v_mfma_f32_16x16x32_bf16 v[64:67], a[80:83], a[0:3], v[44:47]\n\t"
    "s_sub_u32 s72, s58, 12288\n\t"
    "s_sub_u32 s73, s58, 8192\n\t"
    "s_sub_u32 s74, s58, 4096\n\t"
    "s_add_u32 m0, s59, 36864\n\t"
    "v_cvt_pk_bf16_f32 v36, v80, v81\n\t"
    "v_mfma_f32_16x16x32_bf16 v[64:67], a[84:87], a[4:7], v[64:67]\n\t"
    "buffer_load_dwordx4 v20, s[40:43], s72 offen offset:0 nt lds\n\t"
    "v_cvt_pk_bf16_f32 v37, v82, v83\n\t"
    "v_mfma_f32_16x16x32_bf16 v[64:67], a[88:91], a[8:11], v[64:67]\n\t"
    "buffer_load_dwordx4 v21, s[40:43], s72 offen offset:1024 nt lds\n\t"
    "v_cvt_pk_bf16_f32 v38, v84, v85\n\t"
    "v_mfma_f32_16x16x32_bf16 v[64:67], a[92:95], a[12:15], v[64:67]\n\t"
    "buffer_load_dwordx4 v22, s[40:43], s72 offen offset:2048 nt lds\n\t"
    "v_cvt_pk_bf16_f32 v39, v86, v87\n\t"
    "v_mfma_f32_16x16x32_bf16 v[68:71], a[96:99], a[0:3], v[44:47]\n\t"
    "buffer_load_dwordx4 v23, s[40:43], s72 offen offset:3072 nt lds\n\t"
    "s_add_u32 m0, s59, 40960\n\t"
    "v_cvt_pk_bf16_f32 v40, v88, v89\n\t"
    "v_mfma_f32_16x16x32_bf16 v[68:71], a[100:103], a[4:7], v[68:71]\n\t"
    "buffer_load_dwordx4 v20, s[40:43], s73 offen offset:0 nt lds\n\t"
    "v_cvt_pk_bf16_f32 v41, v90, v91\n\t"
    "v_mfma_f32_16x16x32_bf16 v[68:71], a[104:107], a[8:11], v[68:71]\n\t"
    "buffer_load_dwordx4 v21, s[40:43], s73 offen offset:1024 nt lds\n\t"
    "v_cvt_pk_bf16_f32 v42, v92, v93\n\t"
    "v_mfma_f32_16x16x32_bf16 v[68:71], a[108:111], a[12:15], v[68:71]\n\t"
    "s_waitcnt lgkmcnt(0)\n\t"
    "buffer_load_dwordx4 v22, s[40:43], s73 offen offset:2048 nt lds\n\t"
    "v_cvt_pk_bf16_f32 v43, v94, v95\n\t"
    "v_mfma_f32_16x16x32_bf16 v[72:75], a[112:115], a[0:3], v[44:47]\n\t"
    "buffer_load_dwordx4 v23, s[40:43], s73 offen offset:3072 nt lds\n\t"
    "v_exp_f32 v64, v64\n\t"
    "v_mfma_f32_16x16x32_bf16 v[72:75], a[116:119], a[4:7], v[72:75]\n\t"
    "v_exp_f32 v65, v65\n\t"
    "v_mfma_f32_16x16x32_bf16 v[72:75], a[120:123], a[8:11], v[72:75]\n\t"
    "v_exp_f32 v66, v66\n\t"
    "v_mfma_f32_16x16x32_bf16 v[72:75], a[124:127], a[12:15], v[72:75]\n\t"
    "v_exp_f32 v67, v67\n\t"
    "v_mfma_f32_16x16x32_bf16 v[76:79], a[128:131], a[0:3], v[44:47]\n\t"
    "v_exp_f32 v68, v68\n\t"
    "v_mfma_f32_16x16x32_bf16 v[76:79], a[132:135], a[4:7], v[76:79]\n\t"
    "v_exp_f32 v69, v69\n\t"
    "v_mfma_f32_16x16x32_bf16 v[76:79], a[136:139], a[8:11], v[76:79]\n\t"
    "v_exp_f32 v70, v70\n\t"
    "v_mfma_f32_16x16x32_bf16 v[76:79], a[140:143], a[12:15], v[76:79]\n\t"
    "v_exp_f32 v71, v71\n\t"
    "v_mfma_f32_16x16x32_bf16 v[192:195], v[36:39], a[144:147], v[192:195]\n\t"
    "v_exp_f32 v72, v72\n\t"
    "v_mfma_f32_16x16x32_bf16 v[192:195], v[40:43], a[148:151], v[192:195]\n\t"
    "v_exp_f32 v73, v73\n\t"
    "v_mfma_f32_16x16x32_bf16 v[196:199], v[36:39], a[152:155], v[196:199]\n\t"
    "v_exp_f32 v74, v74\n\t"
    "v_mfma_f32_16x16x32_bf16 v[196:199], v[40:43], a[156:159], v[196:199]\n\t"
    "v_exp_f32 v75, v75\n\t"
    "v_mfma_f32_16x16x32_bf16 v[200:203], v[36:39], a[160:163], v[200:203]\n\t"
    "v_exp_f32 v76, v76\n\t"
    "v_mfma_f32_16x16x32_bf16 v[200:203], v[40:43], a[164:167], v[200:203]\n\t"
    "v_exp_f32 v77, v77\n\t"
    "v_mfma_f32_16x16x32_bf16 v[204:207], v[36:39], a[168:171], v[204:207]\n\t"
    "v_exp_f32 v78, v78\n\t"
    "v_mfma_f32_16x16x32_bf16 v[204:207], v[40:43], a[172:175], v[204:207]\n\t"
    "s_add_u32 m0, s59, 45056\n\t"
    "v_exp_f32 v79, v79\n\t"
    "v_mfma_f32_16x16x32_bf16 v[208:211], v[36:39], a[176:179], v[208:211]\n\t"
    "ds_read_b64_tr_b16 a[144:145], v12 offset:16384\n\t"
    "ds_read_b64_tr_b16 a[146:147], v12 offset:20480\n\t"
    "buffer_load_dwordx4 v20, s[40:43], s74 offen offset:0 nt lds\n\t"
    "v_mfma_f32_16x16x32_bf16 v[208:211], v[40:43], a[180:183], v[208:211]\n\t"
    "ds_read_b64_tr_b16 a[148:149], v12 offset:24576\n\t"
    "ds_read_b64_tr_b16 a[150:151], v12 offset:28672\n\t"
    "v_mfma_f32_16x16x32_bf16 v[212:215], v[36:39], a[184:187], v[212:215]\n\t"
    "ds_read_b64_tr_b16 a[152:153], v13 offset:16384\n\t"
    "ds_read_b64_tr_b16 a[154:155], v13 offset:20480\n\t"
    "buffer_load_dwordx4 v21, s[40:43], s74 offen offset:1024 nt lds\n\t"
    "v_mfma_f32_16x16x32_bf16 v[212:215], v[40:43], a[188:191], v[212:215]\n\t"
    "ds_read_b64_tr_b16 a[156:157], v13 offset:24576\n\t"
    "ds_read_b64_tr_b16 a[158:159], v13 offset:28672\n\t"
    "v_mfma_f32_16x16x32_bf16 v[216:219], v[36:39], a[192:195], v[216:219]\n\t"
    "ds_read_b64_tr_b16 a[160:161], v14 offset:16384\n\t"
    "ds_read_b64_tr_b16 a[162:163], v14 offset:20480\n\t"
    "buffer_load_dwordx4 v22, s[40:43], s74 offen offset:2048 nt lds\n\t"
    "v_mfma_f32_16x16x32_bf16 v[216:219], v[40:43], a[196:199], v[216:219]\n\t"
    "ds_read_b64_tr_b16 a[164:165], v14 offset:24576\n\t"
    "ds_read_b64_tr_b16 a[166:167], v14 offset:28672\n\t"
    "v_mfma_f32_16x16x32_bf16 v[220:223], v[36:39], a[200:203], v[220:223]\n\t"
    "ds_read_b64_tr_b16 a[168:169], v15 offset:16384\n\t"
    "ds_read_b64_tr_b16 a[170:171], v15 offset:20480\n\t"
    "buffer_load_dwordx4 v23, s[40:43], s74 offen offset:3072 nt lds\n\t"
    "v_mfma_f32_16x16x32_bf16 v[220:223], v[40:43], a[204:207], v[220:223]\n\t"
    "s_add_u32 m0, s59, 49152\n\t"
    "ds_read_b64_tr_b16 a[172:173], v15 offset:24576\n\t"
    "ds_read_b64_tr_b16 a[174:175], v15 offset:28672\n\t"
    "v_smfmac_f32_16x16x64_bf16 a[224:227], a[208:211], v[36:43], v24\n\t"
    "buffer_load_dwordx4 v20, s[40:43], s58 offen offset:0 nt lds\n\t"
    "v_mfma_f32_16x16x32_bf16 v[80:83], a[80:83], a[16:19], v[48:51]\n\t"
    "ds_read_b64_tr_b16 a[176:177], v16 offset:16384\n\t"
    "v_cvt_pk_bf16_f32 v36, v64, v65\n\t"
    "v_mfma_f32_16x16x32_bf16 v[80:83], a[84:87], a[20:23], v[80:83]\n\t"
    "ds_read_b64_tr_b16 a[178:179], v16 offset:20480\n\t"
    "buffer_load_dwordx4 v21, s[40:43], s58 offen offset:1024 nt lds\n\t"
    "v_cvt_pk_bf16_f32 v37, v66, v67\n\t"
    "v_mfma_f32_16x16x32_bf16 v[80:83], a[88:91], a[24:27], v[80:83]\n\t"
    "ds_read_b64_tr_b16 a[180:181], v16 offset:24576\n\t"
    "v_cvt_pk_bf16_f32 v38, v68, v69\n\t"
    "v_mfma_f32_16x16x32_bf16 v[80:83], a[92:95], a[28:31], v[80:83]\n\t"
    "ds_read_b64_tr_b16 a[182:183], v16 offset:28672\n\t"
    "buffer_load_dwordx4 v22, s[40:43], s58 offen offset:2048 nt lds\n\t"
    "v_cvt_pk_bf16_f32 v39, v70, v71\n\t"
    "v_mfma_f32_16x16x32_bf16 v[84:87], a[96:99], a[16:19], v[48:51]\n\t"
    "ds_read_b64_tr_b16 a[184:185], v17 offset:16384\n\t"
    "v_cvt_pk_bf16_f32 v40, v72, v73\n\t"
    "v_mfma_f32_16x16x32_bf16 v[84:87], a[100:103], a[20:23], v[84:87]\n\t"
    "ds_read_b64_tr_b16 a[186:187], v17 offset:20480\n\t"
    "buffer_load_dwordx4 v23, s[40:43], s58 offen offset:3072 nt lds\n\t"
    "v_cvt_pk_bf16_f32 v41, v74, v75\n\t"
    "v_mfma_f32_16x16x32_bf16 v[84:87], a[104:107], a[24:27], v[84:87]\n\t"
    "s_add_u32 m0, s60, 36864\n\t"
    "ds_read_b64_tr_b16 a[188:189], v17 offset:24576\n\t"
    "v_cvt_pk_bf16_f32 v42, v76, v77\n\t"
    "v_add_u32 v244, 1, v244\n\t"
    "v_mfma_f32_16x16x32_bf16 v[84:87], a[108:111], a[28:31], v[84:87]\n\t"
    "s_waitcnt vmcnt(16)\n\t"
    "ds_write_b32 v27, v244 offset:3072\n\t"
    "ds_read_b64_tr_b16 a[190:191], v17 offset:28672\n\t"
    "buffer_load_dwordx4 v20, s[44:47], s72 offen offset:0 nt lds\n\t"
    "v_cvt_pk_bf16_f32 v43, v78, v79\n\t"
    "v_mfma_f32_16x16x32_bf16 v[88:91], a[112:115], a[16:19], v[48:51]\n\t"
    "ds_write_b32 v27, v244 offset:4096\n\t"
    "v_exp_f32 v80, v80\n\t"
    "v_mfma_f32_16x16x32_bf16 v[88:91], a[116:119], a[20:23], v[88:91]\n\t"
    "ds_write_b32 v27, v244 offset:5120\n\t"
    "v_exp_f32 v81, v81\n\t"
    "v_mfma_f32_16x16x32_bf16 v[88:91], a[120:123], a[24:27], v[88:91]\n\t"
    "v_exp_f32 v82, v82\n\t"
    "v_mfma_f32_16x16x32_bf16 v[88:91], a[124:127], a[28:31], v[88:91]\n\t"
    "v_exp_f32 v83, v83\n\t"
    "v_mfma_f32_16x16x32_bf16 v[92:95], a[128:131], a[16:19], v[48:51]\n\t"
    "v_exp_f32 v84, v84\n\t"
    "v_mfma_f32_16x16x32_bf16 v[92:95], a[132:135], a[20:23], v[92:95]\n\t"
    "v_exp_f32 v85, v85\n\t"
    "v_mfma_f32_16x16x32_bf16 v[92:95], a[136:139], a[24:27], v[92:95]\n\t"
    "v_exp_f32 v86, v86\n\t"
    "v_mfma_f32_16x16x32_bf16 v[92:95], a[140:143], a[28:31], v[92:95]\n\t"
    "s_waitcnt lgkmcnt(2)\n\t"
    "v_exp_f32 v87, v87\n\t"
    "v_mfma_f32_16x16x32_bf16 v[96:99], v[36:39], a[144:147], v[96:99]\n\t"
    "v_exp_f32 v88, v88\n\t"
    "v_mfma_f32_16x16x32_bf16 v[96:99], v[40:43], a[148:151], v[96:99]\n\t"
    "v_exp_f32 v89, v89\n\t"
    "v_mfma_f32_16x16x32_bf16 v[100:103], v[36:39], a[152:155], v[100:103]\n\t"
    "v_exp_f32 v90, v90\n\t"
    "v_mfma_f32_16x16x32_bf16 v[100:103], v[40:43], a[156:159], v[100:103]\n\t"
    "v_exp_f32 v91, v91\n\t"
    "v_mfma_f32_16x16x32_bf16 v[104:107], v[36:39], a[160:163], v[104:107]\n\t"
    "v_exp_f32 v92, v92\n\t"
    "v_mfma_f32_16x16x32_bf16 v[104:107], v[40:43], a[164:167], v[104:107]\n\t"
    "v_exp_f32 v93, v93\n\t"
    "v_mfma_f32_16x16x32_bf16 v[108:111], v[36:39], a[168:171], v[108:111]\n\t"
    "v_exp_f32 v94, v94\n\t"
    "v_mfma_f32_16x16x32_bf16 v[108:111], v[40:43], a[172:175], v[108:111]\n\t"
    "v_exp_f32 v95, v95\n\t"
    "v_mfma_f32_16x16x32_bf16 v[112:115], v[36:39], a[176:179], v[112:115]\n\t"
    "buffer_load_dwordx4 v22, s[44:47], s72 offen offset:1024 nt lds\n\t"
    "v_mfma_f32_16x16x32_bf16 v[112:115], v[40:43], a[180:183], v[112:115]\n\t"
    "buffer_load_dwordx4 v20, s[44:47], s72 offen offset:2048 nt lds\n\t"
    "v_mfma_f32_16x16x32_bf16 v[116:119], v[36:39], a[184:187], v[116:119]\n\t"
    "buffer_load_dwordx4 v22, s[44:47], s72 offen offset:3072 nt lds\n\t"
    "s_add_u32 m0, s60, 40960\n\t"
    "v_mfma_f32_16x16x32_bf16 v[116:119], v[40:43], a[188:191], v[116:119]\n\t"
    "buffer_load_dwordx4 v20, s[44:47], s73 offen offset:0 nt lds\n\t"
    "v_mfma_f32_16x16x32_bf16 v[120:123], v[36:39], a[240:243], v[120:123]\n\t"
    "buffer_load_dwordx4 v22, s[44:47], s73 offen offset:1024 nt lds\n\t"
    "v_mfma_f32_16x16x32_bf16 v[120:123], v[40:43], a[244:247], v[120:123]\n\t"
    "buffer_load_dwordx4 v20, s[44:47], s73 offen offset:2048 nt lds\n\t"
    "v_mfma_f32_16x16x32_bf16 v[124:127], v[36:39], a[248:251], v[124:127]\n\t"
    "buffer_load_dwordx4 v22, s[44:47], s73 offen offset:3072 nt lds\n\t"
    "s_add_u32 m0, s60, 45056\n\t"
    "v_mfma_f32_16x16x32_bf16 v[124:127], v[40:43], a[252:255], v[124:127]\n\t"
    "buffer_load_dwordx4 v20, s[44:47], s74 offen offset:0 nt lds\n\t"
    "v_smfmac_f32_16x16x64_bf16 a[212:215], a[208:211], v[36:43], v24\n\t"
    "v_mfma_f32_16x16x32_bf16 v[64:67], a[80:83], a[32:35], v[52:55]\n\t"
    "ds_read_b64_tr_b16 a[192:193], v18 offset:32768\n\t"
    "v_cvt_pk_bf16_f32 v36, v80, v81\n\t"
    "v_mfma_f32_16x16x32_bf16 v[64:67], a[84:87], a[36:39], v[64:67]\n\t"
    "ds_read_b64_tr_b16 a[194:195], v18 offset:36864\n\t"
    "v_cvt_pk_bf16_f32 v37, v82, v83\n\t"
    "v_mfma_f32_16x16x32_bf16 v[64:67], a[88:91], a[40:43], v[64:67]\n\t"
    "ds_read_b64_tr_b16 a[196:197], v18 offset:40960\n\t"
    "v_cvt_pk_bf16_f32 v38, v84, v85\n\t"
    "v_mfma_f32_16x16x32_bf16 v[64:67], a[92:95], a[44:47], v[64:67]\n\t"
    "ds_read_b64_tr_b16 a[198:199], v18 offset:45056\n\t"
    "v_cvt_pk_bf16_f32 v39, v86, v87\n\t"
    "v_mfma_f32_16x16x32_bf16 v[68:71], a[96:99], a[32:35], v[52:55]\n\t"
    "ds_read_b64_tr_b16 a[200:201], v19 offset:32768\n\t"
    "v_cvt_pk_bf16_f32 v40, v88, v89\n\t"
    "v_mfma_f32_16x16x32_bf16 v[68:71], a[100:103], a[36:39], v[68:71]\n\t"
    "ds_read_b64_tr_b16 a[202:203], v19 offset:36864\n\t"
    "v_cvt_pk_bf16_f32 v41, v90, v91\n\t"
    "v_mfma_f32_16x16x32_bf16 v[68:71], a[104:107], a[40:43], v[68:71]\n\t"
    "ds_read_b64_tr_b16 a[204:205], v19 offset:40960\n\t"
    "v_cvt_pk_bf16_f32 v42, v92, v93\n\t"
    "v_mfma_f32_16x16x32_bf16 v[68:71], a[108:111], a[44:47], v[68:71]\n\t"
    "ds_read_b64_tr_b16 a[206:207], v19 offset:45056\n\t"
    "v_cvt_pk_bf16_f32 v43, v94, v95\n\t"
    "v_mfma_f32_16x16x32_bf16 v[72:75], a[112:115], a[32:35], v[52:55]\n\t"
    "v_exp_f32 v64, v64\n\t"
    "v_mfma_f32_16x16x32_bf16 v[72:75], a[116:119], a[36:39], v[72:75]\n\t"
    "v_exp_f32 v65, v65\n\t"
    "v_mfma_f32_16x16x32_bf16 v[72:75], a[120:123], a[40:43], v[72:75]\n\t"
    "v_exp_f32 v66, v66\n\t"
    "v_mfma_f32_16x16x32_bf16 v[72:75], a[124:127], a[44:47], v[72:75]\n\t"
    "v_exp_f32 v67, v67\n\t"
    "v_mfma_f32_16x16x32_bf16 v[76:79], a[128:131], a[32:35], v[52:55]\n\t"
    "v_exp_f32 v68, v68\n\t"
    "v_mfma_f32_16x16x32_bf16 v[76:79], a[132:135], a[36:39], v[76:79]\n\t"
    "v_exp_f32 v69, v69\n\t"
    "v_mfma_f32_16x16x32_bf16 v[76:79], a[136:139], a[40:43], v[76:79]\n\t"
    "v_exp_f32 v70, v70\n\t"
    "v_mfma_f32_16x16x32_bf16 v[76:79], a[140:143], a[44:47], v[76:79]\n\t"
    "v_exp_f32 v71, v71\n\t"
    "v_mfma_f32_16x16x32_bf16 v[128:131], v[36:39], a[144:147], v[128:131]\n\t"
    "v_exp_f32 v72, v72\n\t"
    "v_mfma_f32_16x16x32_bf16 v[128:131], v[40:43], a[148:151], v[128:131]\n\t"
    "v_exp_f32 v73, v73\n\t"
    "v_mfma_f32_16x16x32_bf16 v[132:135], v[36:39], a[152:155], v[132:135]\n\t"
    "v_exp_f32 v74, v74\n\t"
    "v_mfma_f32_16x16x32_bf16 v[132:135], v[40:43], a[156:159], v[132:135]\n\t"
    "v_exp_f32 v75, v75\n\t"
    "v_mfma_f32_16x16x32_bf16 v[136:139], v[36:39], a[160:163], v[136:139]\n\t"
    "v_exp_f32 v76, v76\n\t"
    "v_mfma_f32_16x16x32_bf16 v[136:139], v[40:43], a[164:167], v[136:139]\n\t"
    "v_exp_f32 v77, v77\n\t"
    "v_mfma_f32_16x16x32_bf16 v[140:143], v[36:39], a[168:171], v[140:143]\n\t"
    "v_exp_f32 v78, v78\n\t"
    "v_mfma_f32_16x16x32_bf16 v[140:143], v[40:43], a[172:175], v[140:143]\n\t"
    "v_exp_f32 v79, v79\n\t"
    "v_mfma_f32_16x16x32_bf16 v[144:147], v[36:39], a[176:179], v[144:147]\n\t"
    "buffer_load_dwordx4 v22, s[44:47], s74 offen offset:1024 nt lds\n\t"
    "v_mfma_f32_16x16x32_bf16 v[144:147], v[40:43], a[180:183], v[144:147]\n\t"
    "buffer_load_dwordx4 v20, s[44:47], s74 offen offset:2048 nt lds\n\t"
    "v_mfma_f32_16x16x32_bf16 v[148:151], v[36:39], a[184:187], v[148:151]\n\t"
    "buffer_load_dwordx4 v22, s[44:47], s74 offen offset:3072 nt lds\n\t"
    "s_add_u32 m0, s60, 49152\n\t"
    "v_mfma_f32_16x16x32_bf16 v[148:151], v[40:43], a[188:191], v[148:151]\n\t"
    "buffer_load_dwordx4 v20, s[44:47], s58 offen offset:0 nt lds\n\t"
    "v_mfma_f32_16x16x32_bf16 v[152:155], v[36:39], a[240:243], v[152:155]\n\t"
    "buffer_load_dwordx4 v22, s[44:47], s58 offen offset:1024 nt lds\n\t"
    "v_mfma_f32_16x16x32_bf16 v[152:155], v[40:43], a[244:247], v[152:155]\n\t"
    "buffer_load_dwordx4 v20, s[44:47], s58 offen offset:2048 nt lds\n\t"
    "v_mfma_f32_16x16x32_bf16 v[156:159], v[36:39], a[248:251], v[156:159]\n\t"
    "buffer_load_dwordx4 v22, s[44:47], s58 offen offset:3072 nt lds\n\t"
    "v_mfma_f32_16x16x32_bf16 v[156:159], v[40:43], a[252:255], v[156:159]\n\t"
    "s_add_u32 s58, s58, 16384\n\t"
    "s_and_b32 s58, s58, 0x7ffff\n\t"
    "v_smfmac_f32_16x16x64_bf16 a[216:219], a[208:211], v[36:43], v24\n\t"
    "v_mfma_f32_16x16x32_bf16 v[80:83], a[80:83], a[48:51], v[56:59]\n\t"
    "ds_read_b128 a[80:83], v8 offset:32768\n\t"
    "v_cvt_pk_bf16_f32 v36, v64, v65\n\t"
    "v_mfma_f32_16x16x32_bf16 v[80:83], a[84:87], a[52:55], v[80:83]\n\t"
    "ds_read_b128 a[84:87], v9 offset:32768\n\t"
    "v_cvt_pk_bf16_f32 v37, v66, v67\n\t"
    "v_mfma_f32_16x16x32_bf16 v[80:83], a[88:91], a[56:59], v[80:83]\n\t"
    "ds_read_b128 a[88:91], v10 offset:32768\n\t"
    "v_cvt_pk_bf16_f32 v38, v68, v69\n\t"
    "v_mfma_f32_16x16x32_bf16 v[80:83], a[92:95], a[60:63], v[80:83]\n\t"
    "ds_read_b128 a[92:95], v11 offset:32768\n\t"
    "v_cvt_pk_bf16_f32 v39, v70, v71\n\t"
    "v_mfma_f32_16x16x32_bf16 v[84:87], a[96:99], a[48:51], v[56:59]\n\t"
    "ds_read_b128 a[96:99], v8 offset:36864\n\t"
    "v_cvt_pk_bf16_f32 v40, v72, v73\n\t"
    "v_mfma_f32_16x16x32_bf16 v[84:87], a[100:103], a[52:55], v[84:87]\n\t"
    "ds_read_b128 a[100:103], v9 offset:36864\n\t"
    "v_cvt_pk_bf16_f32 v41, v74, v75\n\t"
    "v_mfma_f32_16x16x32_bf16 v[84:87], a[104:107], a[56:59], v[84:87]\n\t"
    "ds_read_b128 a[104:107], v10 offset:36864\n\t"
    "v_cvt_pk_bf16_f32 v42, v76, v77\n\t"
    "v_mfma_f32_16x16x32_bf16 v[84:87], a[108:111], a[60:63], v[84:87]\n\t"
    "ds_read_b128 a[108:111], v11 offset:36864\n\t"
    "v_cvt_pk_bf16_f32 v43, v78, v79\n\t"
    "v_mfma_f32_16x16x32_bf16 v[88:91], a[112:115], a[48:51], v[56:59]\n\t"
    "v_exp_f32 v80, v80\n\t"
    "v_mfma_f32_16x16x32_bf16 v[88:91], a[116:119], a[52:55], v[88:91]\n\t"
    "v_exp_f32 v81, v81\n\t"
    "v_mfma_f32_16x16x32_bf16 v[88:91], a[120:123], a[56:59], v[88:91]\n\t"
    "v_exp_f32 v82, v82\n\t"
    "v_mfma_f32_16x16x32_bf16 v[88:91], a[124:127], a[60:63], v[88:91]\n\t"
    "v_exp_f32 v83, v83\n\t"
    "v_mfma_f32_16x16x32_bf16 v[92:95], a[128:131], a[48:51], v[56:59]\n\t"
    "v_exp_f32 v84, v84\n\t"
    "v_mfma_f32_16x16x32_bf16 v[92:95], a[132:135], a[52:55], v[92:95]\n\t"
    "v_exp_f32 v85, v85\n\t"
    "v_mfma_f32_16x16x32_bf16 v[92:95], a[136:139], a[56:59], v[92:95]\n\t"
    "v_exp_f32 v86, v86\n\t"
    "v_mfma_f32_16x16x32_bf16 v[92:95], a[140:143], a[60:63], v[92:95]\n\t"
    "s_waitcnt lgkmcnt(0)\n\t"
    "v_exp_f32 v87, v87\n\t"
    "v_mfma_f32_16x16x32_bf16 v[64:67], a[80:83], a[64:67], v[60:63]\n\t"
    "v_exp_f32 v88, v88\n\t"
    "v_mfma_f32_16x16x32_bf16 v[64:67], a[84:87], a[68:71], v[64:67]\n\t"
    "v_exp_f32 v89, v89\n\t"
    "v_mfma_f32_16x16x32_bf16 v[64:67], a[88:91], a[72:75], v[64:67]\n\t"
    "v_exp_f32 v90, v90\n\t"
    "v_mfma_f32_16x16x32_bf16 v[64:67], a[92:95], a[76:79], v[64:67]\n\t"
    "v_exp_f32 v91, v91\n\t"
    "v_mfma_f32_16x16x32_bf16 v[68:71], a[80:83], v[224:227], v[252:255]\n\t"
    "v_exp_f32 v92, v92\n\t"
    "v_mfma_f32_16x16x32_bf16 v[68:71], a[84:87], v[228:231], v[68:71]\n\t"
    "v_exp_f32 v93, v93\n\t"
    "v_mfma_f32_16x16x32_bf16 v[68:71], a[88:91], v[232:235], v[68:71]\n\t"
    "v_exp_f32 v94, v94\n\t"
    "v_mfma_f32_16x16x32_bf16 v[68:71], a[92:95], v[236:239], v[68:71]\n\t"
    "v_exp_f32 v95, v95\n\t"
    "v_mfma_f32_16x16x32_bf16 v[72:75], a[80:83], a[228:231], v[32:35]\n\t"
    "v_exp_f32 v64, v64\n\t"
    "v_mfma_f32_16x16x32_bf16 v[72:75], a[84:87], v[240:243], v[72:75]\n\t"
    "v_exp_f32 v65, v65\n\t"
    "v_mfma_f32_16x16x32_bf16 v[72:75], a[88:91], a[232:235], v[72:75]\n\t"
    "v_exp_f32 v66, v66\n\t"
    "v_mfma_f32_16x16x32_bf16 v[72:75], a[92:95], a[236:239], v[72:75]\n\t"
    "v_exp_f32 v67, v67\n\t"
    "v_mfma_f32_16x16x32_bf16 v[160:163], v[36:39], a[144:147], v[160:163]\n\t"
    "v_exp_f32 v68, v68\n\t"
    "v_mfma_f32_16x16x32_bf16 v[160:163], v[40:43], a[148:151], v[160:163]\n\t"
    "v_exp_f32 v69, v69\n\t"
    "v_mfma_f32_16x16x32_bf16 v[164:167], v[36:39], a[152:155], v[164:167]\n\t"
    "v_exp_f32 v70, v70\n\t"
    "v_mfma_f32_16x16x32_bf16 v[164:167], v[40:43], a[156:159], v[164:167]\n\t"
    "v_exp_f32 v71, v71\n\t"
    "v_mfma_f32_16x16x32_bf16 v[168:171], v[36:39], a[160:163], v[168:171]\n\t"
    "ds_read_b32 v245, v246\n\t"
    "v_exp_f32 v72, v72\n\t"
    "v_mfma_f32_16x16x32_bf16 v[168:171], v[40:43], a[164:167], v[168:171]\n\t"
    "v_exp_f32 v73, v73\n\t"
    "v_mfma_f32_16x16x32_bf16 v[172:175], v[36:39], a[168:171], v[172:175]\n\t"
    "v_exp_f32 v74, v74\n\t"
    "v_mfma_f32_16x16x32_bf16 v[172:175], v[40:43], a[172:175], v[172:175]\n\t"
    "v_exp_f32 v75, v75\n\t"
    "v_mfma_f32_16x16x32_bf16 v[176:179], v[36:39], a[176:179], v[176:179]\n\t"
    "ds_read_b128 a[112:115], v8 offset:40960\n\t"
    "v_cvt_pk_bf16_f32 v64, v64, v65\n\t"
    "v_mfma_f32_16x16x32_bf16 v[176:179], v[40:43], a[180:183], v[176:179]\n\t"
    "ds_read_b128 a[116:119], v9 offset:40960\n\t"
    "v_cvt_pk_bf16_f32 v65, v66, v67\n\t"
    "s_waitcnt lgkmcnt(2)\n\t"
    "v_cmp_gt_u32 vcc, v244, v245\n\t"
    "v_mfma_f32_16x16x32_bf16 v[180:183], v[36:39], a[184:187], v[180:183]\n\t"
    "ds_read_b128 a[120:123], v10 offset:40960\n\t"
    "v_cvt_pk_bf16_f32 v68, v68, v69\n\t"
    "v_mfma_f32_16x16x32_bf16 v[180:183], v[40:43], a[188:191], v[180:183]\n\t"
    "ds_read_b128 a[124:127], v11 offset:40960\n\t"
    "v_cvt_pk_bf16_f32 v69, v70, v71\n\t"
    "v_mfma_f32_16x16x32_bf16 v[184:187], v[36:39], a[240:243], v[184:187]\n\t"
    "s_cbranch_vccnz SPIN4L1%=\n\t"
    "RET4L1%=:\n\t"
    "ds_read_b128 a[128:131], v8 offset:45056\n\t"
    "v_cvt_pk_bf16_f32 v72, v72, v73\n\t"
    "v_mfma_f32_16x16x32_bf16 v[184:187], v[40:43], a[244:247], v[184:187]\n\t"
    "ds_read_b128 a[132:135], v9 offset:45056\n\t"
    "v_cvt_pk_bf16_f32 v73, v74, v75\n\t"
    "v_mfma_f32_16x16x32_bf16 v[188:191], v[36:39], a[248:251], v[188:191]\n\t"
    "ds_read_b128 a[136:139], v10 offset:45056\n\t"
    "ds_write_b64 v27, v[64:65] offset:0\n\t"
    "v_mfma_f32_16x16x32_bf16 v[188:191], v[40:43], a[252:255], v[188:191]\n\t"
    "ds_read_b128 a[140:143], v11 offset:45056\n\t"
    "ds_write_b64 v27, v[68:69] offset:1024\n\t"
    "v_smfmac_f32_16x16x64_bf16 a[220:223], a[208:211], v[36:43], v24\n\t"
    "ds_write_b64 v27, v[72:73] offset:2048\n\t"
    "s_sub_u32 s57, s57, 1\n\t"
    "s_cmp_lg_u32 s57, 0\n\t"
    "s_cbranch_scc1 LOOPB%=\n\t"
    "; TILE 30\n\t"
    "v_mfma_f32_16x16x32_bf16 v[64:67], a[80:83], a[0:3], v[44:47]\n\t"
    "v_cvt_pk_bf16_f32 v36, v80, v81\n\t"
    "v_mfma_f32_16x16x32_bf16 v[64:67], a[84:87], a[4:7], v[64:67]\n\t"
    "v_cvt_pk_bf16_f32 v37, v82, v83\n\t"
    "v_mfma_f32_16x16x32_bf16 v[64:67], a[88:91], a[8:11], v[64:67]\n\t"
    "v_cvt_pk_bf16_f32 v38, v84, v85\n\t"
    "v_mfma_f32_16x16x32_bf16 v[64:67], a[92:95], a[12:15], v[64:67]\n\t"
    "v_cvt_pk_bf16_f32 v39, v86, v87\n\t"
    "v_mfma_f32_16x16x32_bf16 v[68:71], a[96:99], a[0:3], v[44:47]\n\t"
    "v_cvt_pk_bf16_f32 v40, v88, v89\n\t"
    "v_mfma_f32_16x16x32_bf16 v[68:71], a[100:103], a[4:7], v[68:71]\n\t"
    "v_cvt_pk_bf16_f32 v41, v90, v91\n\t"
    "v_mfma_f32_16x16x32_bf16 v[68:71], a[104:107], a[8:11], v[68:71]\n\t"
    "v_cvt_pk_bf16_f32 v42, v92, v93\n\t"
    "v_mfma_f32_16x16x32_bf16 v[68:71], a[108:111], a[12:15], v[68:71]\n\t"
    "s_waitcnt lgkmcnt(0)\n\t"
    "v_cvt_pk_bf16_f32 v43, v94, v95\n\t"
    "v_mfma_f32_16x16x32_bf16 v[72:75], a[112:115], a[0:3], v[44:47]\n\t"
    "v_exp_f32 v64, v64\n\t"
    "v_mfma_f32_16x16x32_bf16 v[72:75], a[116:119], a[4:7], v[72:75]\n\t"
    "v_exp_f32 v65, v65\n\t"
    "v_mfma_f32_16x16x32_bf16 v[72:75], a[120:123], a[8:11], v[72:75]\n\t"
    "v_exp_f32 v66, v66\n\t"
    "v_mfma_f32_16x16x32_bf16 v[72:75], a[124:127], a[12:15], v[72:75]\n\t"
    "v_exp_f32 v67, v67\n\t"
    "v_mfma_f32_16x16x32_bf16 v[76:79], a[128:131], a[0:3], v[44:47]\n\t"
    "v_exp_f32 v68, v68\n\t"
    "v_mfma_f32_16x16x32_bf16 v[76:79], a[132:135], a[4:7], v[76:79]\n\t"
    "v_exp_f32 v69, v69\n\t"
    "v_mfma_f32_16x16x32_bf16 v[76:79], a[136:139], a[8:11], v[76:79]\n\t"
    "v_exp_f32 v70, v70\n\t"
    "v_mfma_f32_16x16x32_bf16 v[76:79], a[140:143], a[12:15], v[76:79]\n\t"
    "v_exp_f32 v71, v71\n\t"
    "v_mfma_f32_16x16x32_bf16 v[192:195], v[36:39], a[144:147], v[192:195]\n\t"
    "v_exp_f32 v72, v72\n\t"
    "v_mfma_f32_16x16x32_bf16 v[192:195], v[40:43], a[148:151], v[192:195]\n\t"
    "v_exp_f32 v73, v73\n\t"
    "v_mfma_f32_16x16x32_bf16 v[196:199], v[36:39], a[152:155], v[196:199]\n\t"
    "v_exp_f32 v74, v74\n\t"
    "v_mfma_f32_16x16x32_bf16 v[196:199], v[40:43], a[156:159], v[196:199]\n\t"
    "v_exp_f32 v75, v75\n\t"
    "v_mfma_f32_16x16x32_bf16 v[200:203], v[36:39], a[160:163], v[200:203]\n\t"
    "v_exp_f32 v76, v76\n\t"
    "v_mfma_f32_16x16x32_bf16 v[200:203], v[40:43], a[164:167], v[200:203]\n\t"
    "v_exp_f32 v77, v77\n\t"
    "v_mfma_f32_16x16x32_bf16 v[204:207], v[36:39], a[168:171], v[204:207]\n\t"
    "v_exp_f32 v78, v78\n\t"
    "v_mfma_f32_16x16x32_bf16 v[204:207], v[40:43], a[172:175], v[204:207]\n\t"
    "v_exp_f32 v79, v79\n\t"
    "v_mfma_f32_16x16x32_bf16 v[208:211], v[36:39], a[176:179], v[208:211]\n\t"
    "ds_read_b64_tr_b16 a[144:145], v12 offset:32768\n\t"
    "ds_read_b64_tr_b16 a[146:147], v12 offset:36864\n\t"
    "v_mfma_f32_16x16x32_bf16 v[208:211], v[40:43], a[180:183], v[208:211]\n\t"
    "ds_read_b64_tr_b16 a[148:149], v12 offset:40960\n\t"
    "ds_read_b64_tr_b16 a[150:151], v12 offset:45056\n\t"
    "v_mfma_f32_16x16x32_bf16 v[212:215], v[36:39], a[184:187], v[212:215]\n\t"
    "ds_read_b64_tr_b16 a[152:153], v13 offset:32768\n\t"
    "ds_read_b64_tr_b16 a[154:155], v13 offset:36864\n\t"
    "v_mfma_f32_16x16x32_bf16 v[212:215], v[40:43], a[188:191], v[212:215]\n\t"
    "ds_read_b64_tr_b16 a[156:157], v13 offset:40960\n\t"
    "ds_read_b64_tr_b16 a[158:159], v13 offset:45056\n\t"
    "v_mfma_f32_16x16x32_bf16 v[216:219], v[36:39], a[240:243], v[216:219]\n\t"
    "ds_read_b64_tr_b16 a[160:161], v14 offset:32768\n\t"
    "ds_read_b64_tr_b16 a[162:163], v14 offset:36864\n\t"
    "v_mfma_f32_16x16x32_bf16 v[216:219], v[40:43], a[244:247], v[216:219]\n\t"
    "ds_read_b64_tr_b16 a[164:165], v14 offset:40960\n\t"
    "ds_read_b64_tr_b16 a[166:167], v14 offset:45056\n\t"
    "v_mfma_f32_16x16x32_bf16 v[220:223], v[36:39], a[248:251], v[220:223]\n\t"
    "ds_read_b64_tr_b16 a[168:169], v15 offset:32768\n\t"
    "ds_read_b64_tr_b16 a[170:171], v15 offset:36864\n\t"
    "v_mfma_f32_16x16x32_bf16 v[220:223], v[40:43], a[252:255], v[220:223]\n\t"
    "ds_read_b64_tr_b16 a[172:173], v15 offset:40960\n\t"
    "ds_read_b64_tr_b16 a[174:175], v15 offset:45056\n\t"
    "v_smfmac_f32_16x16x64_bf16 a[224:227], a[208:211], v[36:43], v24\n\t"
    "v_mfma_f32_16x16x32_bf16 v[80:83], a[80:83], a[16:19], v[48:51]\n\t"
    "ds_read_b64_tr_b16 a[176:177], v16 offset:32768\n\t"
    "v_cvt_pk_bf16_f32 v36, v64, v65\n\t"
    "v_mfma_f32_16x16x32_bf16 v[80:83], a[84:87], a[20:23], v[80:83]\n\t"
    "ds_read_b64_tr_b16 a[178:179], v16 offset:36864\n\t"
    "v_cvt_pk_bf16_f32 v37, v66, v67\n\t"
    "v_mfma_f32_16x16x32_bf16 v[80:83], a[88:91], a[24:27], v[80:83]\n\t"
    "ds_read_b64_tr_b16 a[180:181], v16 offset:40960\n\t"
    "v_cvt_pk_bf16_f32 v38, v68, v69\n\t"
    "v_mfma_f32_16x16x32_bf16 v[80:83], a[92:95], a[28:31], v[80:83]\n\t"
    "ds_read_b64_tr_b16 a[182:183], v16 offset:45056\n\t"
    "v_cvt_pk_bf16_f32 v39, v70, v71\n\t"
    "v_mfma_f32_16x16x32_bf16 v[84:87], a[96:99], a[16:19], v[48:51]\n\t"
    "ds_read_b64_tr_b16 a[184:185], v17 offset:32768\n\t"
    "v_cvt_pk_bf16_f32 v40, v72, v73\n\t"
    "v_mfma_f32_16x16x32_bf16 v[84:87], a[100:103], a[20:23], v[84:87]\n\t"
    "ds_read_b64_tr_b16 a[186:187], v17 offset:36864\n\t"
    "v_cvt_pk_bf16_f32 v41, v74, v75\n\t"
    "v_mfma_f32_16x16x32_bf16 v[84:87], a[104:107], a[24:27], v[84:87]\n\t"
    "ds_read_b64_tr_b16 a[188:189], v17 offset:40960\n\t"
    "v_cvt_pk_bf16_f32 v42, v76, v77\n\t"
    "v_add_u32 v244, 1, v244\n\t"
    "v_mfma_f32_16x16x32_bf16 v[84:87], a[108:111], a[28:31], v[84:87]\n\t"
    "s_waitcnt vmcnt(0)\n\t"
    "ds_write_b32 v27, v244 offset:3072\n\t"
    "ds_read_b64_tr_b16 a[190:191], v17 offset:45056\n\t"
    "v_cvt_pk_bf16_f32 v43, v78, v79\n\t"
    "v_mfma_f32_16x16x32_bf16 v[88:91], a[112:115], a[16:19], v[48:51]\n\t"
    "ds_write_b32 v27, v244 offset:4096\n\t"
    "v_exp_f32 v80, v80\n\t"
    "v_mfma_f32_16x16x32_bf16 v[88:91], a[116:119], a[20:23], v[88:91]\n\t"
    "ds_write_b32 v27, v244 offset:5120\n\t"
    "v_exp_f32 v81, v81\n\t"
    "v_mfma_f32_16x16x32_bf16 v[88:91], a[120:123], a[24:27], v[88:91]\n\t"
    "v_exp_f32 v82, v82\n\t"
    "v_mfma_f32_16x16x32_bf16 v[88:91], a[124:127], a[28:31], v[88:91]\n\t"
    "v_exp_f32 v83, v83\n\t"
    "v_mfma_f32_16x16x32_bf16 v[92:95], a[128:131], a[16:19], v[48:51]\n\t"
    "v_exp_f32 v84, v84\n\t"
    "v_mfma_f32_16x16x32_bf16 v[92:95], a[132:135], a[20:23], v[92:95]\n\t"
    "v_exp_f32 v85, v85\n\t"
    "v_mfma_f32_16x16x32_bf16 v[92:95], a[136:139], a[24:27], v[92:95]\n\t"
    "v_exp_f32 v86, v86\n\t"
    "v_mfma_f32_16x16x32_bf16 v[92:95], a[140:143], a[28:31], v[92:95]\n\t"
    "s_waitcnt lgkmcnt(2)\n\t"
    "v_exp_f32 v87, v87\n\t"
    "v_mfma_f32_16x16x32_bf16 v[96:99], v[36:39], a[144:147], v[96:99]\n\t"
    "v_exp_f32 v88, v88\n\t"
    "v_mfma_f32_16x16x32_bf16 v[96:99], v[40:43], a[148:151], v[96:99]\n\t"
    "v_exp_f32 v89, v89\n\t"
    "v_mfma_f32_16x16x32_bf16 v[100:103], v[36:39], a[152:155], v[100:103]\n\t"
    "v_exp_f32 v90, v90\n\t"
    "v_mfma_f32_16x16x32_bf16 v[100:103], v[40:43], a[156:159], v[100:103]\n\t"
    "v_exp_f32 v91, v91\n\t"
    "v_mfma_f32_16x16x32_bf16 v[104:107], v[36:39], a[160:163], v[104:107]\n\t"
    "v_exp_f32 v92, v92\n\t"
    "v_mfma_f32_16x16x32_bf16 v[104:107], v[40:43], a[164:167], v[104:107]\n\t"
    "v_exp_f32 v93, v93\n\t"
    "v_mfma_f32_16x16x32_bf16 v[108:111], v[36:39], a[168:171], v[108:111]\n\t"
    "v_exp_f32 v94, v94\n\t"
    "v_mfma_f32_16x16x32_bf16 v[108:111], v[40:43], a[172:175], v[108:111]\n\t"
    "v_exp_f32 v95, v95\n\t"
    "v_mfma_f32_16x16x32_bf16 v[112:115], v[36:39], a[176:179], v[112:115]\n\t"
    "v_mfma_f32_16x16x32_bf16 v[112:115], v[40:43], a[180:183], v[112:115]\n\t"
    "v_mfma_f32_16x16x32_bf16 v[116:119], v[36:39], a[184:187], v[116:119]\n\t"
    "v_mfma_f32_16x16x32_bf16 v[116:119], v[40:43], a[188:191], v[116:119]\n\t"
    "v_mfma_f32_16x16x32_bf16 v[120:123], v[36:39], a[192:195], v[120:123]\n\t"
    "v_mfma_f32_16x16x32_bf16 v[120:123], v[40:43], a[196:199], v[120:123]\n\t"
    "v_mfma_f32_16x16x32_bf16 v[124:127], v[36:39], a[200:203], v[124:127]\n\t"
    "v_mfma_f32_16x16x32_bf16 v[124:127], v[40:43], a[204:207], v[124:127]\n\t"
    "v_smfmac_f32_16x16x64_bf16 a[212:215], a[208:211], v[36:43], v24\n\t"
    "v_mfma_f32_16x16x32_bf16 v[64:67], a[80:83], a[32:35], v[52:55]\n\t"
    "ds_read_b64_tr_b16 a[240:241], v18 offset:49152\n\t"
    "v_cvt_pk_bf16_f32 v36, v80, v81\n\t"
    "v_mfma_f32_16x16x32_bf16 v[64:67], a[84:87], a[36:39], v[64:67]\n\t"
    "ds_read_b64_tr_b16 a[242:243], v18 offset:53248\n\t"
    "v_cvt_pk_bf16_f32 v37, v82, v83\n\t"
    "v_mfma_f32_16x16x32_bf16 v[64:67], a[88:91], a[40:43], v[64:67]\n\t"
    "ds_read_b64_tr_b16 a[244:245], v18 offset:57344\n\t"
    "v_cvt_pk_bf16_f32 v38, v84, v85\n\t"
    "v_mfma_f32_16x16x32_bf16 v[64:67], a[92:95], a[44:47], v[64:67]\n\t"
    "ds_read_b64_tr_b16 a[246:247], v18 offset:61440\n\t"
    "v_cvt_pk_bf16_f32 v39, v86, v87\n\t"
    "v_mfma_f32_16x16x32_bf16 v[68:71], a[96:99], a[32:35], v[52:55]\n\t"
    "ds_read_b64_tr_b16 a[248:249], v19 offset:49152\n\t"
    "v_cvt_pk_bf16_f32 v40, v88, v89\n\t"
    "v_mfma_f32_16x16x32_bf16 v[68:71], a[100:103], a[36:39], v[68:71]\n\t"
    "ds_read_b64_tr_b16 a[250:251], v19 offset:53248\n\t"
    "v_cvt_pk_bf16_f32 v41, v90, v91\n\t"
    "v_mfma_f32_16x16x32_bf16 v[68:71], a[104:107], a[40:43], v[68:71]\n\t"
    "ds_read_b64_tr_b16 a[252:253], v19 offset:57344\n\t"
    "v_cvt_pk_bf16_f32 v42, v92, v93\n\t"
    "v_mfma_f32_16x16x32_bf16 v[68:71], a[108:111], a[44:47], v[68:71]\n\t"
    "ds_read_b64_tr_b16 a[254:255], v19 offset:61440\n\t"
    "v_cvt_pk_bf16_f32 v43, v94, v95\n\t"
    "v_mfma_f32_16x16x32_bf16 v[72:75], a[112:115], a[32:35], v[52:55]\n\t"
    "v_exp_f32 v64, v64\n\t"
    "v_mfma_f32_16x16x32_bf16 v[72:75], a[116:119], a[36:39], v[72:75]\n\t"
    "v_exp_f32 v65, v65\n\t"
    "v_mfma_f32_16x16x32_bf16 v[72:75], a[120:123], a[40:43], v[72:75]\n\t"
    "v_exp_f32 v66, v66\n\t"
    "v_mfma_f32_16x16x32_bf16 v[72:75], a[124:127], a[44:47], v[72:75]\n\t"
    "v_exp_f32 v67, v67\n\t"
    "v_mfma_f32_16x16x32_bf16 v[76:79], a[128:131], a[32:35], v[52:55]\n\t"
    "v_exp_f32 v68, v68\n\t"
    "v_mfma_f32_16x16x32_bf16 v[76:79], a[132:135], a[36:39], v[76:79]\n\t"
    "v_exp_f32 v69, v69\n\t"
    "v_mfma_f32_16x16x32_bf16 v[76:79], a[136:139], a[40:43], v[76:79]\n\t"
    "v_exp_f32 v70, v70\n\t"
    "v_mfma_f32_16x16x32_bf16 v[76:79], a[140:143], a[44:47], v[76:79]\n\t"
    "v_exp_f32 v71, v71\n\t"
    "v_mfma_f32_16x16x32_bf16 v[128:131], v[36:39], a[144:147], v[128:131]\n\t"
    "v_exp_f32 v72, v72\n\t"
    "v_mfma_f32_16x16x32_bf16 v[128:131], v[40:43], a[148:151], v[128:131]\n\t"
    "v_exp_f32 v73, v73\n\t"
    "v_mfma_f32_16x16x32_bf16 v[132:135], v[36:39], a[152:155], v[132:135]\n\t"
    "v_exp_f32 v74, v74\n\t"
    "v_mfma_f32_16x16x32_bf16 v[132:135], v[40:43], a[156:159], v[132:135]\n\t"
    "v_exp_f32 v75, v75\n\t"
    "v_mfma_f32_16x16x32_bf16 v[136:139], v[36:39], a[160:163], v[136:139]\n\t"
    "v_exp_f32 v76, v76\n\t"
    "v_mfma_f32_16x16x32_bf16 v[136:139], v[40:43], a[164:167], v[136:139]\n\t"
    "v_exp_f32 v77, v77\n\t"
    "v_mfma_f32_16x16x32_bf16 v[140:143], v[36:39], a[168:171], v[140:143]\n\t"
    "v_exp_f32 v78, v78\n\t"
    "v_mfma_f32_16x16x32_bf16 v[140:143], v[40:43], a[172:175], v[140:143]\n\t"
    "v_exp_f32 v79, v79\n\t"
    "v_mfma_f32_16x16x32_bf16 v[144:147], v[36:39], a[176:179], v[144:147]\n\t"
    "v_mfma_f32_16x16x32_bf16 v[144:147], v[40:43], a[180:183], v[144:147]\n\t"
    "v_mfma_f32_16x16x32_bf16 v[148:151], v[36:39], a[184:187], v[148:151]\n\t"
    "v_mfma_f32_16x16x32_bf16 v[148:151], v[40:43], a[188:191], v[148:151]\n\t"
    "v_mfma_f32_16x16x32_bf16 v[152:155], v[36:39], a[192:195], v[152:155]\n\t"
    "v_mfma_f32_16x16x32_bf16 v[152:155], v[40:43], a[196:199], v[152:155]\n\t"
    "v_mfma_f32_16x16x32_bf16 v[156:159], v[36:39], a[200:203], v[156:159]\n\t"
    "v_mfma_f32_16x16x32_bf16 v[156:159], v[40:43], a[204:207], v[156:159]\n\t"
    "v_smfmac_f32_16x16x64_bf16 a[216:219], a[208:211], v[36:43], v24\n\t"
    "v_mfma_f32_16x16x32_bf16 v[80:83], a[80:83], a[48:51], v[56:59]\n\t"
    "ds_read_b128 a[80:83], v8 offset:49152\n\t"
    "v_cvt_pk_bf16_f32 v36, v64, v65\n\t"
    "v_mfma_f32_16x16x32_bf16 v[80:83], a[84:87], a[52:55], v[80:83]\n\t"
    "ds_read_b128 a[84:87], v9 offset:49152\n\t"
    "v_cvt_pk_bf16_f32 v37, v66, v67\n\t"
    "v_mfma_f32_16x16x32_bf16 v[80:83], a[88:91], a[56:59], v[80:83]\n\t"
    "ds_read_b128 a[88:91], v10 offset:49152\n\t"
    "v_cvt_pk_bf16_f32 v38, v68, v69\n\t"
    "v_mfma_f32_16x16x32_bf16 v[80:83], a[92:95], a[60:63], v[80:83]\n\t"
    "ds_read_b128 a[92:95], v11 offset:49152\n\t"
    "v_cvt_pk_bf16_f32 v39, v70, v71\n\t"
    "v_mfma_f32_16x16x32_bf16 v[84:87], a[96:99], a[48:51], v[56:59]\n\t"
    "ds_read_b128 a[96:99], v8 offset:53248\n\t"
    "v_cvt_pk_bf16_f32 v40, v72, v73\n\t"
    "v_mfma_f32_16x16x32_bf16 v[84:87], a[100:103], a[52:55], v[84:87]\n\t"
    "ds_read_b128 a[100:103], v9 offset:53248\n\t"
    "v_cvt_pk_bf16_f32 v41, v74, v75\n\t"
    "v_mfma_f32_16x16x32_bf16 v[84:87], a[104:107], a[56:59], v[84:87]\n\t"
    "ds_read_b128 a[104:107], v10 offset:53248\n\t"
    "v_cvt_pk_bf16_f32 v42, v76, v77\n\t"
    "v_mfma_f32_16x16x32_bf16 v[84:87], a[108:111], a[60:63], v[84:87]\n\t"
    "ds_read_b128 a[108:111], v11 offset:53248\n\t"
    "v_cvt_pk_bf16_f32 v43, v78, v79\n\t"
    "v_mfma_f32_16x16x32_bf16 v[88:91], a[112:115], a[48:51], v[56:59]\n\t"
    "v_exp_f32 v80, v80\n\t"
    "v_mfma_f32_16x16x32_bf16 v[88:91], a[116:119], a[52:55], v[88:91]\n\t"
    "v_exp_f32 v81, v81\n\t"
    "v_mfma_f32_16x16x32_bf16 v[88:91], a[120:123], a[56:59], v[88:91]\n\t"
    "v_exp_f32 v82, v82\n\t"
    "v_mfma_f32_16x16x32_bf16 v[88:91], a[124:127], a[60:63], v[88:91]\n\t"
    "v_exp_f32 v83, v83\n\t"
    "v_mfma_f32_16x16x32_bf16 v[92:95], a[128:131], a[48:51], v[56:59]\n\t"
    "v_exp_f32 v84, v84\n\t"
    "v_mfma_f32_16x16x32_bf16 v[92:95], a[132:135], a[52:55], v[92:95]\n\t"
    "v_exp_f32 v85, v85\n\t"
    "v_mfma_f32_16x16x32_bf16 v[92:95], a[136:139], a[56:59], v[92:95]\n\t"
    "v_exp_f32 v86, v86\n\t"
    "v_mfma_f32_16x16x32_bf16 v[92:95], a[140:143], a[60:63], v[92:95]\n\t"
    "s_waitcnt lgkmcnt(0)\n\t"
    "v_exp_f32 v87, v87\n\t"
    "v_mfma_f32_16x16x32_bf16 v[64:67], a[80:83], a[64:67], v[60:63]\n\t"
    "v_exp_f32 v88, v88\n\t"
    "v_mfma_f32_16x16x32_bf16 v[64:67], a[84:87], a[68:71], v[64:67]\n\t"
    "v_exp_f32 v89, v89\n\t"
    "v_mfma_f32_16x16x32_bf16 v[64:67], a[88:91], a[72:75], v[64:67]\n\t"
    "v_exp_f32 v90, v90\n\t"
    "v_mfma_f32_16x16x32_bf16 v[64:67], a[92:95], a[76:79], v[64:67]\n\t"
    "v_exp_f32 v91, v91\n\t"
    "v_mfma_f32_16x16x32_bf16 v[68:71], a[80:83], v[224:227], v[252:255]\n\t"
    "v_exp_f32 v92, v92\n\t"
    "v_mfma_f32_16x16x32_bf16 v[68:71], a[84:87], v[228:231], v[68:71]\n\t"
    "v_exp_f32 v93, v93\n\t"
    "v_mfma_f32_16x16x32_bf16 v[68:71], a[88:91], v[232:235], v[68:71]\n\t"
    "v_exp_f32 v94, v94\n\t"
    "v_mfma_f32_16x16x32_bf16 v[68:71], a[92:95], v[236:239], v[68:71]\n\t"
    "v_exp_f32 v95, v95\n\t"
    "v_mfma_f32_16x16x32_bf16 v[72:75], a[80:83], a[228:231], v[32:35]\n\t"
    "v_exp_f32 v64, v64\n\t"
    "v_mfma_f32_16x16x32_bf16 v[72:75], a[84:87], v[240:243], v[72:75]\n\t"
    "v_exp_f32 v65, v65\n\t"
    "v_mfma_f32_16x16x32_bf16 v[72:75], a[88:91], a[232:235], v[72:75]\n\t"
    "v_exp_f32 v66, v66\n\t"
    "v_mfma_f32_16x16x32_bf16 v[72:75], a[92:95], a[236:239], v[72:75]\n\t"
    "v_exp_f32 v67, v67\n\t"
    "v_mfma_f32_16x16x32_bf16 v[160:163], v[36:39], a[144:147], v[160:163]\n\t"
    "v_exp_f32 v68, v68\n\t"
    "v_mfma_f32_16x16x32_bf16 v[160:163], v[40:43], a[148:151], v[160:163]\n\t"
    "v_exp_f32 v69, v69\n\t"
    "v_mfma_f32_16x16x32_bf16 v[164:167], v[36:39], a[152:155], v[164:167]\n\t"
    "v_exp_f32 v70, v70\n\t"
    "v_mfma_f32_16x16x32_bf16 v[164:167], v[40:43], a[156:159], v[164:167]\n\t"
    "v_exp_f32 v71, v71\n\t"
    "v_mfma_f32_16x16x32_bf16 v[168:171], v[36:39], a[160:163], v[168:171]\n\t"
    "ds_read_b32 v245, v246\n\t"
    "v_exp_f32 v72, v72\n\t"
    "v_mfma_f32_16x16x32_bf16 v[168:171], v[40:43], a[164:167], v[168:171]\n\t"
    "v_exp_f32 v73, v73\n\t"
    "v_mfma_f32_16x16x32_bf16 v[172:175], v[36:39], a[168:171], v[172:175]\n\t"
    "v_exp_f32 v74, v74\n\t"
    "v_mfma_f32_16x16x32_bf16 v[172:175], v[40:43], a[172:175], v[172:175]\n\t"
    "v_exp_f32 v75, v75\n\t"
    "v_mfma_f32_16x16x32_bf16 v[176:179], v[36:39], a[176:179], v[176:179]\n\t"
    "ds_read_b128 a[112:115], v8 offset:57344\n\t"
    "v_cvt_pk_bf16_f32 v64, v64, v65\n\t"
    "v_mfma_f32_16x16x32_bf16 v[176:179], v[40:43], a[180:183], v[176:179]\n\t"
    "ds_read_b128 a[116:119], v9 offset:57344\n\t"
    "v_cvt_pk_bf16_f32 v65, v66, v67\n\t"
    "s_waitcnt lgkmcnt(2)\n\t"
    "v_cmp_gt_u32 vcc, v244, v245\n\t"
    "v_mfma_f32_16x16x32_bf16 v[180:183], v[36:39], a[184:187], v[180:183]\n\t"
    "ds_read_b128 a[120:123], v10 offset:57344\n\t"
    "v_cvt_pk_bf16_f32 v68, v68, v69\n\t"
    "v_mfma_f32_16x16x32_bf16 v[180:183], v[40:43], a[188:191], v[180:183]\n\t"
    "ds_read_b128 a[124:127], v11 offset:57344\n\t"
    "v_cvt_pk_bf16_f32 v69, v70, v71\n\t"
    "v_mfma_f32_16x16x32_bf16 v[184:187], v[36:39], a[192:195], v[184:187]\n\t"
    "s_cbranch_vccnz SPIN4P30%=\n\t"
    "RET4P30%=:\n\t"
    "ds_read_b128 a[128:131], v8 offset:61440\n\t"
    "v_cvt_pk_bf16_f32 v72, v72, v73\n\t"
    "v_mfma_f32_16x16x32_bf16 v[184:187], v[40:43], a[196:199], v[184:187]\n\t"
    "ds_read_b128 a[132:135], v9 offset:61440\n\t"
    "v_cvt_pk_bf16_f32 v73, v74, v75\n\t"
    "v_mfma_f32_16x16x32_bf16 v[188:191], v[36:39], a[200:203], v[188:191]\n\t"
    "ds_read_b128 a[136:139], v10 offset:61440\n\t"
    "ds_write_b64 v27, v[64:65] offset:512\n\t"
    "v_mfma_f32_16x16x32_bf16 v[188:191], v[40:43], a[204:207], v[188:191]\n\t"
    "ds_read_b128 a[140:143], v11 offset:61440\n\t"
    "ds_write_b64 v27, v[68:69] offset:1536\n\t"
    "v_smfmac_f32_16x16x64_bf16 a[220:223], a[208:211], v[36:43], v24\n\t"
    "ds_write_b64 v27, v[72:73] offset:2560\n\t"
    "; TILE 31\n\t"
    "v_mfma_f32_16x16x32_bf16 v[64:67], a[80:83], a[0:3], v[44:47]\n\t"
    "v_cvt_pk_bf16_f32 v36, v80, v81\n\t"
    "v_add_u32 v244, 1, v244\n\t"
    "v_mfma_f32_16x16x32_bf16 v[64:67], a[84:87], a[4:7], v[64:67]\n\t"
    "ds_write_b32 v27, v244 offset:3072\n\t"
    "v_cvt_pk_bf16_f32 v37, v82, v83\n\t"
    "v_mfma_f32_16x16x32_bf16 v[64:67], a[88:91], a[8:11], v[64:67]\n\t"
    "ds_write_b32 v27, v244 offset:4096\n\t"
    "v_cvt_pk_bf16_f32 v38, v84, v85\n\t"
    "v_mfma_f32_16x16x32_bf16 v[64:67], a[92:95], a[12:15], v[64:67]\n\t"
    "ds_write_b32 v27, v244 offset:5120\n\t"
    "v_cvt_pk_bf16_f32 v39, v86, v87\n\t"
    "v_mfma_f32_16x16x32_bf16 v[68:71], a[96:99], a[0:3], v[44:47]\n\t"
    "v_cvt_pk_bf16_f32 v40, v88, v89\n\t"
    "v_mfma_f32_16x16x32_bf16 v[68:71], a[100:103], a[4:7], v[68:71]\n\t"
    "v_cvt_pk_bf16_f32 v41, v90, v91\n\t"
    "v_mfma_f32_16x16x32_bf16 v[68:71], a[104:107], a[8:11], v[68:71]\n\t"
    "v_cvt_pk_bf16_f32 v42, v92, v93\n\t"
    "v_mfma_f32_16x16x32_bf16 v[68:71], a[108:111], a[12:15], v[68:71]\n\t"
    "s_waitcnt lgkmcnt(3)\n\t"
    "v_cvt_pk_bf16_f32 v43, v94, v95\n\t"
    "v_mfma_f32_16x16x32_bf16 v[72:75], a[112:115], a[0:3], v[44:47]\n\t"
    "v_exp_f32 v64, v64\n\t"
    "v_mfma_f32_16x16x32_bf16 v[72:75], a[116:119], a[4:7], v[72:75]\n\t"
    "v_exp_f32 v65, v65\n\t"
    "v_mfma_f32_16x16x32_bf16 v[72:75], a[120:123], a[8:11], v[72:75]\n\t"
    "v_exp_f32 v66, v66\n\t"
    "v_mfma_f32_16x16x32_bf16 v[72:75], a[124:127], a[12:15], v[72:75]\n\t"
    "v_exp_f32 v67, v67\n\t"
    "v_mfma_f32_16x16x32_bf16 v[76:79], a[128:131], a[0:3], v[44:47]\n\t"
    "v_exp_f32 v68, v68\n\t"
    "v_mfma_f32_16x16x32_bf16 v[76:79], a[132:135], a[4:7], v[76:79]\n\t"
    "v_exp_f32 v69, v69\n\t"
    "v_mfma_f32_16x16x32_bf16 v[76:79], a[136:139], a[8:11], v[76:79]\n\t"
    "v_exp_f32 v70, v70\n\t"
    "v_mfma_f32_16x16x32_bf16 v[76:79], a[140:143], a[12:15], v[76:79]\n\t"
    "v_exp_f32 v71, v71\n\t"
    "v_mfma_f32_16x16x32_bf16 v[192:195], v[36:39], a[144:147], v[192:195]\n\t"
    "v_exp_f32 v72, v72\n\t"
    "v_mfma_f32_16x16x32_bf16 v[192:195], v[40:43], a[148:151], v[192:195]\n\t"
    "v_exp_f32 v73, v73\n\t"
    "v_mfma_f32_16x16x32_bf16 v[196:199], v[36:39], a[152:155], v[196:199]\n\t"
    "v_exp_f32 v74, v74\n\t"
    "v_mfma_f32_16x16x32_bf16 v[196:199], v[40:43], a[156:159], v[196:199]\n\t"
    "v_exp_f32 v75, v75\n\t"
    "v_mfma_f32_16x16x32_bf16 v[200:203], v[36:39], a[160:163], v[200:203]\n\t"
    "v_exp_f32 v76, v76\n\t"
    "v_mfma_f32_16x16x32_bf16 v[200:203], v[40:43], a[164:167], v[200:203]\n\t"
    "v_exp_f32 v77, v77\n\t"
    "v_mfma_f32_16x16x32_bf16 v[204:207], v[36:39], a[168:171], v[204:207]\n\t"
    "v_exp_f32 v78, v78\n\t"
    "v_mfma_f32_16x16x32_bf16 v[204:207], v[40:43], a[172:175], v[204:207]\n\t"
    "v_exp_f32 v79, v79\n\t"
    "v_mfma_f32_16x16x32_bf16 v[208:211], v[36:39], a[176:179], v[208:211]\n\t"
    "ds_read_b64_tr_b16 a[144:145], v12 offset:49152\n\t"
    "ds_read_b64_tr_b16 a[146:147], v12 offset:53248\n\t"
    "v_accvgpr_write_b32 a232, 0\n\t"
    "v_mfma_f32_16x16x32_bf16 v[208:211], v[40:43], a[180:183], v[208:211]\n\t"
    "ds_read_b64_tr_b16 a[148:149], v12 offset:57344\n\t"
    "ds_read_b64_tr_b16 a[150:151], v12 offset:61440\n\t"
    "v_accvgpr_write_b32 a233, 0\n\t"
    "v_mfma_f32_16x16x32_bf16 v[212:215], v[36:39], a[184:187], v[212:215]\n\t"
    "ds_read_b64_tr_b16 a[152:153], v13 offset:49152\n\t"
    "ds_read_b64_tr_b16 a[154:155], v13 offset:53248\n\t"
    "v_accvgpr_write_b32 a234, 0\n\t"
    "v_mfma_f32_16x16x32_bf16 v[212:215], v[40:43], a[188:191], v[212:215]\n\t"
    "ds_read_b64_tr_b16 a[156:157], v13 offset:57344\n\t"
    "ds_read_b64_tr_b16 a[158:159], v13 offset:61440\n\t"
    "v_accvgpr_write_b32 a235, 0\n\t"
    "v_mfma_f32_16x16x32_bf16 v[216:219], v[36:39], a[192:195], v[216:219]\n\t"
    "ds_read_b64_tr_b16 a[160:161], v14 offset:49152\n\t"
    "ds_read_b64_tr_b16 a[162:163], v14 offset:53248\n\t"
    "v_accvgpr_write_b32 a236, 0\n\t"
    "v_mfma_f32_16x16x32_bf16 v[216:219], v[40:43], a[196:199], v[216:219]\n\t"
    "ds_read_b64_tr_b16 a[164:165], v14 offset:57344\n\t"
    "ds_read_b64_tr_b16 a[166:167], v14 offset:61440\n\t"
    "v_accvgpr_write_b32 a237, 0\n\t"
    "v_mfma_f32_16x16x32_bf16 v[220:223], v[36:39], a[200:203], v[220:223]\n\t"
    "ds_read_b64_tr_b16 a[168:169], v15 offset:49152\n\t"
    "ds_read_b64_tr_b16 a[170:171], v15 offset:53248\n\t"
    "v_mov_b32 v32, 0x7f000000\n\t"
    "v_accvgpr_write_b32 a238, 0\n\t"
    "v_mfma_f32_16x16x32_bf16 v[220:223], v[40:43], a[204:207], v[220:223]\n\t"
    "ds_read_b64_tr_b16 a[172:173], v15 offset:57344\n\t"
    "ds_read_b64_tr_b16 a[174:175], v15 offset:61440\n\t"
    "v_accvgpr_write_b32 a239, 0\n\t"
    "v_smfmac_f32_16x16x64_bf16 a[224:227], a[208:211], v[36:43], v24\n\t"
    "v_mfma_f32_16x16x32_bf16 v[80:83], a[80:83], a[16:19], v[48:51]\n\t"
    "ds_read_b64_tr_b16 a[176:177], v16 offset:49152\n\t"
    "v_cvt_pk_bf16_f32 v36, v64, v65\n\t"
    "v_mfma_f32_16x16x32_bf16 v[80:83], a[84:87], a[20:23], v[80:83]\n\t"
    "ds_read_b64_tr_b16 a[178:179], v16 offset:53248\n\t"
    "v_cvt_pk_bf16_f32 v37, v66, v67\n\t"
    "v_mfma_f32_16x16x32_bf16 v[80:83], a[88:91], a[24:27], v[80:83]\n\t"
    "ds_read_b64_tr_b16 a[180:181], v16 offset:57344\n\t"
    "v_cvt_pk_bf16_f32 v38, v68, v69\n\t"
    "v_mfma_f32_16x16x32_bf16 v[80:83], a[92:95], a[28:31], v[80:83]\n\t"
    "ds_read_b64_tr_b16 a[182:183], v16 offset:61440\n\t"
    "v_cvt_pk_bf16_f32 v39, v70, v71\n\t"
    "v_mfma_f32_16x16x32_bf16 v[84:87], a[96:99], a[16:19], v[48:51]\n\t"
    "ds_read_b64_tr_b16 a[184:185], v17 offset:49152\n\t"
    "v_cvt_pk_bf16_f32 v40, v72, v73\n\t"
    "v_mfma_f32_16x16x32_bf16 v[84:87], a[100:103], a[20:23], v[84:87]\n\t"
    "ds_read_b64_tr_b16 a[186:187], v17 offset:53248\n\t"
    "v_cvt_pk_bf16_f32 v41, v74, v75\n\t"
    "v_mfma_f32_16x16x32_bf16 v[84:87], a[104:107], a[24:27], v[84:87]\n\t"
    "ds_read_b64_tr_b16 a[188:189], v17 offset:57344\n\t"
    "v_cvt_pk_bf16_f32 v42, v76, v77\n\t"
    "v_mfma_f32_16x16x32_bf16 v[84:87], a[108:111], a[28:31], v[84:87]\n\t"
    "ds_read_b64_tr_b16 a[190:191], v17 offset:61440\n\t"
    "v_cvt_pk_bf16_f32 v43, v78, v79\n\t"
    "v_mfma_f32_16x16x32_bf16 v[88:91], a[112:115], a[16:19], v[48:51]\n\t"
    "v_exp_f32 v80, v80\n\t"
    "v_mfma_f32_16x16x32_bf16 v[88:91], a[116:119], a[20:23], v[88:91]\n\t"
    "v_exp_f32 v81, v81\n\t"
    "v_mfma_f32_16x16x32_bf16 v[88:91], a[120:123], a[24:27], v[88:91]\n\t"
    "v_exp_f32 v82, v82\n\t"
    "v_mfma_f32_16x16x32_bf16 v[88:91], a[124:127], a[28:31], v[88:91]\n\t"
    "v_exp_f32 v83, v83\n\t"
    "v_mfma_f32_16x16x32_bf16 v[92:95], a[128:131], a[16:19], v[48:51]\n\t"
    "v_exp_f32 v84, v84\n\t"
    "v_mfma_f32_16x16x32_bf16 v[92:95], a[132:135], a[20:23], v[92:95]\n\t"
    "v_exp_f32 v85, v85\n\t"
    "v_mfma_f32_16x16x32_bf16 v[92:95], a[136:139], a[24:27], v[92:95]\n\t"
    "v_exp_f32 v86, v86\n\t"
    "v_mfma_f32_16x16x32_bf16 v[92:95], a[140:143], a[28:31], v[92:95]\n\t"
    "s_waitcnt lgkmcnt(0)\n\t"
    "v_exp_f32 v87, v87\n\t"
    "v_mfma_f32_16x16x32_bf16 v[96:99], v[36:39], a[144:147], v[96:99]\n\t"
    "v_exp_f32 v88, v88\n\t"
    "v_mfma_f32_16x16x32_bf16 v[96:99], v[40:43], a[148:151], v[96:99]\n\t"
    "v_exp_f32 v89, v89\n\t"
    "v_mfma_f32_16x16x32_bf16 v[100:103], v[36:39], a[152:155], v[100:103]\n\t"
    "v_exp_f32 v90, v90\n\t"
    "v_mfma_f32_16x16x32_bf16 v[100:103], v[40:43], a[156:159], v[100:103]\n\t"
    "v_exp_f32 v91, v91\n\t"
    "v_mfma_f32_16x16x32_bf16 v[104:107], v[36:39], a[160:163], v[104:107]\n\t"
    "v_exp_f32 v92, v92\n\t"
    "v_mfma_f32_16x16x32_bf16 v[104:107], v[40:43], a[164:167], v[104:107]\n\t"
    "v_exp_f32 v93, v93\n\t"
    "v_mfma_f32_16x16x32_bf16 v[108:111], v[36:39], a[168:171], v[108:111]\n\t"
    "v_exp_f32 v94, v94\n\t"
    "v_mfma_f32_16x16x32_bf16 v[108:111], v[40:43], a[172:175], v[108:111]\n\t"
    "v_exp_f32 v95, v95\n\t"
    "v_mfma_f32_16x16x32_bf16 v[112:115], v[36:39], a[176:179], v[112:115]\n\t"
    "v_mfma_f32_16x16x32_bf16 v[112:115], v[40:43], a[180:183], v[112:115]\n\t"
    "v_mfma_f32_16x16x32_bf16 v[116:119], v[36:39], a[184:187], v[116:119]\n\t"
    "v_mfma_f32_16x16x32_bf16 v[116:119], v[40:43], a[188:191], v[116:119]\n\t"
    "v_mfma_f32_16x16x32_bf16 v[120:123], v[36:39], a[240:243], v[120:123]\n\t"
    "v_mfma_f32_16x16x32_bf16 v[120:123], v[40:43], a[244:247], v[120:123]\n\t"
    "v_mfma_f32_16x16x32_bf16 v[124:127], v[36:39], a[248:251], v[124:127]\n\t"
    "v_mfma_f32_16x16x32_bf16 v[124:127], v[40:43], a[252:255], v[124:127]\n\t"
    "v_smfmac_f32_16x16x64_bf16 a[212:215], a[208:211], v[36:43], v24\n\t"
    "v_mfma_f32_16x16x32_bf16 v[64:67], a[80:83], a[32:35], v[52:55]\n\t"
    "v_cvt_pk_bf16_f32 v36, v80, v81\n\t"
    "v_mfma_f32_16x16x32_bf16 v[64:67], a[84:87], a[36:39], v[64:67]\n\t"
    "v_cvt_pk_bf16_f32 v37, v82, v83\n\t"
    "v_mfma_f32_16x16x32_bf16 v[64:67], a[88:91], a[40:43], v[64:67]\n\t"
    "v_cvt_pk_bf16_f32 v38, v84, v85\n\t"
    "v_mfma_f32_16x16x32_bf16 v[64:67], a[92:95], a[44:47], v[64:67]\n\t"
    "v_cvt_pk_bf16_f32 v39, v86, v87\n\t"
    "v_accvgpr_read_b32 v44, a212\n\t"
    "v_accvgpr_read_b32 v45, a213\n\t"
    "s_add_u32 s61, s70, 0\n\t"
    "v_add_f32 v44, v44, v45\n\t"
    "v_mfma_f32_16x16x32_bf16 v[68:71], a[96:99], a[32:35], v[52:55]\n\t"
    "v_cvt_pk_bf16_f32 v40, v88, v89\n\t"
    "v_rcp_f32 v34, v44\n\t"
    "v_add_f32 v31, v31, v44\n\t"
    "v_min_f32 v32, v32, v44\n\t"
    "v_mfma_f32_16x16x32_bf16 v[68:71], a[100:103], a[36:39], v[68:71]\n\t"
    "ds_bpermute_b32 v44, v30, v34 offset:0\n\t"
    "ds_bpermute_b32 v45, v30, v34 offset:4\n\t"
    "ds_bpermute_b32 v46, v30, v34 offset:8\n\t"
    "ds_bpermute_b32 v47, v30, v34 offset:12\n\t"
    "v_cvt_pk_bf16_f32 v41, v90, v91\n\t"
    "v_mfma_f32_16x16x32_bf16 v[68:71], a[104:107], a[40:43], v[68:71]\n\t"
    "v_cvt_pk_bf16_f32 v42, v92, v93\n\t"
    "v_mfma_f32_16x16x32_bf16 v[68:71], a[108:111], a[44:47], v[68:71]\n\t"
    "v_cvt_pk_bf16_f32 v43, v94, v95\n\t"
    "v_mfma_f32_16x16x32_bf16 v[72:75], a[112:115], a[32:35], v[52:55]\n\t"
    "v_exp_f32 v64, v64\n\t"
    "v_mfma_f32_16x16x32_bf16 v[72:75], a[116:119], a[36:39], v[72:75]\n\t"
    "v_exp_f32 v65, v65\n\t"
    "v_mfma_f32_16x16x32_bf16 v[72:75], a[120:123], a[40:43], v[72:75]\n\t"
    "v_exp_f32 v66, v66\n\t"
    "v_mfma_f32_16x16x32_bf16 v[72:75], a[124:127], a[44:47], v[72:75]\n\t"
    "v_exp_f32 v67, v67\n\t"
    "v_mfma_f32_16x16x32_bf16 v[76:79], a[128:131], a[32:35], v[52:55]\n\t"
    "v_exp_f32 v68, v68\n\t"
    "v_mfma_f32_16x16x32_bf16 v[76:79], a[132:135], a[36:39], v[76:79]\n\t"
    "s_waitcnt lgkmcnt(0)\n\t"
    "v_mul_f32 v96, v96, v44\n\t"
    "v_mul_f32 v97, v97, v45\n\t"
    "v_mul_f32 v100, v100, v44\n\t"
    "v_exp_f32 v69, v69\n\t"
    "v_mfma_f32_16x16x32_bf16 v[76:79], a[136:139], a[40:43], v[76:79]\n\t"
    "v_mul_f32 v101, v101, v45\n\t"
    "v_mul_f32 v104, v104, v44\n\t"
    "v_mul_f32 v105, v105, v45\n\t"
    "v_exp_f32 v70, v70\n\t"
    "v_mfma_f32_16x16x32_bf16 v[76:79], a[140:143], a[44:47], v[76:79]\n\t"
    "v_mul_f32 v108, v108, v44\n\t"
    "v_mul_f32 v109, v109, v45\n\t"
    "v_cvt_pk_bf16_f32 v8, v96, v100\n\t"
    "v_exp_f32 v71, v71\n\t"
    "v_mfma_f32_16x16x32_bf16 v[128:131], v[36:39], a[144:147], v[128:131]\n\t"
    "v_cvt_pk_bf16_f32 v9, v104, v108\n\t"
    "v_exp_f32 v72, v72\n\t"
    "v_mfma_f32_16x16x32_bf16 v[128:131], v[40:43], a[148:151], v[128:131]\n\t"
    "ds_write_b64 v26, v[8:9] offset:0\n\t"
    "v_cvt_pk_bf16_f32 v10, v97, v101\n\t"
    "v_cvt_pk_bf16_f32 v11, v105, v109\n\t"
    "v_exp_f32 v73, v73\n\t"
    "v_mfma_f32_16x16x32_bf16 v[132:135], v[36:39], a[152:155], v[132:135]\n\t"
    "ds_write_b64 v26, v[10:11] offset:256\n\t"
    "v_mul_f32 v112, v112, v44\n\t"
    "v_mul_f32 v113, v113, v45\n\t"
    "v_exp_f32 v74, v74\n\t"
    "v_mfma_f32_16x16x32_bf16 v[132:135], v[40:43], a[156:159], v[132:135]\n\t"
    "v_mul_f32 v116, v116, v44\n\t"
    "v_mul_f32 v117, v117, v45\n\t"
    "v_mul_f32 v120, v120, v44\n\t"
    "v_exp_f32 v75, v75\n\t"
    "v_mfma_f32_16x16x32_bf16 v[136:139], v[36:39], a[160:163], v[136:139]\n\t"
    "v_mul_f32 v121, v121, v45\n\t"
    "v_mul_f32 v124, v124, v44\n\t"
    "v_mul_f32 v125, v125, v45\n\t"
    "v_exp_f32 v76, v76\n\t"
    "v_mfma_f32_16x16x32_bf16 v[136:139], v[40:43], a[164:167], v[136:139]\n\t"
    "v_cvt_pk_bf16_f32 v20, v112, v116\n\t"
    "v_cvt_pk_bf16_f32 v21, v120, v124\n\t"
    "v_exp_f32 v77, v77\n\t"
    "v_mfma_f32_16x16x32_bf16 v[140:143], v[36:39], a[168:171], v[140:143]\n\t"
    "ds_write_b64 v26, v[20:21] offset:128\n\t"
    "v_cvt_pk_bf16_f32 v22, v113, v117\n\t"
    "v_cvt_pk_bf16_f32 v23, v121, v125\n\t"
    "v_exp_f32 v78, v78\n\t"
    "v_mfma_f32_16x16x32_bf16 v[140:143], v[40:43], a[172:175], v[140:143]\n\t"
    "ds_write_b64 v26, v[22:23] offset:384\n\t"
    "ds_read_b64_tr_b16 a[192:193], v28 offset:0\n\t"
    "ds_read_b64_tr_b16 a[194:195], v29 offset:0\n\t"
    "v_exp_f32 v79, v79\n\t"
    "v_mfma_f32_16x16x32_bf16 v[144:147], v[36:39], a[176:179], v[144:147]\n\t"
    "ds_read_b64_tr_b16 a[196:197], v28 offset:256\n\t"
    "ds_read_b64_tr_b16 a[198:199], v29 offset:256\n\t"
    "v_mfma_f32_16x16x32_bf16 v[144:147], v[40:43], a[180:183], v[144:147]\n\t"
    "v_mfma_f32_16x16x32_bf16 v[148:151], v[36:39], a[184:187], v[148:151]\n\t"
    "v_mfma_f32_16x16x32_bf16 v[148:151], v[40:43], a[188:191], v[148:151]\n\t"
    "v_mfma_f32_16x16x32_bf16 v[152:155], v[36:39], a[240:243], v[152:155]\n\t"
    "v_mfma_f32_16x16x32_bf16 v[152:155], v[40:43], a[244:247], v[152:155]\n\t"
    "v_mfma_f32_16x16x32_bf16 v[156:159], v[36:39], a[248:251], v[156:159]\n\t"
    "v_mfma_f32_16x16x32_bf16 v[156:159], v[40:43], a[252:255], v[156:159]\n\t"
    "v_smfmac_f32_16x16x64_bf16 a[216:219], a[208:211], v[36:43], v24\n\t"
    "v_mfma_f32_16x16x32_bf16 v[80:83], a[80:83], a[48:51], v[56:59]\n\t"
    "v_cvt_pk_bf16_f32 v36, v64, v65\n\t"
    "v_mfma_f32_16x16x32_bf16 v[80:83], a[84:87], a[52:55], v[80:83]\n\t"
    "v_cvt_pk_bf16_f32 v37, v66, v67\n\t"
    "v_mfma_f32_16x16x32_bf16 v[80:83], a[88:91], a[56:59], v[80:83]\n\t"
    "v_cvt_pk_bf16_f32 v38, v68, v69\n\t"
    "v_mfma_f32_16x16x32_bf16 v[80:83], a[92:95], a[60:63], v[80:83]\n\t"
    "v_cvt_pk_bf16_f32 v39, v70, v71\n\t"
    "v_accvgpr_read_b32 v48, a216\n\t"
    "v_accvgpr_read_b32 v49, a217\n\t"
    "s_add_u32 s62, s70, 4096\n\t"
    "v_add_f32 v48, v48, v49\n\t"
    "v_mfma_f32_16x16x32_bf16 v[84:87], a[96:99], a[48:51], v[56:59]\n\t"
    "v_cvt_pk_bf16_f32 v40, v72, v73\n\t"
    "v_rcp_f32 v35, v48\n\t"
    "v_add_f32 v31, v31, v48\n\t"
    "v_min_f32 v32, v32, v48\n\t"
    "v_mfma_f32_16x16x32_bf16 v[84:87], a[100:103], a[52:55], v[84:87]\n\t"
    "ds_bpermute_b32 v48, v30, v35 offset:0\n\t"
    "ds_bpermute_b32 v49, v30, v35 offset:4\n\t"
    "ds_bpermute_b32 v50, v30, v35 offset:8\n\t"
    "ds_bpermute_b32 v51, v30, v35 offset:12\n\t"
    "v_cvt_pk_bf16_f32 v41, v74, v75\n\t"
    "v_mfma_f32_16x16x32_bf16 v[84:87], a[104:107], a[56:59], v[84:87]\n\t"
    "v_cvt_pk_bf16_f32 v42, v76, v77\n\t"
    "v_mul_f32 v98, v98, v46\n\t"
    "v_mul_f32 v99, v99, v47\n\t"
    "v_mul_f32 v102, v102, v46\n\t"
    "v_mul_f32 v103, v103, v47\n\t"
    "v_mfma_f32_16x16x32_bf16 v[84:87], a[108:111], a[60:63], v[84:87]\n\t"
    "v_cvt_pk_bf16_f32 v43, v78, v79\n\t"
    "v_mul_f32 v106, v106, v46\n\t"
    "v_mul_f32 v107, v107, v47\n\t"
    "v_mul_f32 v110, v110, v46\n\t"
    "v_mul_f32 v111, v111, v47\n\t"
    "v_mfma_f32_16x16x32_bf16 v[88:91], a[112:115], a[48:51], v[56:59]\n\t"
    "v_cvt_pk_bf16_f32 v8, v98, v102\n\t"
    "v_cvt_pk_bf16_f32 v9, v106, v110\n\t"
    "v_exp_f32 v80, v80\n\t"
    "v_mfma_f32_16x16x32_bf16 v[88:91], a[116:119], a[52:55], v[88:91]\n\t"
    "ds_write_b64 v26, v[8:9] offset:512\n\t"
    "v_cvt_pk_bf16_f32 v10, v99, v103\n\t"
    "v_cvt_pk_bf16_f32 v11, v107, v111\n\t"
    "v_exp_f32 v81, v81\n\t"
    "v_mfma_f32_16x16x32_bf16 v[88:91], a[120:123], a[56:59], v[88:91]\n\t"
    "ds_write_b64 v26, v[10:11] offset:768\n\t"
    "v_mul_f32 v114, v114, v46\n\t"
    "v_mul_f32 v115, v115, v47\n\t"
    "v_exp_f32 v82, v82\n\t"
    "v_mfma_f32_16x16x32_bf16 v[88:91], a[124:127], a[60:63], v[88:91]\n\t"
    "v_mul_f32 v118, v118, v46\n\t"
    "v_mul_f32 v119, v119, v47\n\t"
    "v_mul_f32 v122, v122, v46\n\t"
    "v_exp_f32 v83, v83\n\t"
    "v_mfma_f32_16x16x32_bf16 v[92:95], a[128:131], a[48:51], v[56:59]\n\t"
    "v_mul_f32 v123, v123, v47\n\t"
    "v_mul_f32 v126, v126, v46\n\t"
    "v_mul_f32 v127, v127, v47\n\t"
    "v_exp_f32 v84, v84\n\t"
    "v_mfma_f32_16x16x32_bf16 v[92:95], a[132:135], a[52:55], v[92:95]\n\t"
    "v_cvt_pk_bf16_f32 v20, v114, v118\n\t"
    "v_cvt_pk_bf16_f32 v21, v122, v126\n\t"
    "v_exp_f32 v85, v85\n\t"
    "v_mfma_f32_16x16x32_bf16 v[92:95], a[136:139], a[56:59], v[92:95]\n\t"
    "ds_write_b64 v26, v[20:21] offset:640\n\t"
    "v_cvt_pk_bf16_f32 v22, v115, v119\n\t"
    "v_cvt_pk_bf16_f32 v23, v123, v127\n\t"
    "v_exp_f32 v86, v86\n\t"
    "v_mfma_f32_16x16x32_bf16 v[92:95], a[140:143], a[60:63], v[92:95]\n\t"
    "ds_write_b64 v26, v[22:23] offset:896\n\t"
    "ds_read_b64_tr_b16 a[200:201], v28 offset:512\n\t"
    "ds_read_b64_tr_b16 a[202:203], v29 offset:512\n\t"
    "v_exp_f32 v87, v87\n\t"
    "v_mfma_f32_16x16x32_bf16 v[160:163], v[36:39], a[144:147], v[160:163]\n\t"
    "ds_read_b64_tr_b16 a[204:205], v28 offset:768\n\t"
    "ds_read_b64_tr_b16 a[206:207], v29 offset:768\n\t"
    "v_exp_f32 v88, v88\n\t"
    "v_mfma_f32_16x16x32_bf16 v[160:163], v[40:43], a[148:151], v[160:163]\n\t"
    "s_waitcnt lgkmcnt(7)\n\t"
    "buffer_store_dwordx4 a[192:195], v25, s[52:55], s61 offen offset:0 sc0 sc1\n\t"
    "v_mul_f32 v128, v128, v48\n\t"
    "v_exp_f32 v89, v89\n\t"
    "v_mfma_f32_16x16x32_bf16 v[164:167], v[36:39], a[152:155], v[164:167]\n\t"
    "v_mul_f32 v129, v129, v49\n\t"
    "v_mul_f32 v132, v132, v48\n\t"
    "v_mul_f32 v133, v133, v49\n\t"
    "v_exp_f32 v90, v90\n\t"
    "v_mfma_f32_16x16x32_bf16 v[164:167], v[40:43], a[156:159], v[164:167]\n\t"
    "v_mul_f32 v136, v136, v48\n\t"
    "v_mul_f32 v137, v137, v49\n\t"
    "v_mul_f32 v140, v140, v48\n\t"
    "v_exp_f32 v91, v91\n\t"
    "v_mfma_f32_16x16x32_bf16 v[168:171], v[36:39], a[160:163], v[168:171]\n\t"
    "v_mul_f32 v141, v141, v49\n\t"
    "v_cvt_pk_bf16_f32 v8, v128, v132\n\t"
    "v_cvt_pk_bf16_f32 v9, v136, v140\n\t"
    "v_exp_f32 v92, v92\n\t"
    "v_mfma_f32_16x16x32_bf16 v[168:171], v[40:43], a[164:167], v[168:171]\n\t"
    "ds_write_b64 v26, v[8:9] offset:4096\n\t"
    "v_cvt_pk_bf16_f32 v10, v129, v133\n\t"
    "v_cvt_pk_bf16_f32 v11, v137, v141\n\t"
    "v_exp_f32 v93, v93\n\t"
    "v_mfma_f32_16x16x32_bf16 v[172:175], v[36:39], a[168:171], v[172:175]\n\t"
    "ds_write_b64 v26, v[10:11] offset:4352\n\t"
    "buffer_store_dwordx4 a[196:199], v25, s[52:55], s61 offen offset:256 sc0 sc1\n\t"
    "v_exp_f32 v94, v94\n\t"
    "v_mfma_f32_16x16x32_bf16 v[172:175], v[40:43], a[172:175], v[172:175]\n\t"
    "v_mul_f32 v144, v144, v48\n\t"
    "v_mul_f32 v145, v145, v49\n\t"
    "v_mul_f32 v148, v148, v48\n\t"
    "v_exp_f32 v95, v95\n\t"
    "v_mfma_f32_16x16x32_bf16 v[176:179], v[36:39], a[176:179], v[176:179]\n\t"
    "v_mul_f32 v149, v149, v49\n\t"
    "v_mul_f32 v152, v152, v48\n\t"
    "v_mul_f32 v153, v153, v49\n\t"
    "v_mul_f32 v156, v156, v48\n\t"
    "v_mul_f32 v157, v157, v49\n\t"
    "v_mfma_f32_16x16x32_bf16 v[176:179], v[40:43], a[180:183], v[176:179]\n\t"
    "v_cvt_pk_bf16_f32 v20, v144, v148\n\t"
    "v_cvt_pk_bf16_f32 v21, v152, v156\n\t"
    "v_mfma_f32_16x16x32_bf16 v[180:183], v[36:39], a[184:187], v[180:183]\n\t"
    "ds_write_b64 v26, v[20:21] offset:4224\n\t"
    "v_cvt_pk_bf16_f32 v22, v145, v149\n\t"
    "v_cvt_pk_bf16_f32 v23, v153, v157\n\t"
    "v_mfma_f32_16x16x32_bf16 v[180:183], v[40:43], a[188:191], v[180:183]\n\t"
    "ds_write_b64 v26, v[22:23] offset:4480\n\t"
    "s_waitcnt lgkmcnt(4)\n\t"
    "buffer_store_dwordx4 a[200:203], v25, s[52:55], s61 offen offset:512 sc0 sc1\n\t"
    "ds_read_b64_tr_b16 v[96:97], v28 offset:4096\n\t"
    "ds_read_b64_tr_b16 v[98:99], v29 offset:4096\n\t"
    "v_mfma_f32_16x16x32_bf16 v[184:187], v[36:39], a[240:243], v[184:187]\n\t"
    "ds_read_b64_tr_b16 v[100:101], v28 offset:4352\n\t"
    "ds_read_b64_tr_b16 v[102:103], v29 offset:4352\n\t"
    "v_mfma_f32_16x16x32_bf16 v[184:187], v[40:43], a[244:247], v[184:187]\n\t"
    "v_mfma_f32_16x16x32_bf16 v[188:191], v[36:39], a[248:251], v[188:191]\n\t"
    "v_mfma_f32_16x16x32_bf16 v[188:191], v[40:43], a[252:255], v[188:191]\n\t"
    "v_smfmac_f32_16x16x64_bf16 a[220:223], a[208:211], v[36:43], v24\n\t"
    "v_cvt_pk_bf16_f32 v36, v80, v81\n\t"
    "v_cvt_pk_bf16_f32 v37, v82, v83\n\t"
    "v_cvt_pk_bf16_f32 v38, v84, v85\n\t"
    "v_cvt_pk_bf16_f32 v39, v86, v87\n\t"
    "v_cvt_pk_bf16_f32 v40, v88, v89\n\t"
    "v_cvt_pk_bf16_f32 v41, v90, v91\n\t"
    "v_cvt_pk_bf16_f32 v42, v92, v93\n\t"
    "v_cvt_pk_bf16_f32 v43, v94, v95\n\t"
    "s_nop 1\n\t"
    "v_smfmac_f32_16x16x64_bf16 a[224:227], a[208:211], v[36:43], v24\n\t"
    "v_mfma_f32_16x16x32_bf16 v[192:195], v[36:39], a[144:147], v[192:195]\n\t"
    "v_accvgpr_read_b32 v52, a220\n\t"
    "v_accvgpr_read_b32 v53, a221\n\t"
    "s_add_u32 s63, s70, 8192\n\t"
    "v_add_f32 v52, v52, v53\n\t"
    "v_mfma_f32_16x16x32_bf16 v[192:195], v[40:43], a[148:151], v[192:195]\n\t"
    "v_rcp_f32 v34, v52\n\t"
    "v_add_f32 v31, v31, v52\n\t"
    "v_min_f32 v32, v32, v52\n\t"
    "v_mfma_f32_16x16x32_bf16 v[196:199], v[36:39], a[152:155], v[196:199]\n\t"
    "ds_bpermute_b32 v52, v30, v34 offset:0\n\t"
    "ds_bpermute_b32 v53, v30, v34 offset:4\n\t"
    "ds_bpermute_b32 v54, v30, v34 offset:8\n\t"
    "ds_bpermute_b32 v55, v30, v34 offset:12\n\t"
    "v_mul_f32 v130, v130, v50\n\t"
    "v_mfma_f32_16x16x32_bf16 v[196:199], v[40:43], a[156:159], v[196:199]\n\t"
    "v_mul_f32 v131, v131, v51\n\t"
    "v_mul_f32 v134, v134, v50\n\t"
    "v_mul_f32 v135, v135, v51\n\t"
    "v_mul_f32 v138, v138, v50\n\t"
    "v_mul_f32 v139, v139, v51\n\t"
    "v_mfma_f32_16x16x32_bf16 v[200:203], v[36:39], a[160:163], v[200:203]\n\t"
    "v_mul_f32 v142, v142, v50\n\t"
    "v_mul_f32 v143, v143, v51\n\t"
    "v_cvt_pk_bf16_f32 v8, v130, v134\n\t"
    "v_cvt_pk_bf16_f32 v9, v138, v142\n\t"
    "v_mfma_f32_16x16x32_bf16 v[200:203], v[40:43], a[164:167], v[200:203]\n\t"
    "ds_write_b64 v26, v[8:9] offset:4608\n\t"
    "v_cvt_pk_bf16_f32 v10, v131, v135\n\t"
    "v_cvt_pk_bf16_f32 v11, v139, v143\n\t"
    "v_mfma_f32_16x16x32_bf16 v[204:207], v[36:39], a[168:171], v[204:207]\n\t"
    "ds_write_b64 v26, v[10:11] offset:4864\n\t"
    "buffer_store_dwordx4 a[204:207], v25, s[52:55], s61 offen offset:768 sc0 sc1\n\t"
    "v_mul_f32 v146, v146, v50\n\t"
    "v_mul_f32 v147, v147, v51\n\t"
    "v_mfma_f32_16x16x32_bf16 v[204:207], v[40:43], a[172:175], v[204:207]\n\t"
    "v_mul_f32 v150, v150, v50\n\t"
    "v_mul_f32 v151, v151, v51\n\t"
    "v_mul_f32 v154, v154, v50\n\t"
    "v_mul_f32 v155, v155, v51\n\t"
    "v_mul_f32 v158, v158, v50\n\t"
    "v_mfma_f32_16x16x32_bf16 v[208:211], v[36:39], a[176:179], v[208:211]\n\t"
    "v_mul_f32 v159, v159, v51\n\t"
    "v_cvt_pk_bf16_f32 v20, v146, v150\n\t"
    "v_cvt_pk_bf16_f32 v21, v154, v158\n\t"
    "v_mfma_f32_16x16x32_bf16 v[208:211], v[40:43], a[180:183], v[208:211]\n\t"
    "ds_write_b64 v26, v[20:21] offset:4736\n\t"
    "v_cvt_pk_bf16_f32 v22, v147, v151\n\t"
    "v_cvt_pk_bf16_f32 v23, v155, v159\n\t"
    "v_mfma_f32_16x16x32_bf16 v[212:215], v[36:39], a[184:187], v[212:215]\n\t"
    "ds_write_b64 v26, v[22:23] offset:4992\n\t"
    "ds_read_b64_tr_b16 v[104:105], v28 offset:4608\n\t"
    "ds_read_b64_tr_b16 v[106:107], v29 offset:4608\n\t"
    "ds_read_b64_tr_b16 v[108:109], v28 offset:4864\n\t"
    "ds_read_b64_tr_b16 v[110:111], v29 offset:4864\n\t"
    "v_mfma_f32_16x16x32_bf16 v[212:215], v[40:43], a[188:191], v[212:215]\n\t"
    "s_waitcnt lgkmcnt(8)\n\t"
    "buffer_store_dwordx4 v[96:99], v25, s[52:55], s62 offen offset:0 sc0 sc1\n\t"
    "v_mul_f32 v160, v160, v52\n\t"
    "v_mul_f32 v161, v161, v53\n\t"
    "v_mul_f32 v164, v164, v52\n\t"
    "v_mfma_f32_16x16x32_bf16 v[216:219], v[36:39], a[240:243], v[216:219]\n\t"
    "v_mul_f32 v165, v165, v53\n\t"
    "v_mul_f32 v168, v168, v52\n\t"
    "v_mul_f32 v169, v169, v53\n\t"
    "v_mul_f32 v172, v172, v52\n\t"
    "v_mul_f32 v173, v173, v53\n\t"
    "v_mfma_f32_16x16x32_bf16 v[216:219], v[40:43], a[244:247], v[216:219]\n\t"
    "v_cvt_pk_bf16_f32 v8, v160, v164\n\t"
    "v_cvt_pk_bf16_f32 v9, v168, v172\n\t"
    "v_mfma_f32_16x16x32_bf16 v[220:223], v[36:39], a[248:251], v[220:223]\n\t"
    "ds_write_b64 v26, v[8:9] offset:0\n\t"
    "v_cvt_pk_bf16_f32 v10, v161, v165\n\t"
    "v_cvt_pk_bf16_f32 v11, v169, v173\n\t"
    "v_mfma_f32_16x16x32_bf16 v[220:223], v[40:43], a[252:255], v[220:223]\n\t"
    "ds_write_b64 v26, v[10:11] offset:256\n\t"
    "buffer_store_dwordx4 v[100:103], v25, s[52:55], s62 offen offset:256 sc0 sc1\n\t"
    "v_mul_f32 v176, v176, v52\n\t"
    "v_mul_f32 v177, v177, v53\n\t"
    "; EPILOGUE\n\t"
    "v_mul_f32 v180, v180, v52\n\t"
    "v_mul_f32 v181, v181, v53\n\t"
    "v_mul_f32 v184, v184, v52\n\t"
    "v_mfma_f32_16x16x32_bf16 a[236:239], a[232:235], a[192:195], a[236:239]\n\t"
    "v_mul_f32 v185, v185, v53\n\t"
    "v_mul_f32 v188, v188, v52\n\t"
    "v_mul_f32 v189, v189, v53\n\t"
    "v_mfma_f32_16x16x32_bf16 a[236:239], a[232:235], a[196:199], a[236:239]\n\t"
    "v_cvt_pk_bf16_f32 v20, v176, v180\n\t"
    "v_cvt_pk_bf16_f32 v21, v184, v188\n\t"
    "ds_write_b64 v26, v[20:21] offset:128\n\t"
    "v_cvt_pk_bf16_f32 v22, v177, v181\n\t"
    "v_mfma_f32_16x16x32_bf16 a[236:239], a[232:235], a[200:203], a[236:239]\n\t"
    "v_cvt_pk_bf16_f32 v23, v185, v189\n\t"
    "ds_write_b64 v26, v[22:23] offset:384\n\t"
    "s_waitcnt lgkmcnt(4)\n\t"
    "buffer_store_dwordx4 v[104:107], v25, s[52:55], s62 offen offset:512 sc0 sc1\n\t"
    "ds_read_b64_tr_b16 v[128:129], v28 offset:0\n\t"
    "ds_read_b64_tr_b16 v[130:131], v29 offset:0\n\t"
    "ds_read_b64_tr_b16 v[132:133], v28 offset:256\n\t"
    "ds_read_b64_tr_b16 v[134:135], v29 offset:256\n\t"
    "v_accvgpr_read_b32 v56, a224\n\t"
    "v_accvgpr_read_b32 v57, a225\n\t"
    "v_mfma_f32_16x16x32_bf16 a[236:239], a[232:235], a[204:207], a[236:239]\n\t"
    "s_add_u32 s61, s70, 12288\n\t"
    "v_add_f32 v56, v56, v57\n\t"
    "v_rcp_f32 v35, v56\n\t"
    "v_add_f32 v31, v31, v56\n\t"
    "v_min_f32 v32, v32, v56\n\t"
    "v_mfma_f32_16x16x32_bf16 a[236:239], a[232:235], v[96:99], a[236:239]\n\t"
    "ds_bpermute_b32 v56, v30, v35 offset:0\n\t"
    "ds_bpermute_b32 v57, v30, v35 offset:4\n\t"
    "ds_bpermute_b32 v58, v30, v35 offset:8\n\t"
    "ds_bpermute_b32 v59, v30, v35 offset:12\n\t"
    "v_pk_mul_f32 v[162:163], v[162:163], v[54:55]\n\t"
    "v_pk_mul_f32 v[166:167], v[166:167], v[54:55]\n\t"
    "v_pk_mul_f32 v[170:171], v[170:171], v[54:55]\n\t"
    "v_mfma_f32_16x16x32_bf16 a[236:239], a[232:235], v[100:103], a[236:239]\n\t"
    "v_pk_mul_f32 v[174:175], v[174:175], v[54:55]\n\t"
    "v_cvt_pk_bf16_f32 v8, v162, v166\n\t"
    "v_cvt_pk_bf16_f32 v9, v170, v174\n\t"
    "v_mfma_f32_16x16x32_bf16 a[236:239], a[232:235], v[104:107], a[236:239]\n\t"
    "ds_write_b64 v26, v[8:9] offset:512\n\t"
    "v_cvt_pk_bf16_f32 v10, v163, v167\n\t"
    "v_cvt_pk_bf16_f32 v11, v171, v175\n\t"
    "ds_write_b64 v26, v[10:11] offset:768\n\t"
    "buffer_store_dwordx4 v[108:111], v25, s[52:55], s62 offen offset:768 sc0 sc1\n\t"
    "v_pk_mul_f32 v[178:179], v[178:179], v[54:55]\n\t"
    "v_mfma_f32_16x16x32_bf16 a[236:239], a[232:235], v[108:111], a[236:239]\n\t"
    "v_pk_mul_f32 v[182:183], v[182:183], v[54:55]\n\t"
    "v_pk_mul_f32 v[186:187], v[186:187], v[54:55]\n\t"
    "v_pk_mul_f32 v[190:191], v[190:191], v[54:55]\n\t"
    "v_cvt_pk_bf16_f32 v20, v178, v182\n\t"
    "v_cvt_pk_bf16_f32 v21, v186, v190\n\t"
    "ds_write_b64 v26, v[20:21] offset:640\n\t"
    "v_cvt_pk_bf16_f32 v22, v179, v183\n\t"
    "v_cvt_pk_bf16_f32 v23, v187, v191\n\t"
    "ds_write_b64 v26, v[22:23] offset:896\n\t"
    "ds_read_b64_tr_b16 v[136:137], v28 offset:512\n\t"
    "ds_read_b64_tr_b16 v[138:139], v29 offset:512\n\t"
    "ds_read_b64_tr_b16 v[140:141], v28 offset:768\n\t"
    "ds_read_b64_tr_b16 v[142:143], v29 offset:768\n\t"
    "s_waitcnt lgkmcnt(14)\n\t"
    "buffer_store_dwordx4 v[128:131], v25, s[52:55], s63 offen offset:0 sc0 sc1\n\t"
    "v_mfma_f32_16x16x32_bf16 a[236:239], a[232:235], v[128:131], a[236:239]\n\t"
    "s_waitcnt lgkmcnt(10)\n\t"
    "v_pk_mul_f32 v[192:193], v[192:193], v[56:57]\n\t"
    "v_pk_mul_f32 v[196:197], v[196:197], v[56:57]\n\t"
    "v_pk_mul_f32 v[200:201], v[200:201], v[56:57]\n\t"
    "v_pk_mul_f32 v[204:205], v[204:205], v[56:57]\n\t"
    "v_cvt_pk_bf16_f32 v8, v192, v196\n\t"
    "v_cvt_pk_bf16_f32 v9, v200, v204\n\t"
    "ds_write_b64 v26, v[8:9] offset:4096\n\t"
    "v_cvt_pk_bf16_f32 v10, v193, v197\n\t"
    "v_cvt_pk_bf16_f32 v11, v201, v205\n\t"
    "ds_write_b64 v26, v[10:11] offset:4352\n\t"
    "buffer_store_dwordx4 v[132:135], v25, s[52:55], s63 offen offset:256 sc0 sc1\n\t"
    "v_mfma_f32_16x16x32_bf16 a[236:239], a[232:235], v[132:135], a[236:239]\n\t"
    "v_pk_mul_f32 v[208:209], v[208:209], v[56:57]\n\t"
    "v_pk_mul_f32 v[212:213], v[212:213], v[56:57]\n\t"
    "v_pk_mul_f32 v[216:217], v[216:217], v[56:57]\n\t"
    "v_pk_mul_f32 v[220:221], v[220:221], v[56:57]\n\t"
    "v_cvt_pk_bf16_f32 v20, v208, v212\n\t"
    "v_cvt_pk_bf16_f32 v21, v216, v220\n\t"
    "ds_write_b64 v26, v[20:21] offset:4224\n\t"
    "v_cvt_pk_bf16_f32 v22, v209, v213\n\t"
    "v_cvt_pk_bf16_f32 v23, v217, v221\n\t"
    "ds_write_b64 v26, v[22:23] offset:4480\n\t"
    "s_waitcnt lgkmcnt(6)\n\t"
    "buffer_store_dwordx4 v[136:139], v25, s[52:55], s63 offen offset:512 sc0 sc1\n\t"
    "v_mfma_f32_16x16x32_bf16 a[236:239], a[232:235], v[136:139], a[236:239]\n\t"
    "ds_read_b64_tr_b16 v[160:161], v28 offset:4096\n\t"
    "ds_read_b64_tr_b16 v[162:163], v29 offset:4096\n\t"
    "ds_read_b64_tr_b16 v[164:165], v28 offset:4352\n\t"
    "ds_read_b64_tr_b16 v[166:167], v29 offset:4352\n\t"
    "v_pk_mul_f32 v[194:195], v[194:195], v[58:59]\n\t"
    "v_pk_mul_f32 v[198:199], v[198:199], v[58:59]\n\t"
    "v_pk_mul_f32 v[202:203], v[202:203], v[58:59]\n\t"
    "v_pk_mul_f32 v[206:207], v[206:207], v[58:59]\n\t"
    "v_cvt_pk_bf16_f32 v8, v194, v198\n\t"
    "v_cvt_pk_bf16_f32 v9, v202, v206\n\t"
    "ds_write_b64 v26, v[8:9] offset:4608\n\t"
    "v_cvt_pk_bf16_f32 v10, v195, v199\n\t"
    "v_cvt_pk_bf16_f32 v11, v203, v207\n\t"
    "ds_write_b64 v26, v[10:11] offset:4864\n\t"
    "s_waitcnt lgkmcnt(10)\n\t"
    "buffer_store_dwordx4 v[140:143], v25, s[52:55], s63 offen offset:768 sc0 sc1\n\t"
    "v_mfma_f32_16x16x32_bf16 a[236:239], a[232:235], v[140:143], a[236:239]\n\t"
    "s_waitcnt lgkmcnt(4)\n\t"
    "buffer_store_dwordx4 v[160:163], v25, s[52:55], s61 offen offset:0 sc0 sc1\n\t"
    "v_mfma_f32_16x16x32_bf16 a[236:239], a[232:235], v[160:163], a[236:239]\n\t"
    "v_pk_mul_f32 v[210:211], v[210:211], v[58:59]\n\t"
    "v_pk_mul_f32 v[214:215], v[214:215], v[58:59]\n\t"
    "v_pk_mul_f32 v[218:219], v[218:219], v[58:59]\n\t"
    "v_pk_mul_f32 v[222:223], v[222:223], v[58:59]\n\t"
    "v_cvt_pk_bf16_f32 v20, v210, v214\n\t"
    "v_cvt_pk_bf16_f32 v21, v218, v222\n\t"
    "ds_write_b64 v26, v[20:21] offset:4736\n\t"
    "v_cvt_pk_bf16_f32 v22, v211, v215\n\t"
    "v_cvt_pk_bf16_f32 v23, v219, v223\n\t"
    "ds_write_b64 v26, v[22:23] offset:4992\n\t"
    "ds_read_b64_tr_b16 v[168:169], v28 offset:4608\n\t"
    "ds_read_b64_tr_b16 v[170:171], v29 offset:4608\n\t"
    "ds_read_b64_tr_b16 v[172:173], v28 offset:4864\n\t"
    "ds_read_b64_tr_b16 v[174:175], v29 offset:4864\n\t"
    "s_waitcnt lgkmcnt(8)\n\t"
    "buffer_store_dwordx4 v[164:167], v25, s[52:55], s61 offen offset:256 sc0 sc1\n\t"
    "v_mfma_f32_16x16x32_bf16 a[236:239], a[232:235], v[164:167], a[236:239]\n\t"
    "s_waitcnt lgkmcnt(2)\n\t"
    "buffer_store_dwordx4 v[168:171], v25, s[52:55], s61 offen offset:512 sc0 sc1\n\t"
    "v_mfma_f32_16x16x32_bf16 a[236:239], a[232:235], v[168:171], a[236:239]\n\t"
    "s_waitcnt lgkmcnt(0)\n\t"
    "buffer_store_dwordx4 v[172:175], v25, s[52:55], s61 offen offset:768 sc0 sc1\n\t"
    "v_mfma_f32_16x16x32_bf16 a[236:239], a[232:235], v[172:175], a[236:239]\n\t"
    "s_mov_b32 s63, 0x5d800000\n\t"
    "s_mov_b32 s64, 0x3a800000\n\t"
    "v_cmp_nge_f32 vcc, s63, v31\n\t"
    "s_nop 1\n\t"
    "s_mov_b64 s[72:73], vcc\n\t"
    "v_cmp_nle_f32 vcc, s64, v32\n\t"
    "s_nop 1\n\t"
    "s_or_b64 s[72:73], s[72:73], vcc\n\t"
    "s_and_b64 s[72:73], s[72:73], 0xffff\n\t"
    "v_accvgpr_read_b32 v33, a236\n\t"
    "v_cmp_u_f32 vcc, v33, v33\n\t"
    "s_nop 1\n\t"
    "s_or_b64 s[72:73], s[72:73], vcc\n\t"
    "s_cmp_lg_u64 s[72:73], 0\n\t"
    "s_cselect_b32 %[flag], 1, 0\n\t"
    "s_branch DONE%=\n\t"
    "SPIN4L0%=:\n\t"
    "s_sleep 1\n\t"
    "ds_read_b32 v245, v246\n\t"
    "s_waitcnt lgkmcnt(0)\n\t"
    "v_cmp_gt_u32 vcc, v244, v245\n\t"
    "s_nop 4\n\t"
    "s_cbranch_vccnz SPIN4L0%=\n\t"
    "s_branch RET4L0%=\n\t"
    "SPIN4L1%=:\n\t"
    "s_sleep 1\n\t"
    "ds_read_b32 v245, v246\n\t"
    "s_waitcnt lgkmcnt(0)\n\t"
    "v_cmp_gt_u32 vcc, v244, v245\n\t"
    "s_nop 4\n\t"
    "s_cbranch_vccnz SPIN4L1%=\n\t"
    "s_branch RET4L1%=\n\t"
    "SPIN4L2%=:\n\t"
    "s_sleep 1\n\t"
    "ds_read_b32 v245, v246\n\t"
    "s_waitcnt lgkmcnt(0)\n\t"
    "v_cmp_gt_u32 vcc, v244, v245\n\t"
    "s_nop 4\n\t"
    "s_cbranch_vccnz SPIN4L2%=\n\t"
    "s_branch RET4L2%=\n\t"
    "SPIN4L3%=:\n\t"
    "s_sleep 1\n\t"
    "ds_read_b32 v245, v246\n\t"
    "s_waitcnt lgkmcnt(0)\n\t"
    "v_cmp_gt_u32 vcc, v244, v245\n\t"
    "s_nop 4\n\t"
    "s_cbranch_vccnz SPIN4L3%=\n\t"
    "s_branch RET4L3%=\n\t"
    "SPIN4P1%=:\n\t"
    "s_sleep 1\n\t"
    "ds_read_b32 v245, v246\n\t"
    "s_waitcnt lgkmcnt(0)\n\t"
    "v_cmp_gt_u32 vcc, v244, v245\n\t"
    "s_nop 4\n\t"
    "s_cbranch_vccnz SPIN4P1%=\n\t"
    "s_branch RET4P1%=\n\t"
    "SPIN4P30%=:\n\t"
    "s_sleep 1\n\t"
    "ds_read_b32 v245, v246\n\t"
    "s_waitcnt lgkmcnt(0)\n\t"
    "v_cmp_gt_u32 vcc, v244, v245\n\t"
    "s_nop 4\n\t"
    "s_cbranch_vccnz SPIN4P30%=\n\t"
    "s_branch RET4P30%=\n\t"
    "DONE%=:\n\t"
    : [flag] "=&s"(flag)
    : [kp] "s"(kp), [hkv] "s"(hkv), [hqo] "s"(hqo), [w] "s"(w), [lds] "s"(ldsb), [rot] "s"(rot)
    : "memory", "vcc", "scc", "v8", "v9", "v10", "v11", "v12", "v13", "v14", "v15", "v16", "v17", "v18", "v19", "v20", "v21", "v22", "v23", "v24", "v25", "v26", "v27", "v28", "v29", "v30", "v31", "v32", "v33", "v34", "v35", "v36", "v37", "v38", "v39", "v40", "v41", "v42", "v43", "v44", "v45", "v46", "v47", "v48", "v49", "v50", "v51", "v52", "v53", "v54", "v55", "v56", "v57", "v58", "v59", "v60", "v61", "v62", "v63", "v64", "v65", "v66", "v67", "v68", "v69", "v70", "v71", "v72", "v73", "v74", "v75", "v76", "v77", "v78", "v79", "v80", "v81", "v82", "v83", "v84", "v85", "v86", "v87", "v88", "v89", "v90", "v91", "v92", "v93", "v94", "v95", "v96", "v97", "v98", "v99", "v100", "v101", "v102", "v103", "v104", "v105", "v106", "v107", "v108", "v109", "v110", "v111", "v112", "v113", "v114", "v115", "v116", "v117", "v118", "v119", "v120", "v121", "v122", "v123", "v124", "v125", "v126", "v127", "v128", "v129", "v130", "v131", "v132", "v133", "v134", "v135", "v136", "v137", "v138", "v139", "v140", "v141", "v142", "v143", "v144", "v145", "v146", "v147", "v148", "v149", "v150", "v151", "v152", "v153", "v154", "v155", "v156", "v157", "v158", "v159", "v160", "v161", "v162", "v163", "v164", "v165", "v166", "v167", "v168", "v169", "v170", "v171", "v172", "v173", "v174", "v175", "v176", "v177", "v178", "v179", "v180", "v181", "v182", "v183", "v184", "v185", "v186", "v187", "v188", "v189", "v190", "v191", "v192", "v193", "v194", "v195", "v196", "v197", "v198", "v199", "v200", "v201", "v202", "v203", "v204", "v205", "v206", "v207", "v208", "v209", "v210", "v211", "v212", "v213", "v214", "v215", "v216", "v217", "v218", "v219", "v220", "v221", "v222", "v223", "v224", "v225", "v226", "v227", "v228", "v229", "v230", "v231", "v232", "v233", "v234", "v235", "v236", "v237", "v238", "v239", "v240", "v241", "v242", "v243", "v244", "v245", "v246", "v247", "v248", "v249", "v250", "v251", "v252", "v253", "v254", "v255", "a0", "a1", "a2", "a3", "a4", "a5", "a6", "a7", "a8", "a9", "a10", "a11", "a12", "a13", "a14", "a15", "a16", "a17", "a18", "a19", "a20", "a21", "a22", "a23", "a24", "a25", "a26", "a27", "a28", "a29", "a30", "a31", "a32", "a33", "a34", "a35", "a36", "a37", "a38", "a39", "a40", "a41", "a42", "a43", "a44", "a45", "a46", "a47", "a48", "a49", "a50", "a51", "a52", "a53", "a54", "a55", "a56", "a57", "a58", "a59", "a60", "a61", "a62", "a63", "a64", "a65", "a66", "a67", "a68", "a69", "a70", "a71", "a72", "a73", "a74", "a75", "a76", "a77", "a78", "a79", "a80", "a81", "a82", "a83", "a84", "a85", "a86", "a87", "a88", "a89", "a90", "a91", "a92", "a93", "a94", "a95", "a96", "a97", "a98", "a99", "a100", "a101", "a102", "a103", "a104", "a105", "a106", "a107", "a108", "a109", "a110", "a111", "a112", "a113", "a114", "a115", "a116", "a117", "a118", "a119", "a120", "a121", "a122", "a123", "a124", "a125", "a126", "a127", "a128", "a129", "a130", "a131", "a132", "a133", "a134", "a135", "a136", "a137", "a138", "a139", "a140", "a141", "a142", "a143", "a144", "a145", "a146", "a147", "a148", "a149", "a150", "a151", "a152", "a153", "a154", "a155", "a156", "a157", "a158", "a159", "a160", "a161", "a162", "a163", "a164", "a165", "a166", "a167", "a168", "a169", "a170", "a171", "a172", "a173", "a174", "a175", "a176", "a177", "a178", "a179", "a180", "a181", "a182", "a183", "a184", "a185", "a186", "a187", "a188", "a189", "a190", "a191", "a192", "a193", "a194", "a195", "a196", "a197", "a198", "a199", "a200", "a201", "a202", "a203", "a204", "a205", "a206", "a207", "a208", "a209", "a210", "a211", "a212", "a213", "a214", "a215", "a216", "a217", "a218", "a219", "a220", "a221", "a222", "a223", "a224", "a225", "a226", "a227", "a228", "a229", "a230", "a231", "a232", "a233", "a234", "a235", "a236", "a237", "a238", "a239", "a240", "a241", "a242", "a243", "a244", "a245", "a246", "a247", "a248", "a249", "a250", "a251", "a252", "a253", "a254", "a255", "s40", "s41", "s42", "s43", "s44", "s45", "s46", "s47", "s48", "s49", "s50", "s51", "s52", "s53", "s54", "s55", "s56", "s57", "s58", "s59", "s60", "s61", "s62", "s63", "s64", "s65", "s66", "s67", "s68", "s69", "s70", "s71", "s72", "s73", "s74", "s75");

  if (flag) {
    asm volatile("s_waitcnt vmcnt(0)" ::: "memory");
    const unsigned short* q = Q + (size_t)head * 304 * 128;
    const unsigned short* k = K + (size_t)head * 2048 * 128;
    const unsigned short* v = V + (size_t)head * 2048 * 128;
    unsigned short* o = O + (size_t)head * 304 * 128;
    const int r0 = 80 * (int)w;
    slow_rows(q, k, v, o, r0, r0 + 80 < 304 ? r0 + 80 : 304, (int)(threadIdx.x & 63));
  }
}

extern "C" void kernel_launch(void* const* d_in, const int* in_sizes, int n_in, void* d_out, int out_size, void* d_ws, size_t ws_size, hipStream_t stream) {
  (void)in_sizes; (void)n_in; (void)out_size; (void)ws_size; (void)d_ws;
  (void)hipFuncSetAttribute((const void*)mha_fwd_kernel, hipFuncAttributeMaxDynamicSharedMemorySize, LDS_BYTES);
  hipLaunchKernelGGL(mha_fwd_kernel, dim3(256), dim3(NTHREADS), LDS_BYTES, stream,
                     (const unsigned short*)d_in[0], (const unsigned short*)d_in[1], (const unsigned short*)d_in[2], (unsigned short*)d_out);
}
```
